```python
import torch

def _softmax(x, axis=-1):
    return torch.softmax(x, dim=axis)
def _gelu(x):
    return torch.nn.functional.gelu(x, approximate="tanh")
def _silu(x):
    return torch.nn.functional.silu(x)
def _sigmoid(x):
    return torch.sigmoid(x)
def _layernorm(x, axis=-1, eps=1e-5):
    m = x.mean(dim=axis, keepdim=True)
    v = x.var(dim=axis, keepdim=True, unbiased=False)
    return (x - m) / torch.sqrt(v + eps)
def _scatter_add(tgt, idx, src, axis=0):
    return tgt.clone().index_add_(axis, idx, src)
def _segment_sum(src, seg, n):
    out = torch.zeros((n,) + src.shape[1:], dtype=src.dtype)
    return out.index_add_(0, seg, src)
def _pad(x, pw):
    flat = tuple(v for pair in reversed(pw) for v in pair)
    return torch.nn.functional.pad(x, flat)

kernel_name = 'recurrent_scan__sliding_block_cached__dense_activation_relu__prompt'

def setup_inputs():

    _w = (lambda a: (lambda t: t.to(torch.bfloat16) if t.is_floating_point() else t)(torch.as_tensor(a)))
    def _A_setup():
        B, Sq, D = 1, 2048, 576
        Hq, Hkv, Sk, Dh = 32, 16, 2048, 128
        assert Hq % Hkv == 0, (Hq, Hkv)
        return {
            "x": _w((lambda *s: torch.randn(*s, dtype=torch.bfloat16) * 0.05)(B, Sq, D)),
            "k_cache": _w((lambda *s: torch.randn(*s, dtype=torch.bfloat16) * 0.8)(B, Hkv, Sk, Dh)),
            "v_cache": _w((lambda *s: torch.randn(*s, dtype=torch.bfloat16) * 0.2)(B, Hkv, Sk, Dh)),
            "wq": _w((lambda *s: torch.randn(*s, dtype=torch.bfloat16) * 0.8)(D, Hq * Dh) * (D ** -0.5)),
            "wo": _w((lambda *s: torch.randn(*s, dtype=torch.bfloat16) * 0.2)(Hq * Dh, D) * ((Hq * Dh) ** -0.5)),
        }
    _A_in = _A_setup()

    def _B_setup():
        B, S, D, Dff = 1, 2048, 576, 4608
        return {
            "x": _w((lambda *s: torch.randn(*s, dtype=torch.bfloat16) * 0.05)(B, S, D)),
            "w1": _w((lambda *s: torch.randn(*s, dtype=torch.bfloat16) * 0.1)(D, Dff)),
            "w2": _w((lambda *s: torch.randn(*s, dtype=torch.bfloat16) * 0.1)(Dff, D)),
        }
    _B_in = _B_setup()

    return {
        "A_x": _A_in["x"],
        "A_k_cache": _A_in["k_cache"],
        "A_v_cache": _A_in["v_cache"],
        "A_wq": _A_in["wq"],
        "A_wo": _A_in["wo"],
        "B_x": _B_in["x"],
        "B_w1": _B_in["w1"],
        "B_w2": _B_in["w2"],
    }

def reference(A_x, A_k_cache, A_v_cache, A_wq, A_wo, B_x, B_w1, B_w2):
    def _A_ref(x, k_cache, v_cache, wq, wo):
        B, Sq, D = x.shape
        Hq, Dh = 32, 128
        Hkv, Sk = k_cache.shape[1], k_cache.shape[2]
        W = min(4096, max(256, Sk // 4))
        rep = Hq // Hkv
        q = (lambda x, *d: x.permute(*d))((x @ wq).reshape(B, Sq, Hq, Dh), 0, 2, 1, 3)
        k = (lambda x, n, axis: torch.repeat_interleave(x, n, dim=axis))(k_cache[:, :, Sk - W:, :], rep, axis=1)
        v = (lambda x, n, axis: torch.repeat_interleave(x, n, dim=axis))(v_cache[:, :, Sk - W:, :], rep, axis=1)
        scores = torch.einsum("bhqd,bhkd->bhqk", q, k) * (Dh ** -0.5)
        p = _softmax(scores, axis=-1)
        o = torch.einsum("bhqk,bhkd->bhqd", p, v)
        return (lambda x, *d: x.permute(*d))(o, 0, 2, 1, 3).reshape(B, Sq, Hq * Dh) @ wo

    def _B_ref(x, w1, w2):
        h = torch.einsum("bsd,df->bsf", x, w1)
        zero = torch.zeros_like(h)
        h = torch.maximum(h, zero)
        return torch.einsum("bsf,fd->bsd", h, w2)

    x = A_x
    xn = _layernorm(x, axis=-1)
    s = _A_ref(xn, A_k_cache, A_v_cache, A_wq, A_wo)
    g = _silu(_B_ref(xn, B_w1, B_w2))
    return x + s * g
```

```cpp
#include <hip/hip_runtime.h>
#include <hip/hip_bf16.h>
typedef unsigned short u16;
typedef short bf16x8 __attribute__((ext_vector_type(8)));
typedef float f32x16 __attribute__((ext_vector_type(16)));
typedef float f32x4 __attribute__((ext_vector_type(4)));
__device__ __forceinline__ float bf2f(u16 x){ return __uint_as_float(((unsigned)x)<<16); }
__device__ __forceinline__ u16 f2bf(float f){ unsigned u=__float_as_uint(f); u += 0x7fffu + ((u>>16)&1u); return (u16)(u>>16); }
__device__ __forceinline__ float rbf(float f){ return bf2f(f2bf(f)); }
constexpr int S=2048, D=576, HQ=32, HKV=16, SK=2048, DH=128, W=512, DFF=4608;

__global__ void k_ln(const u16* x, u16* xn){
  int row = blockIdx.x*4 + (threadIdx.x>>6), lane = threadIdx.x&63;
  const u16* p = x + row*D;
  float v[9]; float s=0;
  for(int i=0;i<9;i++){ v[i]=bf2f(p[lane+64*i]); s+=v[i]; }
  for(int o=32;o;o>>=1) s+=__shfl_xor(s,o);
  float m=s/D; float q=0;
  for(int i=0;i<9;i++){ float d=v[i]-m; q+=d*d; }
  for(int o=32;o;o>>=1) q+=__shfl_xor(q,o);
  float var=rbf(q/D); float mb=rbf(m);
  float r=rbf(sqrtf(rbf(var+1e-5f)));
  for(int i=0;i<9;i++) xn[row*D+lane+64*i]=f2bf(rbf(v[i]-mb)/r);
}
template<int MODE>
__global__ void k_gemm(const u16* A, const u16* B, void* C, int M, int N, int K){
  __shared__ u16 As[64][40]; __shared__ u16 Bs[64][40];
  int bm=blockIdx.y*64, bn=blockIdx.x*64, t=threadIdx.x, w=t>>6, l=t&63, wr=w>>1, wc=w&1, r=l&31, h=l>>5;
  f32x16 acc={};
  for(int k0=0;k0<K;k0+=32){
    { int row=t>>2, c=(t&3)*8; *(bf16x8*)&As[row][c] = *(const bf16x8*)&A[(size_t)(bm+row)*K+k0+c]; }
    { int kk=t>>3, c=(t&7)*8; bf16x8 v=*(const bf16x8*)&B[(size_t)(k0+kk)*N+bn+c]; for(int j=0;j<8;j++) Bs[c+j][kk]=v[j]; }
    __syncthreads();
    for(int s=0;s<2;s++){
      bf16x8 a=*(bf16x8*)&As[wr*32+r][s*16+h*8]; bf16x8 b=*(bf16x8*)&Bs[wc*32+r][s*16+h*8];
      acc=__builtin_amdgcn_mfma_f32_32x32x16_bf16(a,b,acc,0,0,0);
    }
    __syncthreads();
  }
  for(int i=0;i<16;i++){ int row=bm+wr*32+(i&3)+8*(i>>2)+4*h, col=bn+wc*32+r; float v=acc[i];
    if(MODE==1) v=fmaxf(v,0.f);
    if(MODE==2) ((float*)C)[(size_t)row*N+col]=v; else ((u16*)C)[(size_t)row*N+col]=f2bf(v); }
}
__global__ void k_attn(const u16* Q, const u16* Kc, const u16* Vc, u16* O){
  __shared__ float Ss[32][W+1];
  int hq=blockIdx.y, q0=blockIdx.x*32, l=threadIdx.x, r=l&31, h=l>>5, hk=hq/2;
  const u16* Kp=Kc+((size_t)hk*SK+(SK-W))*DH; const u16* Vp=Vc+((size_t)hk*SK+(SK-W))*DH;
  bf16x8 q[8]; for(int s=0;s<8;s++) q[s]=*(const bf16x8*)&Q[(size_t)(q0+r)*4096+hq*DH+s*16+h*8];
  for(int kt=0;kt<W/32;kt++){ f32x16 acc={};
    for(int s=0;s<8;s++){ bf16x8 b=*(const bf16x8*)&Kp[(size_t)(kt*32+r)*DH+s*16+h*8]; acc=__builtin_amdgcn_mfma_f32_32x32x16_bf16(q[s],b,acc,0,0,0);}
    for(int i=0;i<16;i++) Ss[(i&3)+8*(i>>2)+4*h][kt*32+r]=acc[i]*0.08838834764831845f; }
  __syncthreads();
  for(int row=0;row<32;row++){ float m=-1e30f; for(int j=l;j<W;j+=64) m=fmaxf(m,Ss[row][j]); for(int o=32;o;o>>=1) m=fmaxf(m,__shfl_xor(m,o));
    float s=0; for(int j=l;j<W;j+=64){ float e=__expf(Ss[row][j]-m); Ss[row][j]=e; s+=e;} for(int o=32;o;o>>=1) s+=__shfl_xor(s,o);
    float inv=1.f/s; for(int j=l;j<W;j+=64) Ss[row][j]*=inv; }
  __syncthreads();
  for(int dt=0;dt<4;dt++){ f32x16 acc={};
    for(int s=0;s<W/16;s++){ bf16x8 a,b; for(int j=0;j<8;j++){ a[j]=(short)f2bf(Ss[r][s*16+h*8+j]); b[j]=(short)Vp[(size_t)(s*16+h*8+j)*DH+dt*32+r]; }
      acc=__builtin_amdgcn_mfma_f32_32x32x16_bf16(a,b,acc,0,0,0);}
    for(int i=0;i<16;i++) O[(size_t)(q0+(i&3)+8*(i>>2)+4*h)*4096+hq*DH+dt*32+r]=f2bf(acc[i]); }
}
__global__ void k_fin(const u16* x, const float* s, const float* f, u16* out, int n){
  int i=blockIdx.x*256+threadIdx.x; if(i>=n) return;
  float sv=rbf(s[i]), fv=rbf(f[i]); float g=rbf(fv/(1.f+__expf(-fv)));
  out[i]=f2bf(bf2f(x[i])+rbf(sv*g));
}
extern "C" void kernel_launch(void* const* d_in, const int* in_sizes, int n_in, void* d_out, int out_size, void* d_ws, size_t ws_size, hipStream_t stream){
  const u16* x=(const u16*)d_in[0]; const u16* kc=(const u16*)d_in[1]; const u16* vc=(const u16*)d_in[2]; const u16* wq=(const u16*)d_in[3]; const u16* wo=(const u16*)d_in[4];
  const u16* w1=(const u16*)d_in[6]; const u16* w2=(const u16*)d_in[7];
  char* ws=(char*)d_ws; u16* xn=(u16*)ws; u16* Q=(u16*)(ws+(4<<20)); u16* O=(u16*)(ws+(24<<20)); u16* H=(u16*)(ws+(44<<20)); float* sp=(float*)(ws+(68<<20)); float* fp=(float*)(ws+(76<<20));
  k_ln<<<S/4,256,0,stream>>>(x,xn);
  k_gemm<0><<<dim3(4096/64,S/64),256,0,stream>>>(xn,wq,Q,S,4096,D);
  k_gemm<1><<<dim3(DFF/64,S/64),256,0,stream>>>(xn,w1,H,S,DFF,D);
  k_attn<<<dim3(S/32,HQ),64,0,stream>>>(Q,kc,vc,O);
  k_gemm<2><<<dim3(D/64,S/64),256,0,stream>>>(O,wo,sp,S,D,4096);
  k_gemm<2><<<dim3(D/64,S/64),256,0,stream>>>(H,w2,fp,S,D,DFF);
  k_fin<<<(S*D+255)/256,256,0,stream>>>(x,sp,fp,(u16*)d_out,S*D);
}
```

```cpp
#include <hip/hip_runtime.h>
#include <hip/hip_bf16.h>

constexpr int LDS_BYTES = 4 * 36864 + 5120;
#define CLOBBERS "v0", "v1", "v2", "v3", "v4", "v5", "v6", "v7", "v8", "v9", "v10", "v11", "v12", "v13", "v14", "v15", "v16", "v17", "v18", "v19", "v20", "v21", "v22",  \
  "v23", "v24", "v25", "v26", "v27", "v28", "v29", "v30", "v31", "v32", "v33", "v34", "v35", "v36", "v37", "v38", "v39", "v40", "v41", "v42", "v43",  \
  "v44", "v45", "v46", "v47", "v48", "v49", "v50", "v51", "v52", "v53", "v54", "v55", "v56", "v57", "v58", "v59", "v60", "v61", "v62", "v63", "v64",  \
  "v65", "v66", "v67", "v68", "v69", "v70", "v71", "v72", "v73", "v74", "v75", "v76", "v77", "v78", "v79", "v80", "v81", "v82", "v83", "v84", "v85",  \
  "v86", "v87", "v88", "v89", "v90", "v91", "v92", "v93", "v94", "v95", "v96", "v97", "v98", "v99", "v100", "v101", "v102", "v103", "v104", "v105",  \
  "v106", "v107", "v108", "v109", "v110", "v111", "v112", "v113", "v114", "v115", "v116", "v117", "v118", "v119", "v120", "v121", "v122", "v123",  \
  "v124", "v125", "v126", "v127", "v128", "v129", "v130", "v131", "v132", "v133", "v134", "v135", "v136", "v137", "v138", "v139", "v140", "v141",  \
  "v142", "v143", "v144", "v145", "v146", "v147", "v148", "v149", "v150", "v151", "v152", "v153", "v154", "v155", "v156", "v157", "v158", "v159",  \
  "v160", "v161", "v162", "v163", "v164", "v165", "v166", "v167", "v168", "v169", "v170", "v171", "v172", "v173", "v174", "v175", "v176", "v177",  \
  "v178", "v179", "v180", "v181", "v182", "v183", "v184", "v185", "v186", "v187", "v188", "v189", "v190", "v191", "v192", "v193", "v194", "v195",  \
  "v196", "v197", "v198", "v199", "v200", "v201", "v202", "v203", "v204", "v205", "v206", "v207", "v208", "v209", "v210", "v211", "v212", "v213",  \
  "v214", "v215", "v216", "v217", "v218", "v219", "v220", "v221", "v222", "v223", "v224", "v225", "v226", "v227", "v228", "v229", "v230", "v231",  \
  "v232", "v233", "v234", "v235", "v236", "v237", "v238", "v239", "v240", "v241", "v242", "v243", "v244", "v245", "v246", "v247", "v248", "v249",  \
  "v250", "v251", "v252", "v253", "v254", "v255", "a0", "a1", "a2", "a3", "a4", "a5", "a6", "a7", "a8", "a9", "a10", "a11", "a12", "a13", "a14", "a15",  \
  "a16", "a17", "a18", "a19", "a20", "a21", "a22", "a23", "a24", "a25", "a26", "a27", "a28", "a29", "a30", "a31", "a32", "a33", "a34", "a35", "a36",  \
  "a37", "a38", "a39", "a40", "a41", "a42", "a43", "a44", "a45", "a46", "a47", "a48", "a49", "a50", "a51", "a52", "a53", "a54", "a55", "a56", "a57",  \
  "a58", "a59", "a60", "a61", "a62", "a63", "a64", "a65", "a66", "a67", "a68", "a69", "a70", "a71", "a72", "a73", "a74", "a75", "a76", "a77", "a78",  \
  "a79", "a80", "a81", "a82", "a83", "a84", "a85", "a86", "a87", "a88", "a89", "a90", "a91", "a92", "a93", "a94", "a95", "a96", "a97", "a98", "a99",  \
  "a100", "a101", "a102", "a103", "a104", "a105", "a106", "a107", "a108", "a109", "a110", "a111", "a112", "a113", "a114", "a115", "a116", "a117",  \
  "a118", "a119", "a120", "a121", "a122", "a123", "a124", "a125", "a126", "a127", "a128", "a129", "a130", "a131", "a132", "a133", "a134", "a135",  \
  "a136", "a137", "a138", "a139", "a140", "a141", "a142", "a143", "a144", "a145", "a146", "a147", "a148", "a149", "a150", "a151", "a152", "a153",  \
  "a154", "a155", "a156", "a157", "a158", "a159", "a160", "a161", "a162", "a163", "a164", "a165", "a166", "a167", "a168", "a169", "a170", "a171",  \
  "a172", "a173", "a174", "a175", "a176", "a177", "a178", "a179", "a180", "a181", "a182", "a183", "a184", "a185", "a186", "a187", "a188", "a189",  \
  "a190", "a191", "a192", "a193", "a194", "a195", "a196", "a197", "a198", "a199", "a200", "a201", "a202", "a203", "a204", "a205", "a206", "a207",  \
  "a208", "a209", "a210", "a211", "a212", "a213", "a214", "a215", "a216", "a217", "a218", "a219", "a220", "a221", "a222", "a223", "a224", "a225",  \
  "a226", "a227", "a228", "a229", "a230", "a231", "a232", "a233", "a234", "a235", "a236", "a237", "a238", "a239", "a240", "a241", "a242", "a243",  \
  "a244", "a245", "a246", "a247", "a248", "a249", "a250", "a251", "a252", "a253", "a254", "a255", "s5", "s6", "s7", "s8", "s9", "s10", "s11", "s12",  \
  "s13", "s14", "s15", "s16", "s17", "s18", "s19", "s20", "s21", "s22", "s23", "s24", "s25", "s26", "s27", "s28", "s29", "s30", "s31", "s34", "s35",  \
  "s36", "s37", "s38", "s39", "s40", "s41", "s42", "s43", "s44", "s45", "s46", "s47", "s48", "s49", "s50", "s51", "s52", "s53", "s54", "s55", "s56",  \
  "s57", "s58", "s59", "s60", "s61", "s62", "s63", "s64", "s65", "s66", "s67", "s68", "s69", "s70", "s71", "s72", "s73", "s74", "s75", "s76", "s77",  \
  "s78", "s79", "s80", "s81", "s82", "s83", "s84", "s85", "s86", "s87", "s88", "s89", "s90", "s91", "s92", "s93", "s94", "s95", "s96", "s97", "s98",  \
  "s99", "vcc", "scc", "memory"

__global__ __launch_bounds__(256, 1) void fused_block(const char* __restrict__ x, const char* __restrict__ kc, const char* __restrict__ vc,
                                                      const char* __restrict__ wq, const char* __restrict__ wo, const char* __restrict__ w1,
                                                      const char* __restrict__ w2, char* __restrict__ out, char* __restrict__ ws) {
  __shared__ __attribute__((aligned(16))) char lds[LDS_BYTES];
  const unsigned wid = __builtin_amdgcn_readfirstlane(threadIdx.x >> 6);
  asm volatile(
R"(
s_load_dwordx16 s[8:23], %[ka], 0
s_load_dwordx2 s[24:25], %[ka], 64
s_and_b32 s34, %[bid], 7
s_bfe_u32 s35, %[bid], 131075
s_lshr_b32 s7, %[bid], 5
s_lshl_b32 s6, s34, 2
s_add_u32 s6, s6, s35
v_mbcnt_lo_u32_b32 v236, -1, 0
v_mbcnt_hi_u32_b32 v236, -1, v236
s_waitcnt lgkmcnt(0)
s_lshl_b32 s88, s7, 5
s_add_u32 s88, s88, s6
s_mul_i32 s88, s88, 9216
s_mov_b64 s[56:57], s[8:9]
s_add_u32 s56, s56, s88
s_addc_u32 s57, s57, 0
s_and_b32 s57, s57, 65535
s_mov_b32 s58, 9216
s_mov_b32 s59, 131072
s_lshl_b32 s88, s7, 13
s_add_u32 s88, s88, 92274688
s_mov_b64 s[52:53], s[24:25]
s_add_u32 s52, s52, s88
s_addc_u32 s53, s53, 0
s_and_b32 s53, s53, 65535
s_mov_b32 s54, 8192
s_mov_b32 s55, 131072
v_and_b32 v14, 7, v236
v_lshrrev_b32 v238, 5, v236
v_bfe_u32 v239, v236, 3, 2
s_mul_i32 s77, %[wid], 384
v_mul_u32_u24 v12, 1152, v14
v_lshl_add_u32 v12, v238, 6, v12
v_lshl_add_u32 v12, v239, 3, v12
buffer_load_dwordx2 v[16:17], off, s[52:55], 0 offset:0 sc1
buffer_load_dwordx2 v[0:1], v12, s[56:59], s77 offen offset:0
buffer_load_dwordx2 v[2:3], v12, s[56:59], s77 offen offset:32
buffer_load_dwordx2 v[4:5], v12, s[56:59], s77 offen offset:128
buffer_load_dwordx2 v[6:7], v12, s[56:59], s77 offen offset:160
buffer_load_dwordx2 v[8:9], v12, s[56:59], s77 offen offset:256
buffer_load_dwordx2 v[10:11], v12, s[56:59], s77 offen offset:288
s_getpc_b64 s[60:61]
KPC:
s_and_b32 s61, s61, 65535
s_mov_b32 s62, KEND-KPC
s_mov_b32 s63, 131072
s_lshl_b32 s78, s7, 2
s_add_u32 s78, s78, s35
s_lshl_b32 s78, s78, 2
s_add_u32 s78, s78, %[wid]
s_lshl_b32 s78, s78, 10
v_lshlrev_b32 v13, 7, v14
buffer_load_dword v250, v13, s[60:63], s78 offen offset:0
v_lshlrev_b32 v13, 4, v14
v_lshl_add_u32 v13, v238, 12, v13
v_lshl_add_u32 v13, v239, 8, v13
s_cmp_lt_u32 %[wid], 2
s_cselect_b64 s[26:27], s[18:19], s[14:15]
s_cselect_b64 s[28:29], s[10:11], s[12:13]
s_mov_b32 s78, 256
s_cselect_b32 s78, 288, s78
s_mul_i32 s78, s78, s6
s_add_u32 s26, s26, s78
s_addc_u32 s27, s27, 0
s_lshr_b32 s78, s6, 1
s_lshl_b32 s78, s78, 19
s_add_u32 s78, s78, 393216
s_add_u32 s28, s28, s78
s_addc_u32 s29, s29, 0
s_cmp_lt_u32 s34, 4
s_cselect_b64 s[30:31], s[16:17], s[20:21]
s_mov_b32 s78, 1327104
s_cselect_b32 s78, 1179648, s78
s_and_b32 s79, s34, 3
s_mul_i32 s78, s78, s79
s_mul_i32 s79, s35, 288
s_add_u32 s78, s78, s79
s_add_u32 s30, s30, s78
s_addc_u32 s31, s31, 0
s_mov_b64 s[40:41], s[26:27]
s_and_b32 s41, s41, 65535
s_mov_b32 s43, 131072
s_cmp_lt_u32 %[wid], 2
s_cselect_b32 s74, 18, 16
s_mov_b32 s73, 8192
s_cselect_b32 s73, 9216, s73
s_mul_i32 s42, s73, 575
s_lshl_b32 s78, s74, 4
s_add_u32 s42, s42, s78
s_and_b32 s78, %[wid], 1
s_mul_i32 s64, s78, s73
s_lshl_b32 s64, s64, 5
s_lshl_b32 s65, s73, 6
s_mul_i32 s70, %[wid], 9216
s_add_u32 s70, s70, %[lds]
s_mov_b32 s67, 0
s_mov_b32 s69, 0
s_mov_b32 s71, 0
s_mov_b32 s68, 9
s_mov_b32 s76, -110592
v_mov_b32 v237, 2130706432
v_lshlrev_b32 v235, 4, v236
s_and_b32 s78, s35, 1
s_lshl_b32 s78, s78, 1
s_cmp_lt_u32 %[wid], 2
s_cselect_b32 s78, s78, 0
v_add_u32 v32, 0, v236
v_add_u32 v33, 64, v236
v_add_u32 v34, 128, v236
v_add_u32 v35, 192, v236
v_add_u32 v36, 256, v236
v_add_u32 v37, 320, v236
v_add_u32 v38, 384, v236
v_add_u32 v39, 448, v236
v_add_u32 v40, 512, v236
v_mul_u32_u24 v48, 3641, v32
v_mul_u32_u24 v49, 3641, v33
v_mul_u32_u24 v50, 3641, v34
v_mul_u32_u24 v51, 3641, v35
v_mul_u32_u24 v52, 3641, v36
v_mul_u32_u24 v53, 3641, v37
v_mul_u32_u24 v54, 3641, v38
v_mul_u32_u24 v55, 3641, v39
v_mul_u32_u24 v56, 3641, v40
v_lshrrev_b32 v48, 16, v48
v_lshrrev_b32 v49, 16, v49
v_lshrrev_b32 v50, 16, v50
v_lshrrev_b32 v51, 16, v51
v_lshrrev_b32 v52, 16, v52
v_lshrrev_b32 v53, 16, v53
v_lshrrev_b32 v54, 16, v54
v_lshrrev_b32 v55, 16, v55
v_lshrrev_b32 v56, 16, v56
v_mul_u32_u24 v224, 18, v48
v_mul_u32_u24 v225, 18, v49
v_mul_u32_u24 v226, 18, v50
v_mul_u32_u24 v227, 18, v51
v_mul_u32_u24 v228, 18, v52
v_mul_u32_u24 v229, 18, v53
v_mul_u32_u24 v230, 18, v54
v_mul_u32_u24 v231, 18, v55
v_mul_u32_u24 v232, 18, v56
v_sub_u32 v32, v32, v224
v_sub_u32 v33, v33, v225
v_sub_u32 v34, v34, v226
v_sub_u32 v35, v35, v227
v_sub_u32 v36, v36, v228
v_sub_u32 v37, v37, v229
v_sub_u32 v38, v38, v230
v_sub_u32 v39, v39, v231
v_sub_u32 v40, v40, v232
v_add_u32 v32, s78, v32
v_add_u32 v33, s78, v33
v_add_u32 v34, s78, v34
v_add_u32 v35, s78, v35
v_add_u32 v36, s78, v36
v_add_u32 v37, s78, v37
v_add_u32 v38, s78, v38
v_add_u32 v39, s78, v39
v_add_u32 v40, s78, v40
v_subrev_u32 v224, 18, v32
v_subrev_u32 v225, 18, v33
v_subrev_u32 v226, 18, v34
v_subrev_u32 v227, 18, v35
v_subrev_u32 v228, 18, v36
v_subrev_u32 v229, 18, v37
v_subrev_u32 v230, 18, v38
v_subrev_u32 v231, 18, v39
v_subrev_u32 v232, 18, v40
v_min_u32 v32, v32, v224
v_min_u32 v33, v33, v225
v_min_u32 v34, v34, v226
v_min_u32 v35, v35, v227
v_min_u32 v36, v36, v228
v_min_u32 v37, v37, v229
v_min_u32 v38, v38, v230
v_min_u32 v39, v39, v231
v_min_u32 v40, v40, v232
v_mul_u32_u24 v48, s73, v48
v_mul_u32_u24 v49, s73, v49
v_mul_u32_u24 v50, s73, v50
v_mul_u32_u24 v51, s73, v51
v_mul_u32_u24 v52, s73, v52
v_mul_u32_u24 v53, s73, v53
v_mul_u32_u24 v54, s73, v54
v_mul_u32_u24 v55, s73, v55
v_mul_u32_u24 v56, s73, v56
v_lshl_add_u32 v224, v32, 4, v48
v_lshl_add_u32 v225, v33, 4, v49
v_lshl_add_u32 v226, v34, 4, v50
v_lshl_add_u32 v227, v35, 4, v51
v_lshl_add_u32 v228, v36, 4, v52
v_lshl_add_u32 v229, v37, 4, v53
v_lshl_add_u32 v230, v38, 4, v54
v_lshl_add_u32 v231, v39, 4, v55
v_lshl_add_u32 v232, v40, 4, v56
v_cmp_le_u32 vcc, s74, v32
s_nop 1
v_cndmask_b32 v224, v224, v237, vcc
v_cmp_le_u32 vcc, s74, v33
s_nop 1
v_cndmask_b32 v225, v225, v237, vcc
v_cmp_le_u32 vcc, s74, v34
s_nop 1
v_cndmask_b32 v226, v226, v237, vcc
v_cmp_le_u32 vcc, s74, v35
s_nop 1
v_cndmask_b32 v227, v227, v237, vcc
v_cmp_le_u32 vcc, s74, v36
s_nop 1
v_cndmask_b32 v228, v228, v237, vcc
v_cmp_le_u32 vcc, s74, v37
s_nop 1
v_cndmask_b32 v229, v229, v237, vcc
v_cmp_le_u32 vcc, s74, v38
s_nop 1
v_cndmask_b32 v230, v230, v237, vcc
v_cmp_le_u32 vcc, s74, v39
s_nop 1
v_cndmask_b32 v231, v231, v237, vcc
v_cmp_le_u32 vcc, s74, v40
s_nop 1
v_cndmask_b32 v232, v232, v237, vcc
s_add_u32 m0, s70, s69
s_nop 0
buffer_load_dwordx4 v224, s[40:43], s64 offen lds
s_add_u32 m0, m0, 1024
s_nop 0
buffer_load_dwordx4 v225, s[40:43], s64 offen lds
s_add_u32 m0, m0, 1024
s_nop 0
buffer_load_dwordx4 v226, s[40:43], s64 offen lds
s_add_u32 m0, m0, 1024
s_nop 0
buffer_load_dwordx4 v227, s[40:43], s64 offen lds
s_add_u32 m0, m0, 1024
s_nop 0
buffer_load_dwordx4 v228, s[40:43], s64 offen lds
s_add_u32 m0, m0, 1024
s_nop 0
buffer_load_dwordx4 v229, s[40:43], s64 offen lds
s_add_u32 m0, m0, 1024
s_nop 0
buffer_load_dwordx4 v230, s[40:43], s64 offen lds
s_add_u32 m0, m0, 1024
s_nop 0
buffer_load_dwordx4 v231, s[40:43], s64 offen lds
s_add_u32 m0, m0, 1024
s_nop 0
buffer_load_dwordx4 v232, s[40:43], s64 offen lds
s_add_u32 s64, s64, s65
s_add_u32 s69, s69, 36864
s_cmp_eq_u32 s69, 147456
s_cselect_b32 s69, 0, s69
s_lshl_b32 s77, s74, 4
s_mul_i32 s77, s77, s35
s_sub_u32 s84, s40, s77
s_subb_u32 s85, s41, 0
s_add_u32 s86, s42, s77
s_mov_b32 s87, 131072
s_lshl_b32 s95, s7, 2
s_add_u32 s95, s95, s35
s_lshl_b32 s95, s95, 1
s_and_b32 s78, %[wid], 1
s_add_u32 s95, s95, s78
s_add_u32 s95, s95, 192
s_mul_i32 s95, s95, s73
s_lshl_b32 s94, s73, 6
s_lshl_b32 s88, s7, 2
s_lshr_b32 s78, s6, 3
s_add_u32 s88, s88, s78
s_mul_i32 s88, s88, 73728
s_bfe_u32 s78, s6, 131073
s_lshl_b32 s78, s78, 10
s_add_u32 s88, s88, s78
s_and_b32 s78, s6, 1
s_lshl_b32 s78, s78, 7
s_add_u32 s88, s88, s78
s_mov_b64 s[60:61], s[24:25]
s_add_u32 s60, s60, s88
s_addc_u32 s61, s61, 0
s_and_b32 s61, s61, 65535
s_mov_b32 s62, 70528
s_mov_b32 s63, 131072
s_lshl_b32 s78, %[wid], 2
s_add_u32 s78, s78, %[lds]
s_add_u32 s78, s78, 147456
v_lshlrev_b32 v81, 4, v14
v_add_u32 v80, s78, v81
s_add_u32 s78, %[lds], 147456
v_add_u32 v81, s78, v81
s_waitcnt vmcnt(10)
v_lshlrev_b32 v32, 16, v0
v_and_b32 v33, 4294901760, v0
v_lshlrev_b32 v34, 16, v1
v_and_b32 v35, 4294901760, v1
v_lshlrev_b32 v36, 16, v2
v_and_b32 v37, 4294901760, v2
v_lshlrev_b32 v38, 16, v3
v_and_b32 v39, 4294901760, v3
v_lshlrev_b32 v40, 16, v4
v_and_b32 v41, 4294901760, v4
v_lshlrev_b32 v42, 16, v5
v_and_b32 v43, 4294901760, v5
v_lshlrev_b32 v44, 16, v6
v_and_b32 v45, 4294901760, v6
v_lshlrev_b32 v46, 16, v7
v_and_b32 v47, 4294901760, v7
v_lshlrev_b32 v48, 16, v8
v_and_b32 v49, 4294901760, v8
v_lshlrev_b32 v50, 16, v9
v_and_b32 v51, 4294901760, v9
v_lshlrev_b32 v52, 16, v10
v_and_b32 v53, 4294901760, v10
v_lshlrev_b32 v54, 16, v11
v_and_b32 v55, 4294901760, v11
v_readfirstlane_b32 s82, v16
v_readfirstlane_b32 s83, v17
v_pk_add_f32 v[64:65], v[32:33], v[40:41]
v_pk_add_f32 v[66:67], v[34:35], v[42:43]
v_pk_add_f32 v[68:69], v[36:37], v[44:45]
v_pk_add_f32 v[70:71], v[38:39], v[46:47]
v_pk_add_f32 v[64:65], v[64:65], v[48:49]
v_pk_add_f32 v[66:67], v[66:67], v[50:51]
v_pk_add_f32 v[68:69], v[68:69], v[52:53]
v_pk_add_f32 v[70:71], v[70:71], v[54:55]
s_add_u32 s82, s82, 1
s_addc_u32 s83, s83, 0
v_pk_add_f32 v[64:65], v[64:65], v[66:67]
v_pk_add_f32 v[68:69], v[68:69], v[70:71]
v_pk_add_f32 v[64:65], v[64:65], v[68:69]
v_add_f32 v72, v64, v65
s_nop 1
v_add_f32_dpp v72, v72, v72 row_ror:8 row_mask:0xf bank_mask:0xf
v_mov_b32 v73, v72
s_nop 1
v_permlane16_swap_b32 v72, v73
v_add_f32 v72, v72, v73
v_mov_b32 v73, v72
s_nop 1
v_permlane32_swap_b32 v72, v73
v_add_f32 v72, v72, v73
ds_write_b32 v80, v72 offset:0
s_waitcnt lgkmcnt(0)
s_barrier
ds_read_b128 v[76:79], v81 offset:0
s_waitcnt lgkmcnt(0)
v_add_f32 v72, v76, v77
v_add_f32 v72, v72, v78
v_mul_f32 v72, 0x3ae38e39, v72
v_mov_b32 v82, v72
v_pk_add_f32 v[88:89], v[32:33], v[82:83] op_sel_hi:[1,0] neg_lo:[0,1] neg_hi:[0,1]
v_pk_add_f32 v[90:91], v[34:35], v[82:83] op_sel_hi:[1,0] neg_lo:[0,1] neg_hi:[0,1]
v_pk_add_f32 v[92:93], v[36:37], v[82:83] op_sel_hi:[1,0] neg_lo:[0,1] neg_hi:[0,1]
v_pk_add_f32 v[94:95], v[38:39], v[82:83] op_sel_hi:[1,0] neg_lo:[0,1] neg_hi:[0,1]
v_pk_mul_f32 v[64:65], v[88:89], v[88:89]
v_pk_mul_f32 v[66:67], v[90:91], v[90:91]
v_pk_mul_f32 v[68:69], v[92:93], v[92:93]
v_pk_mul_f32 v[70:71], v[94:95], v[94:95]
v_pk_add_f32 v[88:89], v[40:41], v[82:83] op_sel_hi:[1,0] neg_lo:[0,1] neg_hi:[0,1]
v_pk_add_f32 v[90:91], v[42:43], v[82:83] op_sel_hi:[1,0] neg_lo:[0,1] neg_hi:[0,1]
v_pk_add_f32 v[92:93], v[44:45], v[82:83] op_sel_hi:[1,0] neg_lo:[0,1] neg_hi:[0,1]
v_pk_add_f32 v[94:95], v[46:47], v[82:83] op_sel_hi:[1,0] neg_lo:[0,1] neg_hi:[0,1]
v_pk_fma_f32 v[64:65], v[88:89], v[88:89], v[64:65]
v_pk_fma_f32 v[66:67], v[90:91], v[90:91], v[66:67]
v_pk_fma_f32 v[68:69], v[92:93], v[92:93], v[68:69]
v_pk_fma_f32 v[70:71], v[94:95], v[94:95], v[70:71]
v_pk_add_f32 v[88:89], v[48:49], v[82:83] op_sel_hi:[1,0] neg_lo:[0,1] neg_hi:[0,1]
v_pk_add_f32 v[90:91], v[50:51], v[82:83] op_sel_hi:[1,0] neg_lo:[0,1] neg_hi:[0,1]
v_pk_add_f32 v[92:93], v[52:53], v[82:83] op_sel_hi:[1,0] neg_lo:[0,1] neg_hi:[0,1]
v_pk_add_f32 v[94:95], v[54:55], v[82:83] op_sel_hi:[1,0] neg_lo:[0,1] neg_hi:[0,1]
v_pk_fma_f32 v[64:65], v[88:89], v[88:89], v[64:65]
v_pk_fma_f32 v[66:67], v[90:91], v[90:91], v[66:67]
v_pk_fma_f32 v[68:69], v[92:93], v[92:93], v[68:69]
v_pk_fma_f32 v[70:71], v[94:95], v[94:95], v[70:71]
v_pk_add_f32 v[64:65], v[64:65], v[66:67]
v_pk_add_f32 v[68:69], v[68:69], v[70:71]
v_pk_add_f32 v[64:65], v[64:65], v[68:69]
v_add_f32 v72, v64, v65
s_nop 1
v_add_f32_dpp v72, v72, v72 row_ror:8 row_mask:0xf bank_mask:0xf
v_mov_b32 v73, v72
s_nop 1
v_permlane16_swap_b32 v72, v73
v_add_f32 v72, v72, v73
v_mov_b32 v73, v72
s_nop 1
v_permlane32_swap_b32 v72, v73
v_add_f32 v72, v72, v73
ds_write_b32 v80, v72 offset:128
s_waitcnt lgkmcnt(0)
s_barrier
ds_read_b128 v[76:79], v81 offset:128
s_waitcnt lgkmcnt(0)
v_add_f32 v72, v76, v77
v_add_f32 v72, v72, v78
v_mul_f32 v72, 0x3ae38e39, v72
v_cvt_pk_bf16_f32 v72, v72, v72
v_lshlrev_b32 v72, 16, v72
v_add_f32 v72, 0x3727c5ac, v72
)"
R"(
v_cvt_pk_bf16_f32 v72, v72, v72
v_lshlrev_b32 v72, 16, v72
v_sqrt_f32 v72, v72
s_nop 0
v_cvt_pk_bf16_f32 v72, v72, v72
v_lshlrev_b32 v72, 16, v72
v_rcp_f32 v84, v72
v_cvt_pk_bf16_f32 v82, v82, v82
v_lshlrev_b32 v82, 16, v82
v_mul_f32 v84, 0x3e0293ee, v84
v_pk_add_f32 v[32:33], v[32:33], v[82:83] op_sel_hi:[1,0] neg_lo:[0,1] neg_hi:[0,1]
v_pk_add_f32 v[34:35], v[34:35], v[82:83] op_sel_hi:[1,0] neg_lo:[0,1] neg_hi:[0,1]
v_pk_add_f32 v[36:37], v[36:37], v[82:83] op_sel_hi:[1,0] neg_lo:[0,1] neg_hi:[0,1]
v_pk_add_f32 v[38:39], v[38:39], v[82:83] op_sel_hi:[1,0] neg_lo:[0,1] neg_hi:[0,1]
v_pk_add_f32 v[40:41], v[40:41], v[82:83] op_sel_hi:[1,0] neg_lo:[0,1] neg_hi:[0,1]
v_pk_add_f32 v[42:43], v[42:43], v[82:83] op_sel_hi:[1,0] neg_lo:[0,1] neg_hi:[0,1]
v_pk_add_f32 v[44:45], v[44:45], v[82:83] op_sel_hi:[1,0] neg_lo:[0,1] neg_hi:[0,1]
v_pk_add_f32 v[46:47], v[46:47], v[82:83] op_sel_hi:[1,0] neg_lo:[0,1] neg_hi:[0,1]
v_pk_add_f32 v[48:49], v[48:49], v[82:83] op_sel_hi:[1,0] neg_lo:[0,1] neg_hi:[0,1]
v_pk_add_f32 v[50:51], v[50:51], v[82:83] op_sel_hi:[1,0] neg_lo:[0,1] neg_hi:[0,1]
v_pk_add_f32 v[52:53], v[52:53], v[82:83] op_sel_hi:[1,0] neg_lo:[0,1] neg_hi:[0,1]
v_pk_add_f32 v[54:55], v[54:55], v[82:83] op_sel_hi:[1,0] neg_lo:[0,1] neg_hi:[0,1]
v_cvt_pk_bf16_f32 v0, v32, v33
v_cvt_pk_bf16_f32 v1, v34, v35
v_cvt_pk_bf16_f32 v2, v36, v37
v_cvt_pk_bf16_f32 v3, v38, v39
v_cvt_pk_bf16_f32 v4, v40, v41
v_cvt_pk_bf16_f32 v5, v42, v43
v_cvt_pk_bf16_f32 v6, v44, v45
v_cvt_pk_bf16_f32 v7, v46, v47
v_cvt_pk_bf16_f32 v8, v48, v49
v_cvt_pk_bf16_f32 v9, v50, v51
v_cvt_pk_bf16_f32 v10, v52, v53
v_cvt_pk_bf16_f32 v11, v54, v55
v_lshlrev_b32 v32, 16, v0
v_and_b32 v33, 4294901760, v0
v_lshlrev_b32 v34, 16, v1
v_and_b32 v35, 4294901760, v1
v_lshlrev_b32 v36, 16, v2
v_and_b32 v37, 4294901760, v2
v_lshlrev_b32 v38, 16, v3
v_and_b32 v39, 4294901760, v3
v_lshlrev_b32 v40, 16, v4
v_and_b32 v41, 4294901760, v4
v_lshlrev_b32 v42, 16, v5
v_and_b32 v43, 4294901760, v5
v_lshlrev_b32 v44, 16, v6
v_and_b32 v45, 4294901760, v6
v_lshlrev_b32 v46, 16, v7
v_and_b32 v47, 4294901760, v7
v_lshlrev_b32 v48, 16, v8
v_and_b32 v49, 4294901760, v8
v_lshlrev_b32 v50, 16, v9
v_and_b32 v51, 4294901760, v9
v_lshlrev_b32 v52, 16, v10
v_and_b32 v53, 4294901760, v10
v_lshlrev_b32 v54, 16, v11
v_and_b32 v55, 4294901760, v11
v_pk_mul_f32 v[32:33], v[32:33], v[84:85] op_sel_hi:[1,0]
v_pk_mul_f32 v[34:35], v[34:35], v[84:85] op_sel_hi:[1,0]
v_pk_mul_f32 v[36:37], v[36:37], v[84:85] op_sel_hi:[1,0]
v_pk_mul_f32 v[38:39], v[38:39], v[84:85] op_sel_hi:[1,0]
v_pk_mul_f32 v[40:41], v[40:41], v[84:85] op_sel_hi:[1,0]
v_pk_mul_f32 v[42:43], v[42:43], v[84:85] op_sel_hi:[1,0]
v_pk_mul_f32 v[44:45], v[44:45], v[84:85] op_sel_hi:[1,0]
v_pk_mul_f32 v[46:47], v[46:47], v[84:85] op_sel_hi:[1,0]
v_pk_mul_f32 v[48:49], v[48:49], v[84:85] op_sel_hi:[1,0]
v_pk_mul_f32 v[50:51], v[50:51], v[84:85] op_sel_hi:[1,0]
v_pk_mul_f32 v[52:53], v[52:53], v[84:85] op_sel_hi:[1,0]
v_pk_mul_f32 v[54:55], v[54:55], v[84:85] op_sel_hi:[1,0]
s_mul_i32 s77, %[wid], 24576
v_cvt_pk_bf16_f32 v0, v32, v33
v_cvt_pk_bf16_f32 v1, v34, v35
v_cvt_pk_bf16_f32 v2, v36, v37
v_cvt_pk_bf16_f32 v3, v38, v39
buffer_store_dwordx4 v[0:3], v13, s[60:63], s77 offen offset:0 sc1
s_add_u32 s77, s77, 8192
v_cvt_pk_bf16_f32 v4, v40, v41
v_cvt_pk_bf16_f32 v5, v42, v43
v_cvt_pk_bf16_f32 v6, v44, v45
v_cvt_pk_bf16_f32 v7, v46, v47
buffer_store_dwordx4 v[4:7], v13, s[60:63], s77 offen offset:0 sc1
s_add_u32 s77, s77, 8192
v_cvt_pk_bf16_f32 v8, v48, v49
v_cvt_pk_bf16_f32 v9, v50, v51
v_cvt_pk_bf16_f32 v10, v52, v53
v_cvt_pk_bf16_f32 v11, v54, v55
buffer_store_dwordx4 v[8:11], v13, s[60:63], s77 offen offset:0 sc1
s_add_u32 s77, s77, 8192
s_lshl_b32 s79, s7, 2
s_add_u32 s79, s79, %[wid]
s_mul_i32 s88, s79, 73728
s_mov_b64 s[44:45], s[24:25]
s_add_u32 s44, s44, s88
s_addc_u32 s45, s45, 0
s_and_b32 s45, s45, 65535
s_mov_b32 s46, 73728
s_mov_b32 s47, 131072
s_mul_i32 s88, s79, 1114112
s_add_u32 s88, s88, 4194304
s_mov_b64 s[48:49], s[24:25]
s_add_u32 s48, s48, s88
s_addc_u32 s49, s49, 0
s_and_b32 s49, s49, 65535
s_mov_b32 s50, 1114112
s_mov_b32 s51, 131072
s_mov_b32 s90, 16256
s_mov_b32 s91, 1065353216
s_lshl_b32 s39, s6, 2
s_add_u32 s39, s39, %[wid]
s_lshl_b32 s39, s39, 3
s_cmp_lt_u32 s34, 4
s_cselect_b32 s93, 32, 36
s_waitcnt vmcnt(0)
s_barrier
s_cmp_lg_u32 %[wid], 0
s_cbranch_scc1 F_LN
s_lshl_b32 s77, s6, 3
s_add_u32 s77, s77, 256
v_mov_b32 v238, s82
v_mov_b32 v239, s83
s_mov_b64 exec, 1
buffer_store_dwordx2 v[238:239], off, s[52:55], s77 offset:0 sc1
s_mov_b64 exec, -1
s_nop 1
F_LN:
s_add_u32 m0, s70, s69
s_nop 0
buffer_load_dwordx4 v224, s[40:43], s64 offen lds
s_add_u32 m0, m0, 1024
s_nop 0
buffer_load_dwordx4 v225, s[40:43], s64 offen lds
s_add_u32 m0, m0, 1024
s_nop 0
buffer_load_dwordx4 v226, s[40:43], s64 offen lds
s_add_u32 m0, m0, 1024
s_nop 0
buffer_load_dwordx4 v227, s[40:43], s64 offen lds
s_add_u32 m0, m0, 1024
s_nop 0
buffer_load_dwordx4 v228, s[40:43], s64 offen lds
s_add_u32 m0, m0, 1024
s_nop 0
buffer_load_dwordx4 v229, s[40:43], s64 offen lds
s_add_u32 m0, m0, 1024
s_nop 0
buffer_load_dwordx4 v230, s[40:43], s64 offen lds
s_add_u32 m0, m0, 1024
s_nop 0
buffer_load_dwordx4 v231, s[40:43], s64 offen lds
s_add_u32 m0, m0, 1024
s_nop 0
buffer_load_dwordx4 v232, s[40:43], s64 offen lds
s_add_u32 s64, s64, s65
s_add_u32 s69, s69, 36864
s_cmp_eq_u32 s69, 147456
s_cselect_b32 s69, 0, s69
s_mov_b32 s73, 256
s_mov_b32 s74, 16
v_add_u32 v96, 0, v236
v_add_u32 v97, 64, v236
v_add_u32 v98, 128, v236
v_add_u32 v99, 192, v236
v_add_u32 v100, 256, v236
v_add_u32 v101, 320, v236
v_add_u32 v102, 384, v236
v_add_u32 v103, 448, v236
v_add_u32 v104, 512, v236
v_mul_u32_u24 v108, 3641, v96
v_mul_u32_u24 v109, 3641, v97
v_mul_u32_u24 v110, 3641, v98
v_mul_u32_u24 v111, 3641, v99
v_mul_u32_u24 v112, 3641, v100
v_mul_u32_u24 v113, 3641, v101
v_mul_u32_u24 v114, 3641, v102
v_mul_u32_u24 v115, 3641, v103
v_mul_u32_u24 v116, 3641, v104
v_lshrrev_b32 v108, 16, v108
v_lshrrev_b32 v109, 16, v109
v_lshrrev_b32 v110, 16, v110
v_lshrrev_b32 v111, 16, v111
v_lshrrev_b32 v112, 16, v112
v_lshrrev_b32 v113, 16, v113
v_lshrrev_b32 v114, 16, v114
v_lshrrev_b32 v115, 16, v115
v_lshrrev_b32 v116, 16, v116
v_mul_u32_u24 v120, 18, v108
v_mul_u32_u24 v121, 18, v109
v_mul_u32_u24 v122, 18, v110
v_mul_u32_u24 v123, 18, v111
v_mul_u32_u24 v124, 18, v112
v_mul_u32_u24 v125, 18, v113
v_mul_u32_u24 v126, 18, v114
v_mul_u32_u24 v127, 18, v115
v_mul_u32_u24 v128, 18, v116
v_sub_u32 v96, v96, v120
v_sub_u32 v97, v97, v121
v_sub_u32 v98, v98, v122
v_sub_u32 v99, v99, v123
v_sub_u32 v100, v100, v124
v_sub_u32 v101, v101, v125
v_sub_u32 v102, v102, v126
v_sub_u32 v103, v103, v127
v_sub_u32 v104, v104, v128
v_mul_u32_u24 v108, s73, v108
v_mul_u32_u24 v109, s73, v109
v_mul_u32_u24 v110, s73, v110
v_mul_u32_u24 v111, s73, v111
v_mul_u32_u24 v112, s73, v112
v_mul_u32_u24 v113, s73, v113
v_mul_u32_u24 v114, s73, v114
v_mul_u32_u24 v115, s73, v115
v_mul_u32_u24 v116, s73, v116
v_lshl_add_u32 v120, v96, 4, v108
v_lshl_add_u32 v121, v97, 4, v109
v_lshl_add_u32 v122, v98, 4, v110
v_lshl_add_u32 v123, v99, 4, v111
v_lshl_add_u32 v124, v100, 4, v112
v_lshl_add_u32 v125, v101, 4, v113
v_lshl_add_u32 v126, v102, 4, v114
v_lshl_add_u32 v127, v103, 4, v115
v_lshl_add_u32 v128, v104, 4, v116
v_cmp_le_u32 vcc, s74, v96
s_nop 1
v_cndmask_b32 v120, v120, v237, vcc
v_cmp_le_u32 vcc, s74, v97
s_nop 1
v_cndmask_b32 v121, v121, v237, vcc
v_cmp_le_u32 vcc, s74, v98
s_nop 1
v_cndmask_b32 v122, v122, v237, vcc
v_cmp_le_u32 vcc, s74, v99
s_nop 1
v_cndmask_b32 v123, v123, v237, vcc
v_cmp_le_u32 vcc, s74, v100
s_nop 1
v_cndmask_b32 v124, v124, v237, vcc
v_cmp_le_u32 vcc, s74, v101
s_nop 1
v_cndmask_b32 v125, v125, v237, vcc
v_cmp_le_u32 vcc, s74, v102
s_nop 1
v_cndmask_b32 v126, v126, v237, vcc
v_cmp_le_u32 vcc, s74, v103
s_nop 1
v_cndmask_b32 v127, v127, v237, vcc
v_cmp_le_u32 vcc, s74, v104
s_nop 1
v_cndmask_b32 v128, v128, v237, vcc
v_accvgpr_write_b32 a183, v120
v_accvgpr_write_b32 a184, v121
v_accvgpr_write_b32 a185, v122
v_accvgpr_write_b32 a186, v123
v_accvgpr_write_b32 a187, v124
v_accvgpr_write_b32 a188, v125
v_accvgpr_write_b32 a189, v126
v_accvgpr_write_b32 a190, v127
v_accvgpr_write_b32 a191, v128
s_mov_b32 s73, 1152
v_add_u32 v96, 0, v236
v_add_u32 v97, 64, v236
v_add_u32 v98, 128, v236
v_add_u32 v99, 192, v236
v_add_u32 v100, 256, v236
v_add_u32 v101, 320, v236
v_add_u32 v102, 384, v236
v_add_u32 v103, 448, v236
v_add_u32 v104, 512, v236
v_mul_u32_u24 v108, 3641, v96
v_mul_u32_u24 v109, 3641, v97
v_mul_u32_u24 v110, 3641, v98
v_mul_u32_u24 v111, 3641, v99
v_mul_u32_u24 v112, 3641, v100
v_mul_u32_u24 v113, 3641, v101
v_mul_u32_u24 v114, 3641, v102
v_mul_u32_u24 v115, 3641, v103
v_mul_u32_u24 v116, 3641, v104
v_lshrrev_b32 v108, 16, v108
v_lshrrev_b32 v109, 16, v109
v_lshrrev_b32 v110, 16, v110
v_lshrrev_b32 v111, 16, v111
v_lshrrev_b32 v112, 16, v112
v_lshrrev_b32 v113, 16, v113
v_lshrrev_b32 v114, 16, v114
v_lshrrev_b32 v115, 16, v115
v_lshrrev_b32 v116, 16, v116
v_mul_u32_u24 v240, 18, v108
v_mul_u32_u24 v241, 18, v109
v_mul_u32_u24 v242, 18, v110
v_mul_u32_u24 v243, 18, v111
v_mul_u32_u24 v244, 18, v112
v_mul_u32_u24 v245, 18, v113
v_mul_u32_u24 v246, 18, v114
v_mul_u32_u24 v247, 18, v115
v_mul_u32_u24 v248, 18, v116
v_sub_u32 v96, v96, v240
v_sub_u32 v97, v97, v241
v_sub_u32 v98, v98, v242
v_sub_u32 v99, v99, v243
v_sub_u32 v100, v100, v244
v_sub_u32 v101, v101, v245
v_sub_u32 v102, v102, v246
v_sub_u32 v103, v103, v247
v_sub_u32 v104, v104, v248
v_mul_u32_u24 v108, s73, v108
v_mul_u32_u24 v109, s73, v109
v_mul_u32_u24 v110, s73, v110
v_mul_u32_u24 v111, s73, v111
v_mul_u32_u24 v112, s73, v112
v_mul_u32_u24 v113, s73, v113
v_mul_u32_u24 v114, s73, v114
v_mul_u32_u24 v115, s73, v115
v_mul_u32_u24 v116, s73, v116
v_lshl_add_u32 v240, v96, 4, v108
v_lshl_add_u32 v241, v97, 4, v109
v_lshl_add_u32 v242, v98, 4, v110
v_lshl_add_u32 v243, v99, 4, v111
v_lshl_add_u32 v244, v100, 4, v112
v_lshl_add_u32 v245, v101, 4, v113
v_lshl_add_u32 v246, v102, 4, v114
v_lshl_add_u32 v247, v103, 4, v115
v_lshl_add_u32 v248, v104, 4, v116
v_lshrrev_b32 v238, 2, v236
v_mul_u32_u24 v238, 288, v238
v_and_b32 v239, 3, v236
v_add_u32 v238, %[lds], v238
v_lshl_add_u32 v233, v239, 3, v238
v_lshl_add_u32 v234, v239, 4, v238
v_lshlrev_b32 v15, 3, v14
v_lshlrev_b32 v252, 5, v14
s_lshl_b32 s77, %[wid], 6
s_add_u32 s77, s77, 256
s_mov_b32 s72, 0
P_LN:
buffer_load_dwordx2 v[250:251], v15, s[52:55], s77 offen offset:0 sc1
s_waitcnt vmcnt(0)
v_cmp_ne_u64 vcc, s[82:83], v[250:251]
s_cbranch_vccz P_LN_OK
s_sleep 1
s_add_u32 s72, s72, 1
s_cmp_lt_u32 s72, 400000
s_cbranch_scc1 P_LN
P_LN_OK:
buffer_load_dwordx4 a[192:195], v235, s[44:47], s71 offen offset:0 sc1
buffer_load_dwordx4 a[196:199], v235, s[44:47], s71 offen offset:1024 sc1
buffer_load_dwordx4 a[200:203], v235, s[44:47], s71 offen offset:2048 sc1
buffer_load_dwordx4 a[204:207], v235, s[44:47], s71 offen offset:3072 sc1
s_add_u32 s71, s71, 4096
buffer_load_dwordx4 a[208:211], v235, s[44:47], s71 offen offset:0 sc1
buffer_load_dwordx4 a[212:215], v235, s[44:47], s71 offen offset:1024 sc1
buffer_load_dwordx4 a[216:219], v235, s[44:47], s71 offen offset:2048 sc1
buffer_load_dwordx4 a[220:223], v235, s[44:47], s71 offen offset:3072 sc1
s_add_u32 s71, s71, 4096
ds_read_b64_tr_b16 a[112:113], v234 offset:18432
ds_read_b64_tr_b16 a[114:115], v234 offset:23040
ds_read_b64_tr_b16 a[116:117], v234 offset:18440
ds_read_b64_tr_b16 a[118:119], v234 offset:23048
ds_read_b64_tr_b16 a[120:121], v234 offset:18496
ds_read_b64_tr_b16 a[122:123], v234 offset:23104
ds_read_b64_tr_b16 a[124:125], v234 offset:18504
ds_read_b64_tr_b16 a[126:127], v234 offset:23112
ds_read_b64_tr_b16 a[128:129], v234 offset:18560
ds_read_b64_tr_b16 a[130:131], v234 offset:23168
ds_read_b64_tr_b16 a[132:133], v234 offset:18568
ds_read_b64_tr_b16 a[134:135], v234 offset:23176
s_waitcnt vmcnt(4) lgkmcnt(10)
v_mfma_f32_16x16x32_bf16 v[0:3], a[112:115], a[192:195], 0
ds_read_b64_tr_b16 a[136:137], v234 offset:18624
v_mfma_f32_16x16x32_bf16 v[4:7], a[112:115], a[196:199], 0
ds_read_b64_tr_b16 a[138:139], v234 offset:23232
s_add_u32 m0, s70, s69
v_mfma_f32_16x16x32_bf16 v[8:11], a[112:115], a[200:203], 0
buffer_load_dwordx4 v224, s[40:43], s64 offen lds
v_mfma_f32_16x16x32_bf16 v[12:15], a[112:115], a[204:207], 0
buffer_load_dwordx4 a[48:51], v235, s[44:47], s71 offen offset:0 sc1
s_waitcnt lgkmcnt(10)
v_mfma_f32_16x16x32_bf16 v[16:19], a[116:119], a[192:195], 0
ds_read_b64_tr_b16 a[140:141], v234 offset:18632
v_mfma_f32_16x16x32_bf16 v[20:23], a[116:119], a[196:199], 0
ds_read_b64_tr_b16 a[142:143], v234 offset:23240
s_add_u32 m0, m0, 1024
v_mfma_f32_16x16x32_bf16 v[24:27], a[116:119], a[200:203], 0
buffer_load_dwordx4 v225, s[40:43], s64 offen lds
v_mfma_f32_16x16x32_bf16 v[28:31], a[116:119], a[204:207], 0
s_waitcnt lgkmcnt(10)
v_mfma_f32_16x16x32_bf16 v[32:35], a[120:123], a[192:195], 0
ds_read_b64_tr_b16 a[144:145], v233 offset:0
v_mfma_f32_16x16x32_bf16 v[36:39], a[120:123], a[196:199], 0
ds_read_b64_tr_b16 a[146:147], v233 offset:4608
s_add_u32 m0, m0, 1024
v_mfma_f32_16x16x32_bf16 v[40:43], a[120:123], a[200:203], 0
buffer_load_dwordx4 v226, s[40:43], s64 offen lds
v_mfma_f32_16x16x32_bf16 v[44:47], a[120:123], a[204:207], 0
s_waitcnt lgkmcnt(10)
v_mfma_f32_16x16x32_bf16 v[48:51], a[124:127], a[192:195], 0
ds_read_b64_tr_b16 a[148:149], v233 offset:32
v_mfma_f32_16x16x32_bf16 v[52:55], a[124:127], a[196:199], 0
ds_read_b64_tr_b16 a[150:151], v233 offset:4640
s_add_u32 m0, m0, 1024
v_mfma_f32_16x16x32_bf16 v[56:59], a[124:127], a[200:203], 0
)"
R"(
buffer_load_dwordx4 v227, s[40:43], s64 offen lds
v_mfma_f32_16x16x32_bf16 v[60:63], a[124:127], a[204:207], 0
buffer_load_dwordx4 a[52:55], v235, s[44:47], s71 offen offset:1024 sc1
s_waitcnt lgkmcnt(10)
v_mfma_f32_16x16x32_bf16 v[64:67], a[128:131], a[192:195], 0
ds_read_b64_tr_b16 a[152:153], v233 offset:64
v_mfma_f32_16x16x32_bf16 v[68:71], a[128:131], a[196:199], 0
ds_read_b64_tr_b16 a[154:155], v233 offset:4672
s_add_u32 m0, m0, 1024
v_mfma_f32_16x16x32_bf16 v[72:75], a[128:131], a[200:203], 0
buffer_load_dwordx4 v228, s[40:43], s64 offen lds
v_mfma_f32_16x16x32_bf16 v[76:79], a[128:131], a[204:207], 0
s_waitcnt lgkmcnt(10)
v_mfma_f32_16x16x32_bf16 v[80:83], a[132:135], a[192:195], 0
ds_read_b64_tr_b16 a[156:157], v233 offset:96
v_mfma_f32_16x16x32_bf16 v[84:87], a[132:135], a[196:199], 0
ds_read_b64_tr_b16 a[158:159], v233 offset:4704
v_mfma_f32_16x16x32_bf16 v[88:91], a[132:135], a[200:203], 0
buffer_load_dwordx3 a[180:182], v235, s[84:87], s95 offen offset:0
s_add_u32 s95, s95, s94
v_mfma_f32_16x16x32_bf16 v[92:95], a[132:135], a[204:207], 0
s_waitcnt lgkmcnt(10)
v_mfma_f32_16x16x32_bf16 v[96:99], a[136:139], a[192:195], 0
ds_read_b64_tr_b16 a[160:161], v233 offset:128
v_mfma_f32_16x16x32_bf16 v[100:103], a[136:139], a[196:199], 0
ds_read_b64_tr_b16 a[162:163], v233 offset:4736
s_add_u32 m0, m0, 1024
v_mfma_f32_16x16x32_bf16 v[104:107], a[136:139], a[200:203], 0
buffer_load_dwordx4 v229, s[40:43], s64 offen lds
v_mfma_f32_16x16x32_bf16 v[108:111], a[136:139], a[204:207], 0
buffer_load_dwordx4 a[56:59], v235, s[44:47], s71 offen offset:2048 sc1
s_waitcnt lgkmcnt(10)
v_mfma_f32_16x16x32_bf16 v[112:115], a[140:143], a[192:195], 0
ds_read_b64_tr_b16 a[164:165], v233 offset:160
v_mfma_f32_16x16x32_bf16 v[116:119], a[140:143], a[196:199], 0
ds_read_b64_tr_b16 a[166:167], v233 offset:4768
s_add_u32 m0, m0, 1024
v_mfma_f32_16x16x32_bf16 v[120:123], a[140:143], a[200:203], 0
buffer_load_dwordx4 v230, s[40:43], s64 offen lds
v_mfma_f32_16x16x32_bf16 v[124:127], a[140:143], a[204:207], 0
s_waitcnt lgkmcnt(10)
v_mfma_f32_16x16x32_bf16 v[128:131], a[144:147], a[192:195], 0
ds_read_b64_tr_b16 a[168:169], v233 offset:192
v_mfma_f32_16x16x32_bf16 v[132:135], a[144:147], a[196:199], 0
ds_read_b64_tr_b16 a[170:171], v233 offset:4800
s_add_u32 m0, m0, 1024
v_mfma_f32_16x16x32_bf16 v[136:139], a[144:147], a[200:203], 0
buffer_load_dwordx4 v231, s[40:43], s64 offen lds
v_mfma_f32_16x16x32_bf16 v[140:143], a[144:147], a[204:207], 0
s_waitcnt lgkmcnt(10)
v_mfma_f32_16x16x32_bf16 v[144:147], a[148:151], a[192:195], 0
ds_read_b64_tr_b16 a[172:173], v233 offset:224
v_mfma_f32_16x16x32_bf16 v[148:151], a[148:151], a[196:199], 0
ds_read_b64_tr_b16 a[174:175], v233 offset:4832
s_add_u32 m0, m0, 1024
v_mfma_f32_16x16x32_bf16 v[152:155], a[148:151], a[200:203], 0
buffer_load_dwordx4 v232, s[40:43], s64 offen lds
v_mfma_f32_16x16x32_bf16 v[156:159], a[148:151], a[204:207], 0
buffer_load_dwordx4 a[60:63], v235, s[44:47], s71 offen offset:3072 sc1
s_add_u32 s71, s71, 4096
s_waitcnt lgkmcnt(10)
v_mfma_f32_16x16x32_bf16 v[160:163], a[152:155], a[192:195], 0
ds_read_b64_tr_b16 a[176:177], v233 offset:256
v_mfma_f32_16x16x32_bf16 v[164:167], a[152:155], a[196:199], 0
ds_read_b64_tr_b16 a[178:179], v233 offset:4864
s_add_u32 s64, s64, s65
s_add_u32 s69, s69, 36864
s_cmp_eq_u32 s69, 147456
s_cselect_b32 s69, 0, s69
v_mfma_f32_16x16x32_bf16 v[168:171], a[152:155], a[200:203], 0
v_mfma_f32_16x16x32_bf16 v[172:175], a[152:155], a[204:207], 0
s_waitcnt lgkmcnt(10)
v_mfma_f32_16x16x32_bf16 v[176:179], a[156:159], a[192:195], 0
ds_read_b64_tr_b16 a[112:113], v234 offset:27648
v_mfma_f32_16x16x32_bf16 v[180:183], a[156:159], a[196:199], 0
ds_read_b64_tr_b16 a[114:115], v234 offset:32256
s_add_u32 m0, s70, s69
v_mfma_f32_16x16x32_bf16 v[184:187], a[156:159], a[200:203], 0
buffer_load_dwordx4 v224, s[40:43], s64 offen lds
v_mfma_f32_16x16x32_bf16 v[188:191], a[156:159], a[204:207], 0
s_waitcnt lgkmcnt(10)
v_mfma_f32_16x16x32_bf16 v[192:195], a[160:163], a[192:195], 0
ds_read_b64_tr_b16 a[116:117], v234 offset:27656
v_mfma_f32_16x16x32_bf16 v[196:199], a[160:163], a[196:199], 0
ds_read_b64_tr_b16 a[118:119], v234 offset:32264
s_add_u32 m0, m0, 1024
v_mfma_f32_16x16x32_bf16 v[200:203], a[160:163], a[200:203], 0
buffer_load_dwordx4 v225, s[40:43], s64 offen lds
v_mfma_f32_16x16x32_bf16 v[204:207], a[160:163], a[204:207], 0
s_waitcnt lgkmcnt(10)
v_mfma_f32_16x16x32_bf16 v[208:211], a[164:167], a[192:195], 0
ds_read_b64_tr_b16 a[120:121], v234 offset:27712
v_mfma_f32_16x16x32_bf16 v[212:215], a[164:167], a[196:199], 0
ds_read_b64_tr_b16 a[122:123], v234 offset:32320
s_add_u32 m0, m0, 1024
v_mfma_f32_16x16x32_bf16 v[216:219], a[164:167], a[200:203], 0
buffer_load_dwordx4 v226, s[40:43], s64 offen lds
v_mfma_f32_16x16x32_bf16 v[220:223], a[164:167], a[204:207], 0
s_waitcnt lgkmcnt(10)
v_mfma_f32_16x16x32_bf16 a[0:3], a[168:171], a[192:195], 0
ds_read_b64_tr_b16 a[124:125], v234 offset:27720
v_mfma_f32_16x16x32_bf16 a[4:7], a[168:171], a[196:199], 0
ds_read_b64_tr_b16 a[126:127], v234 offset:32328
v_mfma_f32_16x16x32_bf16 a[8:11], a[168:171], a[200:203], 0
buffer_load_dwordx3 a[180:182], v235, s[84:87], s95 offen offset:0
s_add_u32 s95, s95, s94
v_mfma_f32_16x16x32_bf16 a[12:15], a[168:171], a[204:207], 0
s_waitcnt lgkmcnt(10)
v_mfma_f32_16x16x32_bf16 a[16:19], a[172:175], a[192:195], 0
ds_read_b64_tr_b16 a[128:129], v234 offset:27776
v_mfma_f32_16x16x32_bf16 a[20:23], a[172:175], a[196:199], 0
ds_read_b64_tr_b16 a[130:131], v234 offset:32384
s_add_u32 m0, m0, 1024
v_mfma_f32_16x16x32_bf16 a[24:27], a[172:175], a[200:203], 0
buffer_load_dwordx4 v227, s[40:43], s64 offen lds
v_mfma_f32_16x16x32_bf16 a[28:31], a[172:175], a[204:207], 0
s_waitcnt lgkmcnt(10)
v_mfma_f32_16x16x32_bf16 a[32:35], a[176:179], a[192:195], 0
ds_read_b64_tr_b16 a[132:133], v234 offset:27784
v_mfma_f32_16x16x32_bf16 a[36:39], a[176:179], a[196:199], 0
ds_read_b64_tr_b16 a[134:135], v234 offset:32392
v_mfma_f32_16x16x32_bf16 a[40:43], a[176:179], a[200:203], 0
v_mfma_f32_16x16x32_bf16 a[44:47], a[176:179], a[204:207], 0
s_waitcnt vmcnt(19) lgkmcnt(10)
v_mfma_f32_16x16x32_bf16 v[0:3], a[112:115], a[208:211], v[0:3]
ds_read_b64_tr_b16 a[136:137], v234 offset:27840
v_mfma_f32_16x16x32_bf16 v[4:7], a[112:115], a[212:215], v[4:7]
ds_read_b64_tr_b16 a[138:139], v234 offset:32448
s_add_u32 m0, m0, 1024
v_mfma_f32_16x16x32_bf16 v[8:11], a[112:115], a[216:219], v[8:11]
buffer_load_dwordx4 v228, s[40:43], s64 offen lds
v_mfma_f32_16x16x32_bf16 v[12:15], a[112:115], a[220:223], v[12:15]
s_waitcnt lgkmcnt(10)
v_mfma_f32_16x16x32_bf16 v[16:19], a[116:119], a[208:211], v[16:19]
ds_read_b64_tr_b16 a[140:141], v234 offset:27848
v_mfma_f32_16x16x32_bf16 v[20:23], a[116:119], a[212:215], v[20:23]
ds_read_b64_tr_b16 a[142:143], v234 offset:32456
v_mfma_f32_16x16x32_bf16 v[24:27], a[116:119], a[216:219], v[24:27]
v_mfma_f32_16x16x32_bf16 v[28:31], a[116:119], a[220:223], v[28:31]
buffer_load_dwordx4 a[64:67], v235, s[44:47], s71 offen offset:0 sc1
s_waitcnt lgkmcnt(10)
v_mfma_f32_16x16x32_bf16 v[32:35], a[120:123], a[208:211], v[32:35]
ds_read_b64_tr_b16 a[144:145], v233 offset:9216
v_mfma_f32_16x16x32_bf16 v[36:39], a[120:123], a[212:215], v[36:39]
ds_read_b64_tr_b16 a[146:147], v233 offset:13824
s_add_u32 m0, m0, 1024
v_mfma_f32_16x16x32_bf16 v[40:43], a[120:123], a[216:219], v[40:43]
buffer_load_dwordx4 v229, s[40:43], s64 offen lds
v_mfma_f32_16x16x32_bf16 v[44:47], a[120:123], a[220:223], v[44:47]
s_waitcnt lgkmcnt(10)
v_mfma_f32_16x16x32_bf16 v[48:51], a[124:127], a[208:211], v[48:51]
ds_read_b64_tr_b16 a[148:149], v233 offset:9248
v_mfma_f32_16x16x32_bf16 v[52:55], a[124:127], a[212:215], v[52:55]
ds_read_b64_tr_b16 a[150:151], v233 offset:13856
v_mfma_f32_16x16x32_bf16 v[56:59], a[124:127], a[216:219], v[56:59]
v_mfma_f32_16x16x32_bf16 v[60:63], a[124:127], a[220:223], v[60:63]
buffer_load_dwordx4 a[68:71], v235, s[44:47], s71 offen offset:1024 sc1
s_waitcnt lgkmcnt(10)
v_mfma_f32_16x16x32_bf16 v[64:67], a[128:131], a[208:211], v[64:67]
ds_read_b64_tr_b16 a[152:153], v233 offset:9280
v_mfma_f32_16x16x32_bf16 v[68:71], a[128:131], a[212:215], v[68:71]
ds_read_b64_tr_b16 a[154:155], v233 offset:13888
s_add_u32 m0, m0, 1024
v_mfma_f32_16x16x32_bf16 v[72:75], a[128:131], a[216:219], v[72:75]
buffer_load_dwordx4 v230, s[40:43], s64 offen lds
v_mfma_f32_16x16x32_bf16 v[76:79], a[128:131], a[220:223], v[76:79]
s_waitcnt lgkmcnt(10)
v_mfma_f32_16x16x32_bf16 v[80:83], a[132:135], a[208:211], v[80:83]
ds_read_b64_tr_b16 a[156:157], v233 offset:9312
v_mfma_f32_16x16x32_bf16 v[84:87], a[132:135], a[212:215], v[84:87]
ds_read_b64_tr_b16 a[158:159], v233 offset:13920
v_mfma_f32_16x16x32_bf16 v[88:91], a[132:135], a[216:219], v[88:91]
v_mfma_f32_16x16x32_bf16 v[92:95], a[132:135], a[220:223], v[92:95]
buffer_load_dwordx4 a[72:75], v235, s[44:47], s71 offen offset:2048 sc1
s_waitcnt lgkmcnt(10)
v_mfma_f32_16x16x32_bf16 v[96:99], a[136:139], a[208:211], v[96:99]
ds_read_b64_tr_b16 a[160:161], v233 offset:9344
v_mfma_f32_16x16x32_bf16 v[100:103], a[136:139], a[212:215], v[100:103]
ds_read_b64_tr_b16 a[162:163], v233 offset:13952
v_mfma_f32_16x16x32_bf16 v[104:107], a[136:139], a[216:219], v[104:107]
buffer_load_dwordx3 a[180:182], v235, s[84:87], s95 offen offset:0
s_add_u32 s95, s95, s94
v_mfma_f32_16x16x32_bf16 v[108:111], a[136:139], a[220:223], v[108:111]
s_waitcnt lgkmcnt(10)
v_mfma_f32_16x16x32_bf16 v[112:115], a[140:143], a[208:211], v[112:115]
ds_read_b64_tr_b16 a[164:165], v233 offset:9376
v_mfma_f32_16x16x32_bf16 v[116:119], a[140:143], a[212:215], v[116:119]
ds_read_b64_tr_b16 a[166:167], v233 offset:13984
s_add_u32 m0, m0, 1024
v_mfma_f32_16x16x32_bf16 v[120:123], a[140:143], a[216:219], v[120:123]
buffer_load_dwordx4 v231, s[40:43], s64 offen lds
v_mfma_f32_16x16x32_bf16 v[124:127], a[140:143], a[220:223], v[124:127]
buffer_load_dwordx4 a[76:79], v235, s[44:47], s71 offen offset:3072 sc1
s_add_u32 s71, s71, 4096
s_waitcnt lgkmcnt(10)
v_mfma_f32_16x16x32_bf16 v[128:131], a[144:147], a[208:211], v[128:131]
ds_read_b64_tr_b16 a[168:169], v233 offset:9408
v_mfma_f32_16x16x32_bf16 v[132:135], a[144:147], a[212:215], v[132:135]
ds_read_b64_tr_b16 a[170:171], v233 offset:14016
s_add_u32 m0, m0, 1024
v_mfma_f32_16x16x32_bf16 v[136:139], a[144:147], a[216:219], v[136:139]
buffer_load_dwordx4 v232, s[40:43], s64 offen lds
v_mfma_f32_16x16x32_bf16 v[140:143], a[144:147], a[220:223], v[140:143]
s_waitcnt lgkmcnt(10)
v_mfma_f32_16x16x32_bf16 v[144:147], a[148:151], a[208:211], v[144:147]
ds_read_b64_tr_b16 a[172:173], v233 offset:9440
v_mfma_f32_16x16x32_bf16 v[148:151], a[148:151], a[212:215], v[148:151]
ds_read_b64_tr_b16 a[174:175], v233 offset:14048
v_mfma_f32_16x16x32_bf16 v[152:155], a[148:151], a[216:219], v[152:155]
v_mfma_f32_16x16x32_bf16 v[156:159], a[148:151], a[220:223], v[156:159]
s_add_u32 s64, s64, s65
s_add_u32 s69, s69, 36864
s_cmp_eq_u32 s69, 147456
s_cselect_b32 s69, 0, s69
buffer_load_dwordx4 a[80:83], v235, s[44:47], s71 offen offset:0 sc1
s_waitcnt lgkmcnt(10)
v_mfma_f32_16x16x32_bf16 v[160:163], a[152:155], a[208:211], v[160:163]
ds_read_b64_tr_b16 a[176:177], v233 offset:9472
v_mfma_f32_16x16x32_bf16 v[164:167], a[152:155], a[212:215], v[164:167]
ds_read_b64_tr_b16 a[178:179], v233 offset:14080
v_mfma_f32_16x16x32_bf16 v[168:171], a[152:155], a[216:219], v[168:171]
v_mfma_f32_16x16x32_bf16 v[172:175], a[152:155], a[220:223], v[172:175]
s_waitcnt lgkmcnt(10)
v_mfma_f32_16x16x32_bf16 v[176:179], a[156:159], a[208:211], v[176:179]
s_add_u32 s67, s67, 36864
s_cmp_eq_u32 s67, 147456
s_cselect_b32 s66, s76, 36864
s_cselect_b32 s67, 0, s67
v_mfma_f32_16x16x32_bf16 v[180:183], a[156:159], a[212:215], v[180:183]
v_mfma_f32_16x16x32_bf16 v[184:187], a[156:159], a[216:219], v[184:187]
v_mfma_f32_16x16x32_bf16 v[188:191], a[156:159], a[220:223], v[188:191]
buffer_load_dwordx4 a[84:87], v235, s[44:47], s71 offen offset:1024 sc1
s_waitcnt lgkmcnt(8)
v_mfma_f32_16x16x32_bf16 v[192:195], a[160:163], a[208:211], v[192:195]
v_add_u32 v233, s66, v233
v_mfma_f32_16x16x32_bf16 v[196:199], a[160:163], a[212:215], v[196:199]
v_add_u32 v234, s66, v234
v_mfma_f32_16x16x32_bf16 v[200:203], a[160:163], a[216:219], v[200:203]
v_mfma_f32_16x16x32_bf16 v[204:207], a[160:163], a[220:223], v[204:207]
s_waitcnt vmcnt(40) lgkmcnt(0)
s_barrier
v_mfma_f32_16x16x32_bf16 v[208:211], a[164:167], a[208:211], v[208:211]
ds_read_b64_tr_b16 a[112:113], v234 offset:18432
ds_read_b64_tr_b16 a[114:115], v234 offset:23040
v_mfma_f32_16x16x32_bf16 v[212:215], a[164:167], a[212:215], v[212:215]
ds_read_b64_tr_b16 a[116:117], v234 offset:18440
ds_read_b64_tr_b16 a[118:119], v234 offset:23048
v_mfma_f32_16x16x32_bf16 v[216:219], a[164:167], a[216:219], v[216:219]
v_mfma_f32_16x16x32_bf16 v[220:223], a[164:167], a[220:223], v[220:223]
buffer_load_dwordx4 a[88:91], v235, s[44:47], s71 offen offset:2048 sc1
s_waitcnt lgkmcnt(8)
v_mfma_f32_16x16x32_bf16 a[0:3], a[168:171], a[208:211], a[0:3]
ds_read_b64_tr_b16 a[120:121], v234 offset:18496
ds_read_b64_tr_b16 a[122:123], v234 offset:23104
v_mfma_f32_16x16x32_bf16 a[4:7], a[168:171], a[212:215], a[4:7]
ds_read_b64_tr_b16 a[124:125], v234 offset:18504
ds_read_b64_tr_b16 a[126:127], v234 offset:23112
v_mfma_f32_16x16x32_bf16 a[8:11], a[168:171], a[216:219], a[8:11]
v_mfma_f32_16x16x32_bf16 a[12:15], a[168:171], a[220:223], a[12:15]
s_waitcnt lgkmcnt(10)
v_mfma_f32_16x16x32_bf16 a[16:19], a[172:175], a[208:211], a[16:19]
ds_read_b64_tr_b16 a[128:129], v234 offset:18560
v_mfma_f32_16x16x32_bf16 a[20:23], a[172:175], a[212:215], a[20:23]
ds_read_b64_tr_b16 a[130:131], v234 offset:23168
v_mfma_f32_16x16x32_bf16 a[24:27], a[172:175], a[216:219], a[24:27]
v_mfma_f32_16x16x32_bf16 a[28:31], a[172:175], a[220:223], a[28:31]
buffer_load_dwordx4 a[92:95], v235, s[44:47], s71 offen offset:3072 sc1
s_add_u32 s71, s71, 4096
s_waitcnt lgkmcnt(10)
v_mfma_f32_16x16x32_bf16 a[32:35], a[176:179], a[208:211], a[32:35]
ds_read_b64_tr_b16 a[132:133], v234 offset:18568
v_mfma_f32_16x16x32_bf16 a[36:39], a[176:179], a[212:215], a[36:39]
ds_read_b64_tr_b16 a[134:135], v234 offset:23176
v_mfma_f32_16x16x32_bf16 a[40:43], a[176:179], a[216:219], a[40:43]
v_mfma_f32_16x16x32_bf16 a[44:47], a[176:179], a[220:223], a[44:47]
s_waitcnt vmcnt(19) lgkmcnt(10)
v_mfma_f32_16x16x32_bf16 v[0:3], a[112:115], a[48:51], v[0:3]
ds_read_b64_tr_b16 a[136:137], v234 offset:18624
v_mfma_f32_16x16x32_bf16 v[4:7], a[112:115], a[52:55], v[4:7]
ds_read_b64_tr_b16 a[138:139], v234 offset:23232
v_mfma_f32_16x16x32_bf16 v[8:11], a[112:115], a[56:59], v[8:11]
v_mfma_f32_16x16x32_bf16 v[12:15], a[112:115], a[60:63], v[12:15]
s_waitcnt lgkmcnt(10)
v_mfma_f32_16x16x32_bf16 v[16:19], a[116:119], a[48:51], v[16:19]
ds_read_b64_tr_b16 a[140:141], v234 offset:18632
v_mfma_f32_16x16x32_bf16 v[20:23], a[116:119], a[52:55], v[20:23]
ds_read_b64_tr_b16 a[142:143], v234 offset:23240
s_add_u32 m0, s70, s69
v_mfma_f32_16x16x32_bf16 v[24:27], a[116:119], a[56:59], v[24:27]
buffer_load_dwordx4 v224, s[40:43], s64 offen lds
v_mfma_f32_16x16x32_bf16 v[28:31], a[116:119], a[60:63], v[28:31]
buffer_load_dwordx4 a[96:99], v235, s[44:47], s71 offen offset:0 sc1
s_waitcnt lgkmcnt(10)
v_mfma_f32_16x16x32_bf16 v[32:35], a[120:123], a[48:51], v[32:35]
ds_read_b64_tr_b16 a[144:145], v233 offset:0
v_mfma_f32_16x16x32_bf16 v[36:39], a[120:123], a[52:55], v[36:39]
ds_read_b64_tr_b16 a[146:147], v233 offset:4608
v_mfma_f32_16x16x32_bf16 v[40:43], a[120:123], a[56:59], v[40:43]
v_mfma_f32_16x16x32_bf16 v[44:47], a[120:123], a[60:63], v[44:47]
s_waitcnt lgkmcnt(10)
v_mfma_f32_16x16x32_bf16 v[48:51], a[124:127], a[48:51], v[48:51]
ds_read_b64_tr_b16 a[148:149], v233 offset:32
v_mfma_f32_16x16x32_bf16 v[52:55], a[124:127], a[52:55], v[52:55]
ds_read_b64_tr_b16 a[150:151], v233 offset:4640
v_mfma_f32_16x16x32_bf16 v[56:59], a[124:127], a[56:59], v[56:59]
v_mfma_f32_16x16x32_bf16 v[60:63], a[124:127], a[60:63], v[60:63]
buffer_load_dwordx4 a[100:103], v235, s[44:47], s71 offen offset:1024 sc1
s_waitcnt lgkmcnt(10)
v_mfma_f32_16x16x32_bf16 v[64:67], a[128:131], a[48:51], v[64:67]
ds_read_b64_tr_b16 a[152:153], v233 offset:64
v_mfma_f32_16x16x32_bf16 v[68:71], a[128:131], a[52:55], v[68:71]
ds_read_b64_tr_b16 a[154:155], v233 offset:4672
s_add_u32 m0, m0, 1024
v_mfma_f32_16x16x32_bf16 v[72:75], a[128:131], a[56:59], v[72:75]
buffer_load_dwordx4 v225, s[40:43], s64 offen lds
v_mfma_f32_16x16x32_bf16 v[76:79], a[128:131], a[60:63], v[76:79]
s_waitcnt lgkmcnt(10)
v_mfma_f32_16x16x32_bf16 v[80:83], a[132:135], a[48:51], v[80:83]
ds_read_b64_tr_b16 a[156:157], v233 offset:96
v_mfma_f32_16x16x32_bf16 v[84:87], a[132:135], a[52:55], v[84:87]
ds_read_b64_tr_b16 a[158:159], v233 offset:4704
v_mfma_f32_16x16x32_bf16 v[88:91], a[132:135], a[56:59], v[88:91]
buffer_load_dwordx3 a[180:182], v235, s[84:87], s95 offen offset:0
s_add_u32 s95, s95, s94
v_mfma_f32_16x16x32_bf16 v[92:95], a[132:135], a[60:63], v[92:95]
buffer_load_dwordx4 a[104:107], v235, s[44:47], s71 offen offset:2048 sc1
s_waitcnt lgkmcnt(10)
v_mfma_f32_16x16x32_bf16 v[96:99], a[136:139], a[48:51], v[96:99]
ds_read_b64_tr_b16 a[160:161], v233 offset:128
v_mfma_f32_16x16x32_bf16 v[100:103], a[136:139], a[52:55], v[100:103]
ds_read_b64_tr_b16 a[162:163], v233 offset:4736
v_mfma_f32_16x16x32_bf16 v[104:107], a[136:139], a[56:59], v[104:107]
v_mfma_f32_16x16x32_bf16 v[108:111], a[136:139], a[60:63], v[108:111]
s_waitcnt lgkmcnt(10)
v_mfma_f32_16x16x32_bf16 v[112:115], a[140:143], a[48:51], v[112:115]
ds_read_b64_tr_b16 a[164:165], v233 offset:160
v_mfma_f32_16x16x32_bf16 v[116:119], a[140:143], a[52:55], v[116:119]
ds_read_b64_tr_b16 a[166:167], v233 offset:4768
s_add_u32 m0, m0, 1024
v_mfma_f32_16x16x32_bf16 v[120:123], a[140:143], a[56:59], v[120:123]
buffer_load_dwordx4 v226, s[40:43], s64 offen lds
v_mfma_f32_16x16x32_bf16 v[124:127], a[140:143], a[60:63], v[124:127]
buffer_load_dwordx4 a[108:111], v235, s[44:47], s71 offen offset:3072 sc1
s_add_u32 s71, s71, 4096
s_waitcnt lgkmcnt(10)
v_mfma_f32_16x16x32_bf16 v[128:131], a[144:147], a[48:51], v[128:131]
ds_read_b64_tr_b16 a[168:169], v233 offset:192
v_mfma_f32_16x16x32_bf16 v[132:135], a[144:147], a[52:55], v[132:135]
ds_read_b64_tr_b16 a[170:171], v233 offset:4800
v_mfma_f32_16x16x32_bf16 v[136:139], a[144:147], a[56:59], v[136:139]
v_mfma_f32_16x16x32_bf16 v[140:143], a[144:147], a[60:63], v[140:143]
s_waitcnt lgkmcnt(10)
v_mfma_f32_16x16x32_bf16 v[144:147], a[148:151], a[48:51], v[144:147]
ds_read_b64_tr_b16 a[172:173], v233 offset:224
v_mfma_f32_16x16x32_bf16 v[148:151], a[148:151], a[52:55], v[148:151]
ds_read_b64_tr_b16 a[174:175], v233 offset:4832
v_mfma_f32_16x16x32_bf16 v[152:155], a[148:151], a[56:59], v[152:155]
v_mfma_f32_16x16x32_bf16 v[156:159], a[148:151], a[60:63], v[156:159]
buffer_load_dwordx4 a[192:195], v235, s[44:47], s71 offen offset:0 sc1
s_waitcnt lgkmcnt(10)
v_mfma_f32_16x16x32_bf16 v[160:163], a[152:155], a[48:51], v[160:163]
ds_read_b64_tr_b16 a[176:177], v233 offset:256
v_mfma_f32_16x16x32_bf16 v[164:167], a[152:155], a[52:55], v[164:167]
ds_read_b64_tr_b16 a[178:179], v233 offset:4864
s_add_u32 m0, m0, 1024
v_mfma_f32_16x16x32_bf16 v[168:171], a[152:155], a[56:59], v[168:171]
buffer_load_dwordx4 v227, s[40:43], s64 offen lds
v_mfma_f32_16x16x32_bf16 v[172:175], a[152:155], a[60:63], v[172:175]
s_waitcnt lgkmcnt(10)
v_mfma_f32_16x16x32_bf16 v[176:179], a[156:159], a[48:51], v[176:179]
ds_read_b64_tr_b16 a[112:113], v234 offset:27648
v_mfma_f32_16x16x32_bf16 v[180:183], a[156:159], a[52:55], v[180:183]
ds_read_b64_tr_b16 a[114:115], v234 offset:32256
v_mfma_f32_16x16x32_bf16 v[184:187], a[156:159], a[56:59], v[184:187]
v_mfma_f32_16x16x32_bf16 v[188:191], a[156:159], a[60:63], v[188:191]
buffer_load_dwordx4 a[196:199], v235, s[44:47], s71 offen offset:1024 sc1
s_waitcnt lgkmcnt(10)
v_mfma_f32_16x16x32_bf16 v[192:195], a[160:163], a[48:51], v[192:195]
ds_read_b64_tr_b16 a[116:117], v234 offset:27656
v_mfma_f32_16x16x32_bf16 v[196:199], a[160:163], a[52:55], v[196:199]
ds_read_b64_tr_b16 a[118:119], v234 offset:32264
v_mfma_f32_16x16x32_bf16 v[200:203], a[160:163], a[56:59], v[200:203]
v_mfma_f32_16x16x32_bf16 v[204:207], a[160:163], a[60:63], v[204:207]
s_waitcnt lgkmcnt(10)
v_mfma_f32_16x16x32_bf16 v[208:211], a[164:167], a[48:51], v[208:211]
ds_read_b64_tr_b16 a[120:121], v234 offset:27712
v_mfma_f32_16x16x32_bf16 v[212:215], a[164:167], a[52:55], v[212:215]
ds_read_b64_tr_b16 a[122:123], v234 offset:32320
s_add_u32 m0, m0, 1024
v_mfma_f32_16x16x32_bf16 v[216:219], a[164:167], a[56:59], v[216:219]
buffer_load_dwordx4 v228, s[40:43], s64 offen lds
v_mfma_f32_16x16x32_bf16 v[220:223], a[164:167], a[60:63], v[220:223]
buffer_load_dwordx4 a[200:203], v235, s[44:47], s71 offen offset:2048 sc1
s_waitcnt lgkmcnt(10)
v_mfma_f32_16x16x32_bf16 a[0:3], a[168:171], a[48:51], a[0:3]
ds_read_b64_tr_b16 a[124:125], v234 offset:27720
v_mfma_f32_16x16x32_bf16 a[4:7], a[168:171], a[52:55], a[4:7]
ds_read_b64_tr_b16 a[126:127], v234 offset:32328
v_mfma_f32_16x16x32_bf16 a[8:11], a[168:171], a[56:59], a[8:11]
buffer_load_dwordx3 a[180:182], v235, s[84:87], s95 offen offset:0
)"
R"(
s_add_u32 s95, s95, s94
v_mfma_f32_16x16x32_bf16 a[12:15], a[168:171], a[60:63], a[12:15]
s_waitcnt lgkmcnt(10)
v_mfma_f32_16x16x32_bf16 a[16:19], a[172:175], a[48:51], a[16:19]
ds_read_b64_tr_b16 a[128:129], v234 offset:27776
v_mfma_f32_16x16x32_bf16 a[20:23], a[172:175], a[52:55], a[20:23]
ds_read_b64_tr_b16 a[130:131], v234 offset:32384
v_mfma_f32_16x16x32_bf16 a[24:27], a[172:175], a[56:59], a[24:27]
v_mfma_f32_16x16x32_bf16 a[28:31], a[172:175], a[60:63], a[28:31]
buffer_load_dwordx4 a[204:207], v235, s[44:47], s71 offen offset:3072 sc1
s_add_u32 s71, s71, 4096
s_waitcnt lgkmcnt(10)
v_mfma_f32_16x16x32_bf16 a[32:35], a[176:179], a[48:51], a[32:35]
ds_read_b64_tr_b16 a[132:133], v234 offset:27784
v_mfma_f32_16x16x32_bf16 a[36:39], a[176:179], a[52:55], a[36:39]
ds_read_b64_tr_b16 a[134:135], v234 offset:32392
s_add_u32 m0, m0, 1024
v_mfma_f32_16x16x32_bf16 a[40:43], a[176:179], a[56:59], a[40:43]
buffer_load_dwordx4 v229, s[40:43], s64 offen lds
v_mfma_f32_16x16x32_bf16 a[44:47], a[176:179], a[60:63], a[44:47]
s_waitcnt vmcnt(21) lgkmcnt(10)
v_mfma_f32_16x16x32_bf16 v[0:3], a[112:115], a[64:67], v[0:3]
ds_read_b64_tr_b16 a[136:137], v234 offset:27840
v_mfma_f32_16x16x32_bf16 v[4:7], a[112:115], a[68:71], v[4:7]
ds_read_b64_tr_b16 a[138:139], v234 offset:32448
v_mfma_f32_16x16x32_bf16 v[8:11], a[112:115], a[72:75], v[8:11]
v_mfma_f32_16x16x32_bf16 v[12:15], a[112:115], a[76:79], v[12:15]
buffer_load_dwordx4 a[208:211], v235, s[44:47], s71 offen offset:0 sc1
s_waitcnt lgkmcnt(10)
v_mfma_f32_16x16x32_bf16 v[16:19], a[116:119], a[64:67], v[16:19]
ds_read_b64_tr_b16 a[140:141], v234 offset:27848
v_mfma_f32_16x16x32_bf16 v[20:23], a[116:119], a[68:71], v[20:23]
ds_read_b64_tr_b16 a[142:143], v234 offset:32456
v_mfma_f32_16x16x32_bf16 v[24:27], a[116:119], a[72:75], v[24:27]
v_mfma_f32_16x16x32_bf16 v[28:31], a[116:119], a[76:79], v[28:31]
s_waitcnt lgkmcnt(10)
v_mfma_f32_16x16x32_bf16 v[32:35], a[120:123], a[64:67], v[32:35]
ds_read_b64_tr_b16 a[144:145], v233 offset:9216
v_mfma_f32_16x16x32_bf16 v[36:39], a[120:123], a[68:71], v[36:39]
ds_read_b64_tr_b16 a[146:147], v233 offset:13824
s_add_u32 m0, m0, 1024
v_mfma_f32_16x16x32_bf16 v[40:43], a[120:123], a[72:75], v[40:43]
buffer_load_dwordx4 v230, s[40:43], s64 offen lds
v_mfma_f32_16x16x32_bf16 v[44:47], a[120:123], a[76:79], v[44:47]
s_waitcnt lgkmcnt(10)
v_mfma_f32_16x16x32_bf16 v[48:51], a[124:127], a[64:67], v[48:51]
ds_read_b64_tr_b16 a[148:149], v233 offset:9248
v_mfma_f32_16x16x32_bf16 v[52:55], a[124:127], a[68:71], v[52:55]
ds_read_b64_tr_b16 a[150:151], v233 offset:13856
v_mfma_f32_16x16x32_bf16 v[56:59], a[124:127], a[72:75], v[56:59]
v_mfma_f32_16x16x32_bf16 v[60:63], a[124:127], a[76:79], v[60:63]
buffer_load_dwordx4 a[212:215], v235, s[44:47], s71 offen offset:1024 sc1
s_waitcnt lgkmcnt(10)
v_mfma_f32_16x16x32_bf16 v[64:67], a[128:131], a[64:67], v[64:67]
ds_read_b64_tr_b16 a[152:153], v233 offset:9280
v_mfma_f32_16x16x32_bf16 v[68:71], a[128:131], a[68:71], v[68:71]
ds_read_b64_tr_b16 a[154:155], v233 offset:13888
v_mfma_f32_16x16x32_bf16 v[72:75], a[128:131], a[72:75], v[72:75]
v_mfma_f32_16x16x32_bf16 v[76:79], a[128:131], a[76:79], v[76:79]
s_waitcnt lgkmcnt(10)
v_mfma_f32_16x16x32_bf16 v[80:83], a[132:135], a[64:67], v[80:83]
ds_read_b64_tr_b16 a[156:157], v233 offset:9312
v_mfma_f32_16x16x32_bf16 v[84:87], a[132:135], a[68:71], v[84:87]
ds_read_b64_tr_b16 a[158:159], v233 offset:13920
s_add_u32 m0, m0, 1024
v_mfma_f32_16x16x32_bf16 v[88:91], a[132:135], a[72:75], v[88:91]
buffer_load_dwordx4 v231, s[40:43], s64 offen lds
v_mfma_f32_16x16x32_bf16 v[92:95], a[132:135], a[76:79], v[92:95]
s_waitcnt lgkmcnt(10)
v_mfma_f32_16x16x32_bf16 v[96:99], a[136:139], a[64:67], v[96:99]
ds_read_b64_tr_b16 a[160:161], v233 offset:9344
v_mfma_f32_16x16x32_bf16 v[100:103], a[136:139], a[68:71], v[100:103]
ds_read_b64_tr_b16 a[162:163], v233 offset:13952
v_mfma_f32_16x16x32_bf16 v[104:107], a[136:139], a[72:75], v[104:107]
buffer_load_dwordx3 a[180:182], v235, s[84:87], s95 offen offset:0
s_add_u32 s95, s95, s94
v_mfma_f32_16x16x32_bf16 v[108:111], a[136:139], a[76:79], v[108:111]
buffer_load_dwordx4 a[216:219], v235, s[44:47], s71 offen offset:2048 sc1
s_waitcnt lgkmcnt(10)
v_mfma_f32_16x16x32_bf16 v[112:115], a[140:143], a[64:67], v[112:115]
ds_read_b64_tr_b16 a[164:165], v233 offset:9376
v_mfma_f32_16x16x32_bf16 v[116:119], a[140:143], a[68:71], v[116:119]
ds_read_b64_tr_b16 a[166:167], v233 offset:13984
v_mfma_f32_16x16x32_bf16 v[120:123], a[140:143], a[72:75], v[120:123]
v_mfma_f32_16x16x32_bf16 v[124:127], a[140:143], a[76:79], v[124:127]
s_waitcnt lgkmcnt(10)
v_mfma_f32_16x16x32_bf16 v[128:131], a[144:147], a[64:67], v[128:131]
ds_read_b64_tr_b16 a[168:169], v233 offset:9408
v_mfma_f32_16x16x32_bf16 v[132:135], a[144:147], a[68:71], v[132:135]
ds_read_b64_tr_b16 a[170:171], v233 offset:14016
s_add_u32 m0, m0, 1024
v_mfma_f32_16x16x32_bf16 v[136:139], a[144:147], a[72:75], v[136:139]
buffer_load_dwordx4 v232, s[40:43], s64 offen lds
v_mfma_f32_16x16x32_bf16 v[140:143], a[144:147], a[76:79], v[140:143]
s_waitcnt lgkmcnt(10)
v_mfma_f32_16x16x32_bf16 v[144:147], a[148:151], a[64:67], v[144:147]
ds_read_b64_tr_b16 a[172:173], v233 offset:9440
v_mfma_f32_16x16x32_bf16 v[148:151], a[148:151], a[68:71], v[148:151]
ds_read_b64_tr_b16 a[174:175], v233 offset:14048
v_mfma_f32_16x16x32_bf16 v[152:155], a[148:151], a[72:75], v[152:155]
v_mfma_f32_16x16x32_bf16 v[156:159], a[148:151], a[76:79], v[156:159]
s_add_u32 s64, s64, s65
s_add_u32 s69, s69, 36864
s_cmp_eq_u32 s69, 147456
s_cselect_b32 s69, 0, s69
buffer_load_dwordx4 a[220:223], v235, s[44:47], s71 offen offset:3072 sc1
s_add_u32 s71, s71, 4096
s_waitcnt lgkmcnt(10)
v_mfma_f32_16x16x32_bf16 v[160:163], a[152:155], a[64:67], v[160:163]
ds_read_b64_tr_b16 a[176:177], v233 offset:9472
v_mfma_f32_16x16x32_bf16 v[164:167], a[152:155], a[68:71], v[164:167]
ds_read_b64_tr_b16 a[178:179], v233 offset:14080
v_mfma_f32_16x16x32_bf16 v[168:171], a[152:155], a[72:75], v[168:171]
v_mfma_f32_16x16x32_bf16 v[172:175], a[152:155], a[76:79], v[172:175]
s_waitcnt lgkmcnt(10)
v_mfma_f32_16x16x32_bf16 v[176:179], a[156:159], a[64:67], v[176:179]
s_add_u32 s67, s67, 36864
s_cmp_eq_u32 s67, 147456
s_cselect_b32 s66, s76, 36864
s_cselect_b32 s67, 0, s67
v_mfma_f32_16x16x32_bf16 v[180:183], a[156:159], a[68:71], v[180:183]
v_mfma_f32_16x16x32_bf16 v[184:187], a[156:159], a[72:75], v[184:187]
v_mfma_f32_16x16x32_bf16 v[188:191], a[156:159], a[76:79], v[188:191]
s_waitcnt lgkmcnt(8)
v_mfma_f32_16x16x32_bf16 v[192:195], a[160:163], a[64:67], v[192:195]
v_add_u32 v233, s66, v233
v_mfma_f32_16x16x32_bf16 v[196:199], a[160:163], a[68:71], v[196:199]
v_add_u32 v234, s66, v234
v_mfma_f32_16x16x32_bf16 v[200:203], a[160:163], a[72:75], v[200:203]
v_mfma_f32_16x16x32_bf16 v[204:207], a[160:163], a[76:79], v[204:207]
s_waitcnt vmcnt(44) lgkmcnt(0)
s_barrier
v_mfma_f32_16x16x32_bf16 v[208:211], a[164:167], a[64:67], v[208:211]
ds_read_b64_tr_b16 a[112:113], v234 offset:18432
ds_read_b64_tr_b16 a[114:115], v234 offset:23040
v_mfma_f32_16x16x32_bf16 v[212:215], a[164:167], a[68:71], v[212:215]
ds_read_b64_tr_b16 a[116:117], v234 offset:18440
ds_read_b64_tr_b16 a[118:119], v234 offset:23048
v_mfma_f32_16x16x32_bf16 v[216:219], a[164:167], a[72:75], v[216:219]
v_mfma_f32_16x16x32_bf16 v[220:223], a[164:167], a[76:79], v[220:223]
s_waitcnt lgkmcnt(8)
v_mfma_f32_16x16x32_bf16 a[0:3], a[168:171], a[64:67], a[0:3]
ds_read_b64_tr_b16 a[120:121], v234 offset:18496
ds_read_b64_tr_b16 a[122:123], v234 offset:23104
v_mfma_f32_16x16x32_bf16 a[4:7], a[168:171], a[68:71], a[4:7]
ds_read_b64_tr_b16 a[124:125], v234 offset:18504
ds_read_b64_tr_b16 a[126:127], v234 offset:23112
v_mfma_f32_16x16x32_bf16 a[8:11], a[168:171], a[72:75], a[8:11]
v_mfma_f32_16x16x32_bf16 a[12:15], a[168:171], a[76:79], a[12:15]
s_waitcnt lgkmcnt(10)
v_mfma_f32_16x16x32_bf16 a[16:19], a[172:175], a[64:67], a[16:19]
ds_read_b64_tr_b16 a[128:129], v234 offset:18560
v_mfma_f32_16x16x32_bf16 a[20:23], a[172:175], a[68:71], a[20:23]
ds_read_b64_tr_b16 a[130:131], v234 offset:23168
v_mfma_f32_16x16x32_bf16 a[24:27], a[172:175], a[72:75], a[24:27]
v_mfma_f32_16x16x32_bf16 a[28:31], a[172:175], a[76:79], a[28:31]
s_waitcnt lgkmcnt(10)
v_mfma_f32_16x16x32_bf16 a[32:35], a[176:179], a[64:67], a[32:35]
ds_read_b64_tr_b16 a[132:133], v234 offset:18568
v_mfma_f32_16x16x32_bf16 a[36:39], a[176:179], a[68:71], a[36:39]
ds_read_b64_tr_b16 a[134:135], v234 offset:23176
v_mfma_f32_16x16x32_bf16 a[40:43], a[176:179], a[72:75], a[40:43]
v_mfma_f32_16x16x32_bf16 a[44:47], a[176:179], a[76:79], a[44:47]
s_waitcnt vmcnt(24) lgkmcnt(10)
v_mfma_f32_16x16x32_bf16 v[0:3], a[112:115], a[80:83], v[0:3]
ds_read_b64_tr_b16 a[136:137], v234 offset:18624
v_mfma_f32_16x16x32_bf16 v[4:7], a[112:115], a[84:87], v[4:7]
ds_read_b64_tr_b16 a[138:139], v234 offset:23232
v_mfma_f32_16x16x32_bf16 v[8:11], a[112:115], a[88:91], v[8:11]
v_mfma_f32_16x16x32_bf16 v[12:15], a[112:115], a[92:95], v[12:15]
s_waitcnt lgkmcnt(10)
v_mfma_f32_16x16x32_bf16 v[16:19], a[116:119], a[80:83], v[16:19]
ds_read_b64_tr_b16 a[140:141], v234 offset:18632
v_mfma_f32_16x16x32_bf16 v[20:23], a[116:119], a[84:87], v[20:23]
ds_read_b64_tr_b16 a[142:143], v234 offset:23240
s_add_u32 m0, s70, s69
v_mfma_f32_16x16x32_bf16 v[24:27], a[116:119], a[88:91], v[24:27]
buffer_load_dwordx4 v224, s[40:43], s64 offen lds
v_mfma_f32_16x16x32_bf16 v[28:31], a[116:119], a[92:95], v[28:31]
buffer_load_dwordx4 a[48:51], v235, s[44:47], s71 offen offset:0 sc1
s_waitcnt lgkmcnt(10)
v_mfma_f32_16x16x32_bf16 v[32:35], a[120:123], a[80:83], v[32:35]
ds_read_b64_tr_b16 a[144:145], v233 offset:0
v_mfma_f32_16x16x32_bf16 v[36:39], a[120:123], a[84:87], v[36:39]
ds_read_b64_tr_b16 a[146:147], v233 offset:4608
v_mfma_f32_16x16x32_bf16 v[40:43], a[120:123], a[88:91], v[40:43]
s_mov_b64 s[56:57], s[28:29]
s_and_b32 s57, s57, 65535
s_mov_b32 s58, 131072
s_lshl_b32 s80, s7, 1
s_and_b32 s77, s35, 1
s_add_u32 s80, s80, s77
s_lshl_b32 s80, s80, 1
s_and_b32 s77, %[wid], 1
s_add_u32 s80, s80, s77
s_lshl_b32 s80, s80, 12
s_mov_b64 s[84:85], s[30:31]
s_mul_i32 s77, s35, 288
s_sub_u32 s84, s84, s77
s_subb_u32 s85, s85, 0
s_and_b32 s85, s85, 65535
s_lshl_b32 s77, s7, 2
s_add_u32 s77, s77, s35
s_lshl_b32 s77, s77, 2
s_add_u32 s77, s77, %[wid]
s_mul_i32 s78, s93, 288
s_mul_i32 s95, s77, s78
s_add_u32 s94, s95, s78
v_mfma_f32_16x16x32_bf16 v[44:47], a[120:123], a[92:95], v[44:47]
s_waitcnt lgkmcnt(10)
v_mfma_f32_16x16x32_bf16 v[48:51], a[124:127], a[80:83], v[48:51]
ds_read_b64_tr_b16 a[148:149], v233 offset:32
v_mfma_f32_16x16x32_bf16 v[52:55], a[124:127], a[84:87], v[52:55]
ds_read_b64_tr_b16 a[150:151], v233 offset:4640
v_mfma_f32_16x16x32_bf16 v[56:59], a[124:127], a[88:91], v[56:59]
v_mfma_f32_16x16x32_bf16 v[60:63], a[124:127], a[92:95], v[60:63]
buffer_load_dwordx4 a[52:55], v235, s[44:47], s71 offen offset:1024 sc1
s_waitcnt lgkmcnt(10)
v_mfma_f32_16x16x32_bf16 v[64:67], a[128:131], a[80:83], v[64:67]
ds_read_b64_tr_b16 a[152:153], v233 offset:64
v_mfma_f32_16x16x32_bf16 v[68:71], a[128:131], a[84:87], v[68:71]
ds_read_b64_tr_b16 a[154:155], v233 offset:4672
s_add_u32 m0, m0, 1024
v_mfma_f32_16x16x32_bf16 v[72:75], a[128:131], a[88:91], v[72:75]
buffer_load_dwordx4 v225, s[40:43], s64 offen lds
v_mfma_f32_16x16x32_bf16 v[76:79], a[128:131], a[92:95], v[76:79]
s_waitcnt lgkmcnt(10)
v_mfma_f32_16x16x32_bf16 v[80:83], a[132:135], a[80:83], v[80:83]
ds_read_b64_tr_b16 a[156:157], v233 offset:96
v_mfma_f32_16x16x32_bf16 v[84:87], a[132:135], a[84:87], v[84:87]
ds_read_b64_tr_b16 a[158:159], v233 offset:4704
v_mfma_f32_16x16x32_bf16 v[88:91], a[132:135], a[88:91], v[88:91]
v_mfma_f32_16x16x32_bf16 v[92:95], a[132:135], a[92:95], v[92:95]
buffer_load_dwordx4 a[56:59], v235, s[44:47], s71 offen offset:2048 sc1
s_waitcnt lgkmcnt(10)
v_mfma_f32_16x16x32_bf16 v[96:99], a[136:139], a[80:83], v[96:99]
ds_read_b64_tr_b16 a[160:161], v233 offset:128
v_mfma_f32_16x16x32_bf16 v[100:103], a[136:139], a[84:87], v[100:103]
ds_read_b64_tr_b16 a[162:163], v233 offset:4736
v_mfma_f32_16x16x32_bf16 v[104:107], a[136:139], a[88:91], v[104:107]
v_mfma_f32_16x16x32_bf16 v[108:111], a[136:139], a[92:95], v[108:111]
s_waitcnt lgkmcnt(10)
v_mfma_f32_16x16x32_bf16 v[112:115], a[140:143], a[80:83], v[112:115]
ds_read_b64_tr_b16 a[164:165], v233 offset:160
v_mfma_f32_16x16x32_bf16 v[116:119], a[140:143], a[84:87], v[116:119]
ds_read_b64_tr_b16 a[166:167], v233 offset:4768
s_add_u32 m0, m0, 1024
v_mfma_f32_16x16x32_bf16 v[120:123], a[140:143], a[88:91], v[120:123]
buffer_load_dwordx4 v226, s[40:43], s64 offen lds
v_mfma_f32_16x16x32_bf16 v[124:127], a[140:143], a[92:95], v[124:127]
buffer_load_dwordx4 a[60:63], v235, s[44:47], s71 offen offset:3072 sc1
s_add_u32 s71, s71, 4096
s_waitcnt lgkmcnt(10)
v_mfma_f32_16x16x32_bf16 v[128:131], a[144:147], a[80:83], v[128:131]
ds_read_b64_tr_b16 a[168:169], v233 offset:192
v_mfma_f32_16x16x32_bf16 v[132:135], a[144:147], a[84:87], v[132:135]
ds_read_b64_tr_b16 a[170:171], v233 offset:4800
v_mfma_f32_16x16x32_bf16 v[136:139], a[144:147], a[88:91], v[136:139]
v_mfma_f32_16x16x32_bf16 v[140:143], a[144:147], a[92:95], v[140:143]
s_waitcnt lgkmcnt(10)
v_mfma_f32_16x16x32_bf16 v[144:147], a[148:151], a[80:83], v[144:147]
ds_read_b64_tr_b16 a[172:173], v233 offset:224
v_mfma_f32_16x16x32_bf16 v[148:151], a[148:151], a[84:87], v[148:151]
ds_read_b64_tr_b16 a[174:175], v233 offset:4832
v_mfma_f32_16x16x32_bf16 v[152:155], a[148:151], a[88:91], v[152:155]
v_mfma_f32_16x16x32_bf16 v[156:159], a[148:151], a[92:95], v[156:159]
buffer_load_dwordx4 a[64:67], v235, s[44:47], s71 offen offset:0 sc1
s_waitcnt lgkmcnt(10)
v_mfma_f32_16x16x32_bf16 v[160:163], a[152:155], a[80:83], v[160:163]
ds_read_b64_tr_b16 a[176:177], v233 offset:256
v_mfma_f32_16x16x32_bf16 v[164:167], a[152:155], a[84:87], v[164:167]
ds_read_b64_tr_b16 a[178:179], v233 offset:4864
s_add_u32 m0, m0, 1024
v_mfma_f32_16x16x32_bf16 v[168:171], a[152:155], a[88:91], v[168:171]
buffer_load_dwordx4 v227, s[40:43], s64 offen lds
v_mfma_f32_16x16x32_bf16 v[172:175], a[152:155], a[92:95], v[172:175]
s_waitcnt lgkmcnt(10)
v_mfma_f32_16x16x32_bf16 v[176:179], a[156:159], a[80:83], v[176:179]
ds_read_b64_tr_b16 a[112:113], v234 offset:27648
v_mfma_f32_16x16x32_bf16 v[180:183], a[156:159], a[84:87], v[180:183]
ds_read_b64_tr_b16 a[114:115], v234 offset:32256
v_mfma_f32_16x16x32_bf16 v[184:187], a[156:159], a[88:91], v[184:187]
v_mfma_f32_16x16x32_bf16 v[188:191], a[156:159], a[92:95], v[188:191]
buffer_load_dwordx4 a[68:71], v235, s[44:47], s71 offen offset:1024 sc1
s_waitcnt lgkmcnt(10)
v_mfma_f32_16x16x32_bf16 v[192:195], a[160:163], a[80:83], v[192:195]
ds_read_b64_tr_b16 a[116:117], v234 offset:27656
v_mfma_f32_16x16x32_bf16 v[196:199], a[160:163], a[84:87], v[196:199]
ds_read_b64_tr_b16 a[118:119], v234 offset:32264
v_mfma_f32_16x16x32_bf16 v[200:203], a[160:163], a[88:91], v[200:203]
v_mfma_f32_16x16x32_bf16 v[204:207], a[160:163], a[92:95], v[204:207]
s_waitcnt lgkmcnt(10)
v_mfma_f32_16x16x32_bf16 v[208:211], a[164:167], a[80:83], v[208:211]
ds_read_b64_tr_b16 a[120:121], v234 offset:27712
v_mfma_f32_16x16x32_bf16 v[212:215], a[164:167], a[84:87], v[212:215]
ds_read_b64_tr_b16 a[122:123], v234 offset:32320
s_add_u32 m0, m0, 1024
v_mfma_f32_16x16x32_bf16 v[216:219], a[164:167], a[88:91], v[216:219]
buffer_load_dwordx4 v228, s[40:43], s64 offen lds
v_mfma_f32_16x16x32_bf16 v[220:223], a[164:167], a[92:95], v[220:223]
buffer_load_dwordx4 a[72:75], v235, s[44:47], s71 offen offset:2048 sc1
s_waitcnt lgkmcnt(10)
v_mfma_f32_16x16x32_bf16 a[0:3], a[168:171], a[80:83], a[0:3]
ds_read_b64_tr_b16 a[124:125], v234 offset:27720
v_mfma_f32_16x16x32_bf16 a[4:7], a[168:171], a[84:87], a[4:7]
ds_read_b64_tr_b16 a[126:127], v234 offset:32328
v_mfma_f32_16x16x32_bf16 a[8:11], a[168:171], a[88:91], a[8:11]
v_mfma_f32_16x16x32_bf16 a[12:15], a[168:171], a[92:95], a[12:15]
s_waitcnt lgkmcnt(10)
v_mfma_f32_16x16x32_bf16 a[16:19], a[172:175], a[80:83], a[16:19]
ds_read_b64_tr_b16 a[128:129], v234 offset:27776
v_mfma_f32_16x16x32_bf16 a[20:23], a[172:175], a[84:87], a[20:23]
ds_read_b64_tr_b16 a[130:131], v234 offset:32384
v_mfma_f32_16x16x32_bf16 a[24:27], a[172:175], a[88:91], a[24:27]
v_mfma_f32_16x16x32_bf16 a[28:31], a[172:175], a[92:95], a[28:31]
buffer_load_dwordx4 a[76:79], v235, s[44:47], s71 offen offset:3072 sc1
s_add_u32 s71, s71, 4096
s_waitcnt lgkmcnt(10)
v_mfma_f32_16x16x32_bf16 a[32:35], a[176:179], a[80:83], a[32:35]
ds_read_b64_tr_b16 a[132:133], v234 offset:27784
v_mfma_f32_16x16x32_bf16 a[36:39], a[176:179], a[84:87], a[36:39]
ds_read_b64_tr_b16 a[134:135], v234 offset:32392
s_add_u32 m0, m0, 1024
v_mfma_f32_16x16x32_bf16 a[40:43], a[176:179], a[88:91], a[40:43]
buffer_load_dwordx4 v229, s[40:43], s64 offen lds
v_mfma_f32_16x16x32_bf16 a[44:47], a[176:179], a[92:95], a[44:47]
s_waitcnt vmcnt(30) lgkmcnt(10)
v_mfma_f32_16x16x32_bf16 v[0:3], a[112:115], a[96:99], v[0:3]
ds_read_b64_tr_b16 a[136:137], v234 offset:27840
v_mfma_f32_16x16x32_bf16 v[4:7], a[112:115], a[100:103], v[4:7]
ds_read_b64_tr_b16 a[138:139], v234 offset:32448
v_mfma_f32_16x16x32_bf16 v[8:11], a[112:115], a[104:107], v[8:11]
v_mfma_f32_16x16x32_bf16 v[12:15], a[112:115], a[108:111], v[12:15]
buffer_load_dwordx4 a[80:83], v235, s[44:47], s71 offen offset:0 sc1
s_waitcnt lgkmcnt(10)
v_mfma_f32_16x16x32_bf16 v[16:19], a[116:119], a[96:99], v[16:19]
ds_read_b64_tr_b16 a[140:141], v234 offset:27848
v_mfma_f32_16x16x32_bf16 v[20:23], a[116:119], a[100:103], v[20:23]
ds_read_b64_tr_b16 a[142:143], v234 offset:32456
v_mfma_f32_16x16x32_bf16 v[24:27], a[116:119], a[104:107], v[24:27]
v_mfma_f32_16x16x32_bf16 v[28:31], a[116:119], a[108:111], v[28:31]
s_waitcnt lgkmcnt(10)
v_mfma_f32_16x16x32_bf16 v[32:35], a[120:123], a[96:99], v[32:35]
ds_read_b64_tr_b16 a[144:145], v233 offset:9216
v_mfma_f32_16x16x32_bf16 v[36:39], a[120:123], a[100:103], v[36:39]
ds_read_b64_tr_b16 a[146:147], v233 offset:13824
s_add_u32 m0, m0, 1024
v_mfma_f32_16x16x32_bf16 v[40:43], a[120:123], a[104:107], v[40:43]
buffer_load_dwordx4 v230, s[40:43], s64 offen lds
v_mfma_f32_16x16x32_bf16 v[44:47], a[120:123], a[108:111], v[44:47]
s_waitcnt lgkmcnt(10)
v_mfma_f32_16x16x32_bf16 v[48:51], a[124:127], a[96:99], v[48:51]
ds_read_b64_tr_b16 a[148:149], v233 offset:9248
v_mfma_f32_16x16x32_bf16 v[52:55], a[124:127], a[100:103], v[52:55]
ds_read_b64_tr_b16 a[150:151], v233 offset:13856
v_mfma_f32_16x16x32_bf16 v[56:59], a[124:127], a[104:107], v[56:59]
v_mfma_f32_16x16x32_bf16 v[60:63], a[124:127], a[108:111], v[60:63]
buffer_load_dwordx4 a[84:87], v235, s[44:47], s71 offen offset:1024 sc1
s_waitcnt lgkmcnt(10)
v_mfma_f32_16x16x32_bf16 v[64:67], a[128:131], a[96:99], v[64:67]
ds_read_b64_tr_b16 a[152:153], v233 offset:9280
v_mfma_f32_16x16x32_bf16 v[68:71], a[128:131], a[100:103], v[68:71]
ds_read_b64_tr_b16 a[154:155], v233 offset:13888
v_mfma_f32_16x16x32_bf16 v[72:75], a[128:131], a[104:107], v[72:75]
v_mfma_f32_16x16x32_bf16 v[76:79], a[128:131], a[108:111], v[76:79]
s_waitcnt lgkmcnt(10)
v_mfma_f32_16x16x32_bf16 v[80:83], a[132:135], a[96:99], v[80:83]
ds_read_b64_tr_b16 a[156:157], v233 offset:9312
v_mfma_f32_16x16x32_bf16 v[84:87], a[132:135], a[100:103], v[84:87]
ds_read_b64_tr_b16 a[158:159], v233 offset:13920
s_add_u32 m0, m0, 1024
v_mfma_f32_16x16x32_bf16 v[88:91], a[132:135], a[104:107], v[88:91]
buffer_load_dwordx4 v231, s[40:43], s64 offen lds
v_mfma_f32_16x16x32_bf16 v[92:95], a[132:135], a[108:111], v[92:95]
s_waitcnt lgkmcnt(10)
v_mfma_f32_16x16x32_bf16 v[96:99], a[136:139], a[96:99], v[96:99]
ds_read_b64_tr_b16 a[160:161], v233 offset:9344
v_mfma_f32_16x16x32_bf16 v[100:103], a[136:139], a[100:103], v[100:103]
ds_read_b64_tr_b16 a[162:163], v233 offset:13952
v_mfma_f32_16x16x32_bf16 v[104:107], a[136:139], a[104:107], v[104:107]
v_mfma_f32_16x16x32_bf16 v[108:111], a[136:139], a[108:111], v[108:111]
buffer_load_dwordx4 a[88:91], v235, s[44:47], s71 offen offset:2048 sc1
s_waitcnt lgkmcnt(10)
v_mfma_f32_16x16x32_bf16 v[112:115], a[140:143], a[96:99], v[112:115]
ds_read_b64_tr_b16 a[164:165], v233 offset:9376
v_mfma_f32_16x16x32_bf16 v[116:119], a[140:143], a[100:103], v[116:119]
ds_read_b64_tr_b16 a[166:167], v233 offset:13984
v_mfma_f32_16x16x32_bf16 v[120:123], a[140:143], a[104:107], v[120:123]
v_mfma_f32_16x16x32_bf16 v[124:127], a[140:143], a[108:111], v[124:127]
s_waitcnt lgkmcnt(10)
v_mfma_f32_16x16x32_bf16 v[128:131], a[144:147], a[96:99], v[128:131]
ds_read_b64_tr_b16 a[168:169], v233 offset:9408
v_mfma_f32_16x16x32_bf16 v[132:135], a[144:147], a[100:103], v[132:135]
ds_read_b64_tr_b16 a[170:171], v233 offset:14016
s_add_u32 m0, m0, 1024
v_mfma_f32_16x16x32_bf16 v[136:139], a[144:147], a[104:107], v[136:139]
buffer_load_dwordx4 v232, s[40:43], s64 offen lds
v_mfma_f32_16x16x32_bf16 v[140:143], a[144:147], a[108:111], v[140:143]
s_waitcnt lgkmcnt(10)
v_mfma_f32_16x16x32_bf16 v[144:147], a[148:151], a[96:99], v[144:147]
)"
R"(
ds_read_b64_tr_b16 a[172:173], v233 offset:9440
v_mfma_f32_16x16x32_bf16 v[148:151], a[148:151], a[100:103], v[148:151]
ds_read_b64_tr_b16 a[174:175], v233 offset:14048
v_mfma_f32_16x16x32_bf16 v[152:155], a[148:151], a[104:107], v[152:155]
v_mfma_f32_16x16x32_bf16 v[156:159], a[148:151], a[108:111], v[156:159]
s_add_u32 s64, s64, s65
s_add_u32 s69, s69, 36864
s_cmp_eq_u32 s69, 147456
s_cselect_b32 s69, 0, s69
buffer_load_dwordx4 a[92:95], v235, s[44:47], s71 offen offset:3072 sc1
s_add_u32 s71, s71, 4096
s_waitcnt lgkmcnt(10)
v_mfma_f32_16x16x32_bf16 v[160:163], a[152:155], a[96:99], v[160:163]
ds_read_b64_tr_b16 a[176:177], v233 offset:9472
v_mfma_f32_16x16x32_bf16 v[164:167], a[152:155], a[100:103], v[164:167]
ds_read_b64_tr_b16 a[178:179], v233 offset:14080
v_mfma_f32_16x16x32_bf16 v[168:171], a[152:155], a[104:107], v[168:171]
v_mfma_f32_16x16x32_bf16 v[172:175], a[152:155], a[108:111], v[172:175]
s_waitcnt lgkmcnt(10)
v_mfma_f32_16x16x32_bf16 v[176:179], a[156:159], a[96:99], v[176:179]
s_add_u32 s67, s67, 36864
s_cmp_eq_u32 s67, 147456
s_cselect_b32 s66, s76, 36864
s_cselect_b32 s67, 0, s67
v_mfma_f32_16x16x32_bf16 v[180:183], a[156:159], a[100:103], v[180:183]
v_mfma_f32_16x16x32_bf16 v[184:187], a[156:159], a[104:107], v[184:187]
v_mfma_f32_16x16x32_bf16 v[188:191], a[156:159], a[108:111], v[188:191]
s_waitcnt lgkmcnt(8)
v_mfma_f32_16x16x32_bf16 v[192:195], a[160:163], a[96:99], v[192:195]
v_add_u32 v233, s66, v233
v_mfma_f32_16x16x32_bf16 v[196:199], a[160:163], a[100:103], v[196:199]
v_add_u32 v234, s66, v234
v_mfma_f32_16x16x32_bf16 v[200:203], a[160:163], a[104:107], v[200:203]
v_mfma_f32_16x16x32_bf16 v[204:207], a[160:163], a[108:111], v[204:207]
s_waitcnt vmcnt(49) lgkmcnt(0)
s_barrier
v_mfma_f32_16x16x32_bf16 v[208:211], a[164:167], a[96:99], v[208:211]
ds_read_b64_tr_b16 a[112:113], v234 offset:18432
ds_read_b64_tr_b16 a[114:115], v234 offset:23040
v_mfma_f32_16x16x32_bf16 v[212:215], a[164:167], a[100:103], v[212:215]
ds_read_b64_tr_b16 a[116:117], v234 offset:18440
ds_read_b64_tr_b16 a[118:119], v234 offset:23048
v_mfma_f32_16x16x32_bf16 v[216:219], a[164:167], a[104:107], v[216:219]
v_mfma_f32_16x16x32_bf16 v[220:223], a[164:167], a[108:111], v[220:223]
s_waitcnt lgkmcnt(8)
v_mfma_f32_16x16x32_bf16 a[0:3], a[168:171], a[96:99], a[0:3]
ds_read_b64_tr_b16 a[120:121], v234 offset:18496
ds_read_b64_tr_b16 a[122:123], v234 offset:23104
v_mfma_f32_16x16x32_bf16 a[4:7], a[168:171], a[100:103], a[4:7]
ds_read_b64_tr_b16 a[124:125], v234 offset:18504
ds_read_b64_tr_b16 a[126:127], v234 offset:23112
v_mfma_f32_16x16x32_bf16 a[8:11], a[168:171], a[104:107], a[8:11]
v_mfma_f32_16x16x32_bf16 a[12:15], a[168:171], a[108:111], a[12:15]
s_waitcnt lgkmcnt(10)
v_mfma_f32_16x16x32_bf16 a[16:19], a[172:175], a[96:99], a[16:19]
ds_read_b64_tr_b16 a[128:129], v234 offset:18560
v_mfma_f32_16x16x32_bf16 a[20:23], a[172:175], a[100:103], a[20:23]
ds_read_b64_tr_b16 a[130:131], v234 offset:23168
v_mfma_f32_16x16x32_bf16 a[24:27], a[172:175], a[104:107], a[24:27]
v_mfma_f32_16x16x32_bf16 a[28:31], a[172:175], a[108:111], a[28:31]
s_waitcnt lgkmcnt(10)
v_mfma_f32_16x16x32_bf16 a[32:35], a[176:179], a[96:99], a[32:35]
ds_read_b64_tr_b16 a[132:133], v234 offset:18568
v_mfma_f32_16x16x32_bf16 a[36:39], a[176:179], a[100:103], a[36:39]
ds_read_b64_tr_b16 a[134:135], v234 offset:23176
v_mfma_f32_16x16x32_bf16 a[40:43], a[176:179], a[104:107], a[40:43]
v_mfma_f32_16x16x32_bf16 a[44:47], a[176:179], a[108:111], a[44:47]
s_waitcnt vmcnt(30) lgkmcnt(10)
v_mfma_f32_16x16x32_bf16 v[0:3], a[112:115], a[192:195], v[0:3]
ds_read_b64_tr_b16 a[136:137], v234 offset:18624
v_mfma_f32_16x16x32_bf16 v[4:7], a[112:115], a[196:199], v[4:7]
ds_read_b64_tr_b16 a[138:139], v234 offset:23232
v_mfma_f32_16x16x32_bf16 v[8:11], a[112:115], a[200:203], v[8:11]
v_mfma_f32_16x16x32_bf16 v[12:15], a[112:115], a[204:207], v[12:15]
buffer_load_dwordx4 a[96:99], v235, s[44:47], s71 offen offset:0 sc1
s_waitcnt lgkmcnt(10)
v_mfma_f32_16x16x32_bf16 v[16:19], a[116:119], a[192:195], v[16:19]
ds_read_b64_tr_b16 a[140:141], v234 offset:18632
v_mfma_f32_16x16x32_bf16 v[20:23], a[116:119], a[196:199], v[20:23]
ds_read_b64_tr_b16 a[142:143], v234 offset:23240
s_add_u32 m0, s70, s69
v_mfma_f32_16x16x32_bf16 v[24:27], a[116:119], a[200:203], v[24:27]
buffer_load_dwordx4 v224, s[40:43], s64 offen lds
v_mfma_f32_16x16x32_bf16 v[28:31], a[116:119], a[204:207], v[28:31]
s_waitcnt lgkmcnt(10)
v_mfma_f32_16x16x32_bf16 v[32:35], a[120:123], a[192:195], v[32:35]
ds_read_b64_tr_b16 a[144:145], v233 offset:0
v_mfma_f32_16x16x32_bf16 v[36:39], a[120:123], a[196:199], v[36:39]
ds_read_b64_tr_b16 a[146:147], v233 offset:4608
v_mfma_f32_16x16x32_bf16 v[40:43], a[120:123], a[200:203], v[40:43]
v_mfma_f32_16x16x32_bf16 v[44:47], a[120:123], a[204:207], v[44:47]
s_waitcnt lgkmcnt(10)
v_mfma_f32_16x16x32_bf16 v[48:51], a[124:127], a[192:195], v[48:51]
ds_read_b64_tr_b16 a[148:149], v233 offset:32
v_mfma_f32_16x16x32_bf16 v[52:55], a[124:127], a[196:199], v[52:55]
ds_read_b64_tr_b16 a[150:151], v233 offset:4640
v_mfma_f32_16x16x32_bf16 v[56:59], a[124:127], a[200:203], v[56:59]
v_mfma_f32_16x16x32_bf16 v[60:63], a[124:127], a[204:207], v[60:63]
buffer_load_dwordx4 a[100:103], v235, s[44:47], s71 offen offset:1024 sc1
s_waitcnt lgkmcnt(10)
v_mfma_f32_16x16x32_bf16 v[64:67], a[128:131], a[192:195], v[64:67]
ds_read_b64_tr_b16 a[152:153], v233 offset:64
v_mfma_f32_16x16x32_bf16 v[68:71], a[128:131], a[196:199], v[68:71]
ds_read_b64_tr_b16 a[154:155], v233 offset:4672
s_add_u32 m0, m0, 1024
v_mfma_f32_16x16x32_bf16 v[72:75], a[128:131], a[200:203], v[72:75]
buffer_load_dwordx4 v225, s[40:43], s64 offen lds
v_mfma_f32_16x16x32_bf16 v[76:79], a[128:131], a[204:207], v[76:79]
s_waitcnt lgkmcnt(10)
v_mfma_f32_16x16x32_bf16 v[80:83], a[132:135], a[192:195], v[80:83]
ds_read_b64_tr_b16 a[156:157], v233 offset:96
v_mfma_f32_16x16x32_bf16 v[84:87], a[132:135], a[196:199], v[84:87]
ds_read_b64_tr_b16 a[158:159], v233 offset:4704
v_mfma_f32_16x16x32_bf16 v[88:91], a[132:135], a[200:203], v[88:91]
v_mfma_f32_16x16x32_bf16 v[92:95], a[132:135], a[204:207], v[92:95]
s_waitcnt lgkmcnt(10)
v_mfma_f32_16x16x32_bf16 v[96:99], a[136:139], a[192:195], v[96:99]
ds_read_b64_tr_b16 a[160:161], v233 offset:128
v_mfma_f32_16x16x32_bf16 v[100:103], a[136:139], a[196:199], v[100:103]
ds_read_b64_tr_b16 a[162:163], v233 offset:4736
v_mfma_f32_16x16x32_bf16 v[104:107], a[136:139], a[200:203], v[104:107]
v_mfma_f32_16x16x32_bf16 v[108:111], a[136:139], a[204:207], v[108:111]
buffer_load_dwordx4 a[104:107], v235, s[44:47], s71 offen offset:2048 sc1
s_waitcnt lgkmcnt(10)
v_mfma_f32_16x16x32_bf16 v[112:115], a[140:143], a[192:195], v[112:115]
ds_read_b64_tr_b16 a[164:165], v233 offset:160
v_mfma_f32_16x16x32_bf16 v[116:119], a[140:143], a[196:199], v[116:119]
ds_read_b64_tr_b16 a[166:167], v233 offset:4768
s_add_u32 m0, m0, 1024
v_mfma_f32_16x16x32_bf16 v[120:123], a[140:143], a[200:203], v[120:123]
buffer_load_dwordx4 v226, s[40:43], s64 offen lds
v_mfma_f32_16x16x32_bf16 v[124:127], a[140:143], a[204:207], v[124:127]
s_waitcnt lgkmcnt(10)
v_mfma_f32_16x16x32_bf16 v[128:131], a[144:147], a[192:195], v[128:131]
ds_read_b64_tr_b16 a[168:169], v233 offset:192
v_mfma_f32_16x16x32_bf16 v[132:135], a[144:147], a[196:199], v[132:135]
ds_read_b64_tr_b16 a[170:171], v233 offset:4800
v_mfma_f32_16x16x32_bf16 v[136:139], a[144:147], a[200:203], v[136:139]
v_mfma_f32_16x16x32_bf16 v[140:143], a[144:147], a[204:207], v[140:143]
s_waitcnt lgkmcnt(10)
v_mfma_f32_16x16x32_bf16 v[144:147], a[148:151], a[192:195], v[144:147]
ds_read_b64_tr_b16 a[172:173], v233 offset:224
v_mfma_f32_16x16x32_bf16 v[148:151], a[148:151], a[196:199], v[148:151]
ds_read_b64_tr_b16 a[174:175], v233 offset:4832
v_mfma_f32_16x16x32_bf16 v[152:155], a[148:151], a[200:203], v[152:155]
v_mfma_f32_16x16x32_bf16 v[156:159], a[148:151], a[204:207], v[156:159]
buffer_load_dwordx4 a[108:111], v235, s[44:47], s71 offen offset:3072 sc1
s_add_u32 s71, s71, 4096
s_waitcnt lgkmcnt(10)
v_mfma_f32_16x16x32_bf16 v[160:163], a[152:155], a[192:195], v[160:163]
ds_read_b64_tr_b16 a[176:177], v233 offset:256
v_mfma_f32_16x16x32_bf16 v[164:167], a[152:155], a[196:199], v[164:167]
ds_read_b64_tr_b16 a[178:179], v233 offset:4864
s_add_u32 m0, m0, 1024
v_mfma_f32_16x16x32_bf16 v[168:171], a[152:155], a[200:203], v[168:171]
buffer_load_dwordx4 v227, s[40:43], s64 offen lds
v_mfma_f32_16x16x32_bf16 v[172:175], a[152:155], a[204:207], v[172:175]
s_waitcnt lgkmcnt(10)
v_mfma_f32_16x16x32_bf16 v[176:179], a[156:159], a[192:195], v[176:179]
ds_read_b64_tr_b16 a[112:113], v234 offset:27648
v_mfma_f32_16x16x32_bf16 v[180:183], a[156:159], a[196:199], v[180:183]
ds_read_b64_tr_b16 a[114:115], v234 offset:32256
v_mfma_f32_16x16x32_bf16 v[184:187], a[156:159], a[200:203], v[184:187]
v_mfma_f32_16x16x32_bf16 v[188:191], a[156:159], a[204:207], v[188:191]
s_waitcnt lgkmcnt(10)
v_mfma_f32_16x16x32_bf16 v[192:195], a[160:163], a[192:195], v[192:195]
ds_read_b64_tr_b16 a[116:117], v234 offset:27656
v_mfma_f32_16x16x32_bf16 v[196:199], a[160:163], a[196:199], v[196:199]
ds_read_b64_tr_b16 a[118:119], v234 offset:32264
v_mfma_f32_16x16x32_bf16 v[200:203], a[160:163], a[200:203], v[200:203]
v_mfma_f32_16x16x32_bf16 v[204:207], a[160:163], a[204:207], v[204:207]
s_waitcnt lgkmcnt(10)
v_mfma_f32_16x16x32_bf16 v[208:211], a[164:167], a[192:195], v[208:211]
ds_read_b64_tr_b16 a[120:121], v234 offset:27712
v_mfma_f32_16x16x32_bf16 v[212:215], a[164:167], a[196:199], v[212:215]
ds_read_b64_tr_b16 a[122:123], v234 offset:32320
s_add_u32 m0, m0, 1024
v_mfma_f32_16x16x32_bf16 v[216:219], a[164:167], a[200:203], v[216:219]
buffer_load_dwordx4 v228, s[40:43], s64 offen lds
v_mfma_f32_16x16x32_bf16 v[220:223], a[164:167], a[204:207], v[220:223]
s_waitcnt lgkmcnt(10)
v_mfma_f32_16x16x32_bf16 a[0:3], a[168:171], a[192:195], a[0:3]
ds_read_b64_tr_b16 a[124:125], v234 offset:27720
v_mfma_f32_16x16x32_bf16 a[4:7], a[168:171], a[196:199], a[4:7]
ds_read_b64_tr_b16 a[126:127], v234 offset:32328
v_mfma_f32_16x16x32_bf16 a[8:11], a[168:171], a[200:203], a[8:11]
v_mfma_f32_16x16x32_bf16 a[12:15], a[168:171], a[204:207], a[12:15]
s_waitcnt lgkmcnt(10)
v_mfma_f32_16x16x32_bf16 a[16:19], a[172:175], a[192:195], a[16:19]
ds_read_b64_tr_b16 a[128:129], v234 offset:27776
v_mfma_f32_16x16x32_bf16 a[20:23], a[172:175], a[196:199], a[20:23]
ds_read_b64_tr_b16 a[130:131], v234 offset:32384
v_mfma_f32_16x16x32_bf16 a[24:27], a[172:175], a[200:203], a[24:27]
v_mfma_f32_16x16x32_bf16 a[28:31], a[172:175], a[204:207], a[28:31]
s_waitcnt lgkmcnt(10)
v_mfma_f32_16x16x32_bf16 a[32:35], a[176:179], a[192:195], a[32:35]
ds_read_b64_tr_b16 a[132:133], v234 offset:27784
v_mfma_f32_16x16x32_bf16 a[36:39], a[176:179], a[196:199], a[36:39]
ds_read_b64_tr_b16 a[134:135], v234 offset:32392
s_add_u32 m0, m0, 1024
v_mfma_f32_16x16x32_bf16 a[40:43], a[176:179], a[200:203], a[40:43]
buffer_load_dwordx4 v229, s[40:43], s64 offen lds
v_mfma_f32_16x16x32_bf16 a[44:47], a[176:179], a[204:207], a[44:47]
s_waitcnt vmcnt(31) lgkmcnt(10)
v_mfma_f32_16x16x32_bf16 v[0:3], a[112:115], a[208:211], v[0:3]
ds_read_b64_tr_b16 a[136:137], v234 offset:27840
v_mfma_f32_16x16x32_bf16 v[4:7], a[112:115], a[212:215], v[4:7]
ds_read_b64_tr_b16 a[138:139], v234 offset:32448
v_mfma_f32_16x16x32_bf16 v[8:11], a[112:115], a[216:219], v[8:11]
v_mfma_f32_16x16x32_bf16 v[12:15], a[112:115], a[220:223], v[12:15]
buffer_load_dwordx4 a[192:195], v235, s[44:47], s71 offen offset:0 sc1
s_waitcnt lgkmcnt(10)
v_mfma_f32_16x16x32_bf16 v[16:19], a[116:119], a[208:211], v[16:19]
ds_read_b64_tr_b16 a[140:141], v234 offset:27848
v_mfma_f32_16x16x32_bf16 v[20:23], a[116:119], a[212:215], v[20:23]
ds_read_b64_tr_b16 a[142:143], v234 offset:32456
v_mfma_f32_16x16x32_bf16 v[24:27], a[116:119], a[216:219], v[24:27]
v_mfma_f32_16x16x32_bf16 v[28:31], a[116:119], a[220:223], v[28:31]
s_waitcnt lgkmcnt(10)
v_mfma_f32_16x16x32_bf16 v[32:35], a[120:123], a[208:211], v[32:35]
ds_read_b64_tr_b16 a[144:145], v233 offset:9216
v_mfma_f32_16x16x32_bf16 v[36:39], a[120:123], a[212:215], v[36:39]
ds_read_b64_tr_b16 a[146:147], v233 offset:13824
s_add_u32 m0, m0, 1024
v_mfma_f32_16x16x32_bf16 v[40:43], a[120:123], a[216:219], v[40:43]
buffer_load_dwordx4 v230, s[40:43], s64 offen lds
v_mfma_f32_16x16x32_bf16 v[44:47], a[120:123], a[220:223], v[44:47]
s_waitcnt lgkmcnt(10)
v_mfma_f32_16x16x32_bf16 v[48:51], a[124:127], a[208:211], v[48:51]
ds_read_b64_tr_b16 a[148:149], v233 offset:9248
v_mfma_f32_16x16x32_bf16 v[52:55], a[124:127], a[212:215], v[52:55]
ds_read_b64_tr_b16 a[150:151], v233 offset:13856
v_mfma_f32_16x16x32_bf16 v[56:59], a[124:127], a[216:219], v[56:59]
v_mfma_f32_16x16x32_bf16 v[60:63], a[124:127], a[220:223], v[60:63]
buffer_load_dwordx4 a[196:199], v235, s[44:47], s71 offen offset:1024 sc1
s_waitcnt lgkmcnt(10)
v_mfma_f32_16x16x32_bf16 v[64:67], a[128:131], a[208:211], v[64:67]
ds_read_b64_tr_b16 a[152:153], v233 offset:9280
v_mfma_f32_16x16x32_bf16 v[68:71], a[128:131], a[212:215], v[68:71]
ds_read_b64_tr_b16 a[154:155], v233 offset:13888
v_mfma_f32_16x16x32_bf16 v[72:75], a[128:131], a[216:219], v[72:75]
v_mfma_f32_16x16x32_bf16 v[76:79], a[128:131], a[220:223], v[76:79]
s_waitcnt lgkmcnt(10)
v_mfma_f32_16x16x32_bf16 v[80:83], a[132:135], a[208:211], v[80:83]
ds_read_b64_tr_b16 a[156:157], v233 offset:9312
v_mfma_f32_16x16x32_bf16 v[84:87], a[132:135], a[212:215], v[84:87]
ds_read_b64_tr_b16 a[158:159], v233 offset:13920
s_add_u32 m0, m0, 1024
v_mfma_f32_16x16x32_bf16 v[88:91], a[132:135], a[216:219], v[88:91]
buffer_load_dwordx4 v231, s[40:43], s64 offen lds
v_mfma_f32_16x16x32_bf16 v[92:95], a[132:135], a[220:223], v[92:95]
s_waitcnt lgkmcnt(10)
v_mfma_f32_16x16x32_bf16 v[96:99], a[136:139], a[208:211], v[96:99]
ds_read_b64_tr_b16 a[160:161], v233 offset:9344
v_mfma_f32_16x16x32_bf16 v[100:103], a[136:139], a[212:215], v[100:103]
ds_read_b64_tr_b16 a[162:163], v233 offset:13952
v_mfma_f32_16x16x32_bf16 v[104:107], a[136:139], a[216:219], v[104:107]
v_mfma_f32_16x16x32_bf16 v[108:111], a[136:139], a[220:223], v[108:111]
buffer_load_dwordx4 a[200:203], v235, s[44:47], s71 offen offset:2048 sc1
s_waitcnt lgkmcnt(10)
v_mfma_f32_16x16x32_bf16 v[112:115], a[140:143], a[208:211], v[112:115]
ds_read_b64_tr_b16 a[164:165], v233 offset:9376
v_mfma_f32_16x16x32_bf16 v[116:119], a[140:143], a[212:215], v[116:119]
ds_read_b64_tr_b16 a[166:167], v233 offset:13984
v_mfma_f32_16x16x32_bf16 v[120:123], a[140:143], a[216:219], v[120:123]
v_mfma_f32_16x16x32_bf16 v[124:127], a[140:143], a[220:223], v[124:127]
s_waitcnt lgkmcnt(10)
v_mfma_f32_16x16x32_bf16 v[128:131], a[144:147], a[208:211], v[128:131]
ds_read_b64_tr_b16 a[168:169], v233 offset:9408
v_mfma_f32_16x16x32_bf16 v[132:135], a[144:147], a[212:215], v[132:135]
ds_read_b64_tr_b16 a[170:171], v233 offset:14016
s_add_u32 m0, m0, 1024
v_mfma_f32_16x16x32_bf16 v[136:139], a[144:147], a[216:219], v[136:139]
buffer_load_dwordx4 v232, s[40:43], s64 offen lds
v_mfma_f32_16x16x32_bf16 v[140:143], a[144:147], a[220:223], v[140:143]
s_waitcnt lgkmcnt(10)
v_mfma_f32_16x16x32_bf16 v[144:147], a[148:151], a[208:211], v[144:147]
ds_read_b64_tr_b16 a[172:173], v233 offset:9440
v_mfma_f32_16x16x32_bf16 v[148:151], a[148:151], a[212:215], v[148:151]
ds_read_b64_tr_b16 a[174:175], v233 offset:14048
v_mfma_f32_16x16x32_bf16 v[152:155], a[148:151], a[216:219], v[152:155]
v_mfma_f32_16x16x32_bf16 v[156:159], a[148:151], a[220:223], v[156:159]
s_add_u32 s64, s64, s65
s_add_u32 s69, s69, 36864
s_cmp_eq_u32 s69, 147456
s_cselect_b32 s69, 0, s69
buffer_load_dwordx4 a[204:207], v235, s[44:47], s71 offen offset:3072 sc1
s_add_u32 s71, s71, 4096
s_waitcnt lgkmcnt(10)
v_mfma_f32_16x16x32_bf16 v[160:163], a[152:155], a[208:211], v[160:163]
ds_read_b64_tr_b16 a[176:177], v233 offset:9472
v_mfma_f32_16x16x32_bf16 v[164:167], a[152:155], a[212:215], v[164:167]
ds_read_b64_tr_b16 a[178:179], v233 offset:14080
v_mfma_f32_16x16x32_bf16 v[168:171], a[152:155], a[216:219], v[168:171]
v_mfma_f32_16x16x32_bf16 v[172:175], a[152:155], a[220:223], v[172:175]
s_waitcnt lgkmcnt(10)
v_mfma_f32_16x16x32_bf16 v[176:179], a[156:159], a[208:211], v[176:179]
s_add_u32 s67, s67, 36864
s_cmp_eq_u32 s67, 147456
s_cselect_b32 s66, s76, 36864
s_cselect_b32 s67, 0, s67
v_mfma_f32_16x16x32_bf16 v[180:183], a[156:159], a[212:215], v[180:183]
v_mfma_f32_16x16x32_bf16 v[184:187], a[156:159], a[216:219], v[184:187]
v_mfma_f32_16x16x32_bf16 v[188:191], a[156:159], a[220:223], v[188:191]
s_waitcnt lgkmcnt(8)
v_mfma_f32_16x16x32_bf16 v[192:195], a[160:163], a[208:211], v[192:195]
v_add_u32 v233, s66, v233
v_mfma_f32_16x16x32_bf16 v[196:199], a[160:163], a[212:215], v[196:199]
v_add_u32 v234, s66, v234
v_mfma_f32_16x16x32_bf16 v[200:203], a[160:163], a[216:219], v[200:203]
v_mfma_f32_16x16x32_bf16 v[204:207], a[160:163], a[220:223], v[204:207]
s_waitcnt vmcnt(39) lgkmcnt(0)
s_barrier
v_mfma_f32_16x16x32_bf16 v[208:211], a[164:167], a[208:211], v[208:211]
ds_read_b64_tr_b16 a[112:113], v234 offset:18432
ds_read_b64_tr_b16 a[114:115], v234 offset:23040
v_mfma_f32_16x16x32_bf16 v[212:215], a[164:167], a[212:215], v[212:215]
ds_read_b64_tr_b16 a[116:117], v234 offset:18440
ds_read_b64_tr_b16 a[118:119], v234 offset:23048
v_mfma_f32_16x16x32_bf16 v[216:219], a[164:167], a[216:219], v[216:219]
v_mfma_f32_16x16x32_bf16 v[220:223], a[164:167], a[220:223], v[220:223]
s_waitcnt lgkmcnt(8)
v_mfma_f32_16x16x32_bf16 a[0:3], a[168:171], a[208:211], a[0:3]
ds_read_b64_tr_b16 a[120:121], v234 offset:18496
ds_read_b64_tr_b16 a[122:123], v234 offset:23104
v_mfma_f32_16x16x32_bf16 a[4:7], a[168:171], a[212:215], a[4:7]
ds_read_b64_tr_b16 a[124:125], v234 offset:18504
ds_read_b64_tr_b16 a[126:127], v234 offset:23112
v_mfma_f32_16x16x32_bf16 a[8:11], a[168:171], a[216:219], a[8:11]
v_mfma_f32_16x16x32_bf16 a[12:15], a[168:171], a[220:223], a[12:15]
s_waitcnt lgkmcnt(10)
v_mfma_f32_16x16x32_bf16 a[16:19], a[172:175], a[208:211], a[16:19]
ds_read_b64_tr_b16 a[128:129], v234 offset:18560
v_mfma_f32_16x16x32_bf16 a[20:23], a[172:175], a[212:215], a[20:23]
ds_read_b64_tr_b16 a[130:131], v234 offset:23168
v_mfma_f32_16x16x32_bf16 a[24:27], a[172:175], a[216:219], a[24:27]
v_mfma_f32_16x16x32_bf16 a[28:31], a[172:175], a[220:223], a[28:31]
s_waitcnt lgkmcnt(10)
v_mfma_f32_16x16x32_bf16 a[32:35], a[176:179], a[208:211], a[32:35]
ds_read_b64_tr_b16 a[132:133], v234 offset:18568
v_mfma_f32_16x16x32_bf16 a[36:39], a[176:179], a[212:215], a[36:39]
ds_read_b64_tr_b16 a[134:135], v234 offset:23176
v_mfma_f32_16x16x32_bf16 a[40:43], a[176:179], a[216:219], a[40:43]
v_mfma_f32_16x16x32_bf16 a[44:47], a[176:179], a[220:223], a[44:47]
s_waitcnt vmcnt(31) lgkmcnt(10)
v_mfma_f32_16x16x32_bf16 v[0:3], a[112:115], a[48:51], v[0:3]
ds_read_b64_tr_b16 a[136:137], v234 offset:18624
v_mfma_f32_16x16x32_bf16 v[4:7], a[112:115], a[52:55], v[4:7]
ds_read_b64_tr_b16 a[138:139], v234 offset:23232
v_mfma_f32_16x16x32_bf16 v[8:11], a[112:115], a[56:59], v[8:11]
v_mfma_f32_16x16x32_bf16 v[12:15], a[112:115], a[60:63], v[12:15]
buffer_load_dwordx4 a[208:211], v235, s[44:47], s71 offen offset:0 sc1
s_waitcnt lgkmcnt(10)
v_mfma_f32_16x16x32_bf16 v[16:19], a[116:119], a[48:51], v[16:19]
ds_read_b64_tr_b16 a[140:141], v234 offset:18632
v_mfma_f32_16x16x32_bf16 v[20:23], a[116:119], a[52:55], v[20:23]
ds_read_b64_tr_b16 a[142:143], v234 offset:23240
s_add_u32 m0, s70, s69
v_mfma_f32_16x16x32_bf16 v[24:27], a[116:119], a[56:59], v[24:27]
buffer_load_dwordx4 v224, s[40:43], s64 offen lds
v_mfma_f32_16x16x32_bf16 v[28:31], a[116:119], a[60:63], v[28:31]
s_waitcnt lgkmcnt(10)
v_mfma_f32_16x16x32_bf16 v[32:35], a[120:123], a[48:51], v[32:35]
ds_read_b64_tr_b16 a[144:145], v233 offset:0
v_mfma_f32_16x16x32_bf16 v[36:39], a[120:123], a[52:55], v[36:39]
ds_read_b64_tr_b16 a[146:147], v233 offset:4608
v_mfma_f32_16x16x32_bf16 v[40:43], a[120:123], a[56:59], v[40:43]
v_mfma_f32_16x16x32_bf16 v[44:47], a[120:123], a[60:63], v[44:47]
s_waitcnt lgkmcnt(10)
v_mfma_f32_16x16x32_bf16 v[48:51], a[124:127], a[48:51], v[48:51]
ds_read_b64_tr_b16 a[148:149], v233 offset:32
v_mfma_f32_16x16x32_bf16 v[52:55], a[124:127], a[52:55], v[52:55]
ds_read_b64_tr_b16 a[150:151], v233 offset:4640
v_mfma_f32_16x16x32_bf16 v[56:59], a[124:127], a[56:59], v[56:59]
v_mfma_f32_16x16x32_bf16 v[60:63], a[124:127], a[60:63], v[60:63]
buffer_load_dwordx4 a[212:215], v235, s[44:47], s71 offen offset:1024 sc1
s_waitcnt lgkmcnt(10)
v_mfma_f32_16x16x32_bf16 v[64:67], a[128:131], a[48:51], v[64:67]
ds_read_b64_tr_b16 a[152:153], v233 offset:64
v_mfma_f32_16x16x32_bf16 v[68:71], a[128:131], a[52:55], v[68:71]
ds_read_b64_tr_b16 a[154:155], v233 offset:4672
s_add_u32 m0, m0, 1024
v_mfma_f32_16x16x32_bf16 v[72:75], a[128:131], a[56:59], v[72:75]
buffer_load_dwordx4 v225, s[40:43], s64 offen lds
v_mfma_f32_16x16x32_bf16 v[76:79], a[128:131], a[60:63], v[76:79]
s_waitcnt lgkmcnt(10)
v_mfma_f32_16x16x32_bf16 v[80:83], a[132:135], a[48:51], v[80:83]
ds_read_b64_tr_b16 a[156:157], v233 offset:96
v_mfma_f32_16x16x32_bf16 v[84:87], a[132:135], a[52:55], v[84:87]
ds_read_b64_tr_b16 a[158:159], v233 offset:4704
v_mfma_f32_16x16x32_bf16 v[88:91], a[132:135], a[56:59], v[88:91]
buffer_load_dwordx3 a[180:182], v235, s[56:59], s80 offen offset:0
s_add_u32 s80, s80, 1024
v_mfma_f32_16x16x32_bf16 v[92:95], a[132:135], a[60:63], v[92:95]
s_waitcnt lgkmcnt(10)
v_mfma_f32_16x16x32_bf16 v[96:99], a[136:139], a[48:51], v[96:99]
ds_read_b64_tr_b16 a[160:161], v233 offset:128
v_mfma_f32_16x16x32_bf16 v[100:103], a[136:139], a[52:55], v[100:103]
ds_read_b64_tr_b16 a[162:163], v233 offset:4736
v_mfma_f32_16x16x32_bf16 v[104:107], a[136:139], a[56:59], v[104:107]
)"
R"(
v_mfma_f32_16x16x32_bf16 v[108:111], a[136:139], a[60:63], v[108:111]
buffer_load_dwordx4 a[216:219], v235, s[44:47], s71 offen offset:2048 sc1
s_waitcnt lgkmcnt(10)
v_mfma_f32_16x16x32_bf16 v[112:115], a[140:143], a[48:51], v[112:115]
ds_read_b64_tr_b16 a[164:165], v233 offset:160
v_mfma_f32_16x16x32_bf16 v[116:119], a[140:143], a[52:55], v[116:119]
ds_read_b64_tr_b16 a[166:167], v233 offset:4768
s_add_u32 m0, m0, 1024
v_mfma_f32_16x16x32_bf16 v[120:123], a[140:143], a[56:59], v[120:123]
buffer_load_dwordx4 v226, s[40:43], s64 offen lds
v_mfma_f32_16x16x32_bf16 v[124:127], a[140:143], a[60:63], v[124:127]
s_waitcnt lgkmcnt(10)
v_mfma_f32_16x16x32_bf16 v[128:131], a[144:147], a[48:51], v[128:131]
ds_read_b64_tr_b16 a[168:169], v233 offset:192
v_mfma_f32_16x16x32_bf16 v[132:135], a[144:147], a[52:55], v[132:135]
ds_read_b64_tr_b16 a[170:171], v233 offset:4800
v_mfma_f32_16x16x32_bf16 v[136:139], a[144:147], a[56:59], v[136:139]
v_mfma_f32_16x16x32_bf16 v[140:143], a[144:147], a[60:63], v[140:143]
s_waitcnt lgkmcnt(10)
v_mfma_f32_16x16x32_bf16 v[144:147], a[148:151], a[48:51], v[144:147]
ds_read_b64_tr_b16 a[172:173], v233 offset:224
v_mfma_f32_16x16x32_bf16 v[148:151], a[148:151], a[52:55], v[148:151]
ds_read_b64_tr_b16 a[174:175], v233 offset:4832
v_mfma_f32_16x16x32_bf16 v[152:155], a[148:151], a[56:59], v[152:155]
v_mfma_f32_16x16x32_bf16 v[156:159], a[148:151], a[60:63], v[156:159]
buffer_load_dwordx4 a[220:223], v235, s[44:47], s71 offen offset:3072 sc1
s_add_u32 s71, s71, 4096
s_waitcnt lgkmcnt(10)
v_mfma_f32_16x16x32_bf16 v[160:163], a[152:155], a[48:51], v[160:163]
ds_read_b64_tr_b16 a[176:177], v233 offset:256
v_mfma_f32_16x16x32_bf16 v[164:167], a[152:155], a[52:55], v[164:167]
ds_read_b64_tr_b16 a[178:179], v233 offset:4864
s_add_u32 m0, m0, 1024
v_mfma_f32_16x16x32_bf16 v[168:171], a[152:155], a[56:59], v[168:171]
buffer_load_dwordx4 v227, s[40:43], s64 offen lds
v_mfma_f32_16x16x32_bf16 v[172:175], a[152:155], a[60:63], v[172:175]
s_waitcnt lgkmcnt(10)
v_mfma_f32_16x16x32_bf16 v[176:179], a[156:159], a[48:51], v[176:179]
ds_read_b64_tr_b16 a[112:113], v234 offset:27648
v_mfma_f32_16x16x32_bf16 v[180:183], a[156:159], a[52:55], v[180:183]
ds_read_b64_tr_b16 a[114:115], v234 offset:32256
v_mfma_f32_16x16x32_bf16 v[184:187], a[156:159], a[56:59], v[184:187]
buffer_load_dwordx3 a[180:182], v235, s[56:59], s80 offen offset:0
s_add_u32 s80, s80, 1024
v_mfma_f32_16x16x32_bf16 v[188:191], a[156:159], a[60:63], v[188:191]
s_waitcnt lgkmcnt(10)
v_mfma_f32_16x16x32_bf16 v[192:195], a[160:163], a[48:51], v[192:195]
ds_read_b64_tr_b16 a[116:117], v234 offset:27656
v_mfma_f32_16x16x32_bf16 v[196:199], a[160:163], a[52:55], v[196:199]
ds_read_b64_tr_b16 a[118:119], v234 offset:32264
v_mfma_f32_16x16x32_bf16 v[200:203], a[160:163], a[56:59], v[200:203]
v_mfma_f32_16x16x32_bf16 v[204:207], a[160:163], a[60:63], v[204:207]
s_waitcnt lgkmcnt(10)
v_mfma_f32_16x16x32_bf16 v[208:211], a[164:167], a[48:51], v[208:211]
ds_read_b64_tr_b16 a[120:121], v234 offset:27712
v_mfma_f32_16x16x32_bf16 v[212:215], a[164:167], a[52:55], v[212:215]
ds_read_b64_tr_b16 a[122:123], v234 offset:32320
s_add_u32 m0, m0, 1024
v_mfma_f32_16x16x32_bf16 v[216:219], a[164:167], a[56:59], v[216:219]
buffer_load_dwordx4 v228, s[40:43], s64 offen lds
v_mfma_f32_16x16x32_bf16 v[220:223], a[164:167], a[60:63], v[220:223]
s_waitcnt lgkmcnt(10)
v_mfma_f32_16x16x32_bf16 a[0:3], a[168:171], a[48:51], a[0:3]
ds_read_b64_tr_b16 a[124:125], v234 offset:27720
v_mfma_f32_16x16x32_bf16 a[4:7], a[168:171], a[52:55], a[4:7]
ds_read_b64_tr_b16 a[126:127], v234 offset:32328
v_mfma_f32_16x16x32_bf16 a[8:11], a[168:171], a[56:59], a[8:11]
v_mfma_f32_16x16x32_bf16 a[12:15], a[168:171], a[60:63], a[12:15]
s_waitcnt lgkmcnt(10)
v_mfma_f32_16x16x32_bf16 a[16:19], a[172:175], a[48:51], a[16:19]
ds_read_b64_tr_b16 a[128:129], v234 offset:27776
v_mfma_f32_16x16x32_bf16 a[20:23], a[172:175], a[52:55], a[20:23]
ds_read_b64_tr_b16 a[130:131], v234 offset:32384
v_mfma_f32_16x16x32_bf16 a[24:27], a[172:175], a[56:59], a[24:27]
v_mfma_f32_16x16x32_bf16 a[28:31], a[172:175], a[60:63], a[28:31]
s_waitcnt lgkmcnt(10)
v_mfma_f32_16x16x32_bf16 a[32:35], a[176:179], a[48:51], a[32:35]
ds_read_b64_tr_b16 a[132:133], v234 offset:27784
v_mfma_f32_16x16x32_bf16 a[36:39], a[176:179], a[52:55], a[36:39]
ds_read_b64_tr_b16 a[134:135], v234 offset:32392
s_add_u32 m0, m0, 1024
v_mfma_f32_16x16x32_bf16 a[40:43], a[176:179], a[56:59], a[40:43]
buffer_load_dwordx4 v229, s[40:43], s64 offen lds
v_mfma_f32_16x16x32_bf16 a[44:47], a[176:179], a[60:63], a[44:47]
s_waitcnt vmcnt(37) lgkmcnt(10)
v_mfma_f32_16x16x32_bf16 v[0:3], a[112:115], a[64:67], v[0:3]
ds_read_b64_tr_b16 a[136:137], v234 offset:27840
v_mfma_f32_16x16x32_bf16 v[4:7], a[112:115], a[68:71], v[4:7]
ds_read_b64_tr_b16 a[138:139], v234 offset:32448
v_mfma_f32_16x16x32_bf16 v[8:11], a[112:115], a[72:75], v[8:11]
v_mfma_f32_16x16x32_bf16 v[12:15], a[112:115], a[76:79], v[12:15]
buffer_load_dwordx4 a[48:51], v235, s[44:47], s71 offen offset:0 sc1
s_waitcnt lgkmcnt(10)
v_mfma_f32_16x16x32_bf16 v[16:19], a[116:119], a[64:67], v[16:19]
ds_read_b64_tr_b16 a[140:141], v234 offset:27848
v_mfma_f32_16x16x32_bf16 v[20:23], a[116:119], a[68:71], v[20:23]
ds_read_b64_tr_b16 a[142:143], v234 offset:32456
v_mfma_f32_16x16x32_bf16 v[24:27], a[116:119], a[72:75], v[24:27]
v_mfma_f32_16x16x32_bf16 v[28:31], a[116:119], a[76:79], v[28:31]
s_waitcnt lgkmcnt(10)
v_mfma_f32_16x16x32_bf16 v[32:35], a[120:123], a[64:67], v[32:35]
ds_read_b64_tr_b16 a[144:145], v233 offset:9216
v_mfma_f32_16x16x32_bf16 v[36:39], a[120:123], a[68:71], v[36:39]
ds_read_b64_tr_b16 a[146:147], v233 offset:13824
s_add_u32 m0, m0, 1024
v_mfma_f32_16x16x32_bf16 v[40:43], a[120:123], a[72:75], v[40:43]
buffer_load_dwordx4 v230, s[40:43], s64 offen lds
v_mfma_f32_16x16x32_bf16 v[44:47], a[120:123], a[76:79], v[44:47]
s_waitcnt lgkmcnt(10)
v_mfma_f32_16x16x32_bf16 v[48:51], a[124:127], a[64:67], v[48:51]
ds_read_b64_tr_b16 a[148:149], v233 offset:9248
v_mfma_f32_16x16x32_bf16 v[52:55], a[124:127], a[68:71], v[52:55]
ds_read_b64_tr_b16 a[150:151], v233 offset:13856
v_mfma_f32_16x16x32_bf16 v[56:59], a[124:127], a[72:75], v[56:59]
v_mfma_f32_16x16x32_bf16 v[60:63], a[124:127], a[76:79], v[60:63]
buffer_load_dwordx4 a[52:55], v235, s[44:47], s71 offen offset:1024 sc1
s_waitcnt lgkmcnt(10)
v_mfma_f32_16x16x32_bf16 v[64:67], a[128:131], a[64:67], v[64:67]
ds_read_b64_tr_b16 a[152:153], v233 offset:9280
v_mfma_f32_16x16x32_bf16 v[68:71], a[128:131], a[68:71], v[68:71]
ds_read_b64_tr_b16 a[154:155], v233 offset:13888
v_mfma_f32_16x16x32_bf16 v[72:75], a[128:131], a[72:75], v[72:75]
v_mfma_f32_16x16x32_bf16 v[76:79], a[128:131], a[76:79], v[76:79]
s_waitcnt lgkmcnt(10)
v_mfma_f32_16x16x32_bf16 v[80:83], a[132:135], a[64:67], v[80:83]
ds_read_b64_tr_b16 a[156:157], v233 offset:9312
v_mfma_f32_16x16x32_bf16 v[84:87], a[132:135], a[68:71], v[84:87]
ds_read_b64_tr_b16 a[158:159], v233 offset:13920
s_add_u32 m0, m0, 1024
v_mfma_f32_16x16x32_bf16 v[88:91], a[132:135], a[72:75], v[88:91]
buffer_load_dwordx4 v231, s[40:43], s64 offen lds
v_mfma_f32_16x16x32_bf16 v[92:95], a[132:135], a[76:79], v[92:95]
s_waitcnt lgkmcnt(10)
v_mfma_f32_16x16x32_bf16 v[96:99], a[136:139], a[64:67], v[96:99]
ds_read_b64_tr_b16 a[160:161], v233 offset:9344
v_mfma_f32_16x16x32_bf16 v[100:103], a[136:139], a[68:71], v[100:103]
ds_read_b64_tr_b16 a[162:163], v233 offset:13952
v_mfma_f32_16x16x32_bf16 v[104:107], a[136:139], a[72:75], v[104:107]
v_mfma_f32_16x16x32_bf16 v[108:111], a[136:139], a[76:79], v[108:111]
buffer_load_dwordx4 a[56:59], v235, s[44:47], s71 offen offset:2048 sc1
s_waitcnt lgkmcnt(10)
v_mfma_f32_16x16x32_bf16 v[112:115], a[140:143], a[64:67], v[112:115]
ds_read_b64_tr_b16 a[164:165], v233 offset:9376
v_mfma_f32_16x16x32_bf16 v[116:119], a[140:143], a[68:71], v[116:119]
ds_read_b64_tr_b16 a[166:167], v233 offset:13984
v_mfma_f32_16x16x32_bf16 v[120:123], a[140:143], a[72:75], v[120:123]
v_mfma_f32_16x16x32_bf16 v[124:127], a[140:143], a[76:79], v[124:127]
s_waitcnt lgkmcnt(10)
v_mfma_f32_16x16x32_bf16 v[128:131], a[144:147], a[64:67], v[128:131]
ds_read_b64_tr_b16 a[168:169], v233 offset:9408
v_mfma_f32_16x16x32_bf16 v[132:135], a[144:147], a[68:71], v[132:135]
ds_read_b64_tr_b16 a[170:171], v233 offset:14016
s_add_u32 m0, m0, 1024
v_mfma_f32_16x16x32_bf16 v[136:139], a[144:147], a[72:75], v[136:139]
buffer_load_dwordx4 v232, s[40:43], s64 offen lds
v_mfma_f32_16x16x32_bf16 v[140:143], a[144:147], a[76:79], v[140:143]
s_waitcnt lgkmcnt(10)
v_mfma_f32_16x16x32_bf16 v[144:147], a[148:151], a[64:67], v[144:147]
ds_read_b64_tr_b16 a[172:173], v233 offset:9440
v_mfma_f32_16x16x32_bf16 v[148:151], a[148:151], a[68:71], v[148:151]
ds_read_b64_tr_b16 a[174:175], v233 offset:14048
v_mfma_f32_16x16x32_bf16 v[152:155], a[148:151], a[72:75], v[152:155]
v_mfma_f32_16x16x32_bf16 v[156:159], a[148:151], a[76:79], v[156:159]
s_add_u32 s64, s64, s65
s_add_u32 s69, s69, 36864
s_cmp_eq_u32 s69, 147456
s_cselect_b32 s69, 0, s69
buffer_load_dwordx4 a[60:63], v235, s[44:47], s71 offen offset:3072 sc1
s_add_u32 s71, s71, 4096
s_waitcnt lgkmcnt(10)
v_mfma_f32_16x16x32_bf16 v[160:163], a[152:155], a[64:67], v[160:163]
ds_read_b64_tr_b16 a[176:177], v233 offset:9472
v_mfma_f32_16x16x32_bf16 v[164:167], a[152:155], a[68:71], v[164:167]
ds_read_b64_tr_b16 a[178:179], v233 offset:14080
v_mfma_f32_16x16x32_bf16 v[168:171], a[152:155], a[72:75], v[168:171]
v_mfma_f32_16x16x32_bf16 v[172:175], a[152:155], a[76:79], v[172:175]
s_waitcnt lgkmcnt(10)
v_mfma_f32_16x16x32_bf16 v[176:179], a[156:159], a[64:67], v[176:179]
s_add_u32 s67, s67, 36864
s_cmp_eq_u32 s67, 147456
s_cselect_b32 s66, s76, 36864
s_cselect_b32 s67, 0, s67
v_mfma_f32_16x16x32_bf16 v[180:183], a[156:159], a[68:71], v[180:183]
v_mfma_f32_16x16x32_bf16 v[184:187], a[156:159], a[72:75], v[184:187]
v_mfma_f32_16x16x32_bf16 v[188:191], a[156:159], a[76:79], v[188:191]
s_waitcnt lgkmcnt(8)
v_mfma_f32_16x16x32_bf16 v[192:195], a[160:163], a[64:67], v[192:195]
v_add_u32 v233, s66, v233
v_mfma_f32_16x16x32_bf16 v[196:199], a[160:163], a[68:71], v[196:199]
v_add_u32 v234, s66, v234
v_mfma_f32_16x16x32_bf16 v[200:203], a[160:163], a[72:75], v[200:203]
v_mfma_f32_16x16x32_bf16 v[204:207], a[160:163], a[76:79], v[204:207]
s_waitcnt vmcnt(37) lgkmcnt(0)
s_barrier
v_mfma_f32_16x16x32_bf16 v[208:211], a[164:167], a[64:67], v[208:211]
ds_read_b64_tr_b16 a[112:113], v234 offset:18432
ds_read_b64_tr_b16 a[114:115], v234 offset:23040
v_mfma_f32_16x16x32_bf16 v[212:215], a[164:167], a[68:71], v[212:215]
ds_read_b64_tr_b16 a[116:117], v234 offset:18440
ds_read_b64_tr_b16 a[118:119], v234 offset:23048
v_mfma_f32_16x16x32_bf16 v[216:219], a[164:167], a[72:75], v[216:219]
v_mfma_f32_16x16x32_bf16 v[220:223], a[164:167], a[76:79], v[220:223]
s_waitcnt lgkmcnt(8)
v_mfma_f32_16x16x32_bf16 a[0:3], a[168:171], a[64:67], a[0:3]
ds_read_b64_tr_b16 a[120:121], v234 offset:18496
ds_read_b64_tr_b16 a[122:123], v234 offset:23104
v_mfma_f32_16x16x32_bf16 a[4:7], a[168:171], a[68:71], a[4:7]
ds_read_b64_tr_b16 a[124:125], v234 offset:18504
ds_read_b64_tr_b16 a[126:127], v234 offset:23112
v_mfma_f32_16x16x32_bf16 a[8:11], a[168:171], a[72:75], a[8:11]
v_mfma_f32_16x16x32_bf16 a[12:15], a[168:171], a[76:79], a[12:15]
s_waitcnt lgkmcnt(10)
v_mfma_f32_16x16x32_bf16 a[16:19], a[172:175], a[64:67], a[16:19]
ds_read_b64_tr_b16 a[128:129], v234 offset:18560
v_mfma_f32_16x16x32_bf16 a[20:23], a[172:175], a[68:71], a[20:23]
ds_read_b64_tr_b16 a[130:131], v234 offset:23168
v_mfma_f32_16x16x32_bf16 a[24:27], a[172:175], a[72:75], a[24:27]
v_mfma_f32_16x16x32_bf16 a[28:31], a[172:175], a[76:79], a[28:31]
s_waitcnt lgkmcnt(10)
v_mfma_f32_16x16x32_bf16 a[32:35], a[176:179], a[64:67], a[32:35]
ds_read_b64_tr_b16 a[132:133], v234 offset:18568
v_mfma_f32_16x16x32_bf16 a[36:39], a[176:179], a[68:71], a[36:39]
ds_read_b64_tr_b16 a[134:135], v234 offset:23176
v_mfma_f32_16x16x32_bf16 a[40:43], a[176:179], a[72:75], a[40:43]
v_mfma_f32_16x16x32_bf16 a[44:47], a[176:179], a[76:79], a[44:47]
s_waitcnt vmcnt(36) lgkmcnt(10)
v_mfma_f32_16x16x32_bf16 v[0:3], a[112:115], a[80:83], v[0:3]
ds_read_b64_tr_b16 a[136:137], v234 offset:18624
v_mfma_f32_16x16x32_bf16 v[4:7], a[112:115], a[84:87], v[4:7]
ds_read_b64_tr_b16 a[138:139], v234 offset:23232
v_mfma_f32_16x16x32_bf16 v[8:11], a[112:115], a[88:91], v[8:11]
v_mfma_f32_16x16x32_bf16 v[12:15], a[112:115], a[92:95], v[12:15]
buffer_load_dwordx4 a[64:67], v235, s[44:47], s71 offen offset:0 sc1
s_waitcnt lgkmcnt(10)
v_mfma_f32_16x16x32_bf16 v[16:19], a[116:119], a[80:83], v[16:19]
ds_read_b64_tr_b16 a[140:141], v234 offset:18632
v_mfma_f32_16x16x32_bf16 v[20:23], a[116:119], a[84:87], v[20:23]
ds_read_b64_tr_b16 a[142:143], v234 offset:23240
s_add_u32 m0, s70, s69
v_mfma_f32_16x16x32_bf16 v[24:27], a[116:119], a[88:91], v[24:27]
buffer_load_dwordx4 v224, s[40:43], s64 offen lds
v_mfma_f32_16x16x32_bf16 v[28:31], a[116:119], a[92:95], v[28:31]
s_waitcnt lgkmcnt(10)
v_mfma_f32_16x16x32_bf16 v[32:35], a[120:123], a[80:83], v[32:35]
ds_read_b64_tr_b16 a[144:145], v233 offset:0
v_mfma_f32_16x16x32_bf16 v[36:39], a[120:123], a[84:87], v[36:39]
ds_read_b64_tr_b16 a[146:147], v233 offset:4608
v_mfma_f32_16x16x32_bf16 v[40:43], a[120:123], a[88:91], v[40:43]
v_mfma_f32_16x16x32_bf16 v[44:47], a[120:123], a[92:95], v[44:47]
s_waitcnt lgkmcnt(10)
v_mfma_f32_16x16x32_bf16 v[48:51], a[124:127], a[80:83], v[48:51]
ds_read_b64_tr_b16 a[148:149], v233 offset:32
v_mfma_f32_16x16x32_bf16 v[52:55], a[124:127], a[84:87], v[52:55]
ds_read_b64_tr_b16 a[150:151], v233 offset:4640
v_mfma_f32_16x16x32_bf16 v[56:59], a[124:127], a[88:91], v[56:59]
v_mfma_f32_16x16x32_bf16 v[60:63], a[124:127], a[92:95], v[60:63]
buffer_load_dwordx4 a[68:71], v235, s[44:47], s71 offen offset:1024 sc1
s_waitcnt lgkmcnt(10)
v_mfma_f32_16x16x32_bf16 v[64:67], a[128:131], a[80:83], v[64:67]
ds_read_b64_tr_b16 a[152:153], v233 offset:64
v_mfma_f32_16x16x32_bf16 v[68:71], a[128:131], a[84:87], v[68:71]
ds_read_b64_tr_b16 a[154:155], v233 offset:4672
s_add_u32 m0, m0, 1024
v_mfma_f32_16x16x32_bf16 v[72:75], a[128:131], a[88:91], v[72:75]
buffer_load_dwordx4 v225, s[40:43], s64 offen lds
v_mfma_f32_16x16x32_bf16 v[76:79], a[128:131], a[92:95], v[76:79]
s_waitcnt lgkmcnt(10)
v_mfma_f32_16x16x32_bf16 v[80:83], a[132:135], a[80:83], v[80:83]
ds_read_b64_tr_b16 a[156:157], v233 offset:96
v_mfma_f32_16x16x32_bf16 v[84:87], a[132:135], a[84:87], v[84:87]
ds_read_b64_tr_b16 a[158:159], v233 offset:4704
v_mfma_f32_16x16x32_bf16 v[88:91], a[132:135], a[88:91], v[88:91]
buffer_load_dwordx3 a[180:182], v235, s[56:59], s80 offen offset:0
s_add_u32 s80, s80, 1024
v_mfma_f32_16x16x32_bf16 v[92:95], a[132:135], a[92:95], v[92:95]
s_waitcnt lgkmcnt(10)
v_mfma_f32_16x16x32_bf16 v[96:99], a[136:139], a[80:83], v[96:99]
ds_read_b64_tr_b16 a[160:161], v233 offset:128
v_mfma_f32_16x16x32_bf16 v[100:103], a[136:139], a[84:87], v[100:103]
ds_read_b64_tr_b16 a[162:163], v233 offset:4736
v_mfma_f32_16x16x32_bf16 v[104:107], a[136:139], a[88:91], v[104:107]
v_mfma_f32_16x16x32_bf16 v[108:111], a[136:139], a[92:95], v[108:111]
buffer_load_dwordx4 a[72:75], v235, s[44:47], s71 offen offset:2048 sc1
s_waitcnt lgkmcnt(10)
v_mfma_f32_16x16x32_bf16 v[112:115], a[140:143], a[80:83], v[112:115]
ds_read_b64_tr_b16 a[164:165], v233 offset:160
v_mfma_f32_16x16x32_bf16 v[116:119], a[140:143], a[84:87], v[116:119]
ds_read_b64_tr_b16 a[166:167], v233 offset:4768
s_add_u32 m0, m0, 1024
v_mfma_f32_16x16x32_bf16 v[120:123], a[140:143], a[88:91], v[120:123]
buffer_load_dwordx4 v226, s[40:43], s64 offen lds
v_mfma_f32_16x16x32_bf16 v[124:127], a[140:143], a[92:95], v[124:127]
s_waitcnt lgkmcnt(10)
v_mfma_f32_16x16x32_bf16 v[128:131], a[144:147], a[80:83], v[128:131]
ds_read_b64_tr_b16 a[168:169], v233 offset:192
v_mfma_f32_16x16x32_bf16 v[132:135], a[144:147], a[84:87], v[132:135]
ds_read_b64_tr_b16 a[170:171], v233 offset:4800
v_mfma_f32_16x16x32_bf16 v[136:139], a[144:147], a[88:91], v[136:139]
v_mfma_f32_16x16x32_bf16 v[140:143], a[144:147], a[92:95], v[140:143]
s_waitcnt lgkmcnt(10)
v_mfma_f32_16x16x32_bf16 v[144:147], a[148:151], a[80:83], v[144:147]
ds_read_b64_tr_b16 a[172:173], v233 offset:224
v_mfma_f32_16x16x32_bf16 v[148:151], a[148:151], a[84:87], v[148:151]
ds_read_b64_tr_b16 a[174:175], v233 offset:4832
v_mfma_f32_16x16x32_bf16 v[152:155], a[148:151], a[88:91], v[152:155]
v_mfma_f32_16x16x32_bf16 v[156:159], a[148:151], a[92:95], v[156:159]
buffer_load_dwordx4 a[76:79], v235, s[44:47], s71 offen offset:3072 sc1
s_add_u32 s71, s71, 4096
s_waitcnt lgkmcnt(10)
v_mfma_f32_16x16x32_bf16 v[160:163], a[152:155], a[80:83], v[160:163]
ds_read_b64_tr_b16 a[176:177], v233 offset:256
v_mfma_f32_16x16x32_bf16 v[164:167], a[152:155], a[84:87], v[164:167]
ds_read_b64_tr_b16 a[178:179], v233 offset:4864
s_add_u32 m0, m0, 1024
v_mfma_f32_16x16x32_bf16 v[168:171], a[152:155], a[88:91], v[168:171]
buffer_load_dwordx4 v227, s[40:43], s64 offen lds
v_mfma_f32_16x16x32_bf16 v[172:175], a[152:155], a[92:95], v[172:175]
s_waitcnt lgkmcnt(10)
v_mfma_f32_16x16x32_bf16 v[176:179], a[156:159], a[80:83], v[176:179]
ds_read_b64_tr_b16 a[112:113], v234 offset:27648
v_mfma_f32_16x16x32_bf16 v[180:183], a[156:159], a[84:87], v[180:183]
ds_read_b64_tr_b16 a[114:115], v234 offset:32256
v_mfma_f32_16x16x32_bf16 v[184:187], a[156:159], a[88:91], v[184:187]
buffer_load_dwordx3 a[180:182], v235, s[56:59], s80 offen offset:0
s_add_u32 s80, s80, 1024
v_mfma_f32_16x16x32_bf16 v[188:191], a[156:159], a[92:95], v[188:191]
s_waitcnt lgkmcnt(10)
v_mfma_f32_16x16x32_bf16 v[192:195], a[160:163], a[80:83], v[192:195]
ds_read_b64_tr_b16 a[116:117], v234 offset:27656
v_mfma_f32_16x16x32_bf16 v[196:199], a[160:163], a[84:87], v[196:199]
ds_read_b64_tr_b16 a[118:119], v234 offset:32264
v_mfma_f32_16x16x32_bf16 v[200:203], a[160:163], a[88:91], v[200:203]
v_mfma_f32_16x16x32_bf16 v[204:207], a[160:163], a[92:95], v[204:207]
s_waitcnt lgkmcnt(10)
v_mfma_f32_16x16x32_bf16 v[208:211], a[164:167], a[80:83], v[208:211]
ds_read_b64_tr_b16 a[120:121], v234 offset:27712
v_mfma_f32_16x16x32_bf16 v[212:215], a[164:167], a[84:87], v[212:215]
ds_read_b64_tr_b16 a[122:123], v234 offset:32320
s_add_u32 m0, m0, 1024
v_mfma_f32_16x16x32_bf16 v[216:219], a[164:167], a[88:91], v[216:219]
buffer_load_dwordx4 v228, s[40:43], s64 offen lds
v_mfma_f32_16x16x32_bf16 v[220:223], a[164:167], a[92:95], v[220:223]
s_waitcnt lgkmcnt(10)
v_mfma_f32_16x16x32_bf16 a[0:3], a[168:171], a[80:83], a[0:3]
ds_read_b64_tr_b16 a[124:125], v234 offset:27720
v_mfma_f32_16x16x32_bf16 a[4:7], a[168:171], a[84:87], a[4:7]
ds_read_b64_tr_b16 a[126:127], v234 offset:32328
v_mfma_f32_16x16x32_bf16 a[8:11], a[168:171], a[88:91], a[8:11]
v_mfma_f32_16x16x32_bf16 a[12:15], a[168:171], a[92:95], a[12:15]
s_waitcnt lgkmcnt(10)
v_mfma_f32_16x16x32_bf16 a[16:19], a[172:175], a[80:83], a[16:19]
ds_read_b64_tr_b16 a[128:129], v234 offset:27776
v_mfma_f32_16x16x32_bf16 a[20:23], a[172:175], a[84:87], a[20:23]
ds_read_b64_tr_b16 a[130:131], v234 offset:32384
v_mfma_f32_16x16x32_bf16 a[24:27], a[172:175], a[88:91], a[24:27]
v_mfma_f32_16x16x32_bf16 a[28:31], a[172:175], a[92:95], a[28:31]
s_waitcnt lgkmcnt(10)
v_mfma_f32_16x16x32_bf16 a[32:35], a[176:179], a[80:83], a[32:35]
ds_read_b64_tr_b16 a[132:133], v234 offset:27784
v_mfma_f32_16x16x32_bf16 a[36:39], a[176:179], a[84:87], a[36:39]
ds_read_b64_tr_b16 a[134:135], v234 offset:32392
s_add_u32 m0, m0, 1024
v_mfma_f32_16x16x32_bf16 a[40:43], a[176:179], a[88:91], a[40:43]
buffer_load_dwordx4 v229, s[40:43], s64 offen lds
v_mfma_f32_16x16x32_bf16 a[44:47], a[176:179], a[92:95], a[44:47]
s_waitcnt vmcnt(41) lgkmcnt(10)
v_mfma_f32_16x16x32_bf16 v[0:3], a[112:115], a[96:99], v[0:3]
ds_read_b64_tr_b16 a[136:137], v234 offset:27840
v_mfma_f32_16x16x32_bf16 v[4:7], a[112:115], a[100:103], v[4:7]
ds_read_b64_tr_b16 a[138:139], v234 offset:32448
v_mfma_f32_16x16x32_bf16 v[8:11], a[112:115], a[104:107], v[8:11]
v_mfma_f32_16x16x32_bf16 v[12:15], a[112:115], a[108:111], v[12:15]
buffer_load_dwordx4 a[80:83], v235, s[44:47], s71 offen offset:0 sc1
s_waitcnt lgkmcnt(10)
v_mfma_f32_16x16x32_bf16 v[16:19], a[116:119], a[96:99], v[16:19]
ds_read_b64_tr_b16 a[140:141], v234 offset:27848
v_mfma_f32_16x16x32_bf16 v[20:23], a[116:119], a[100:103], v[20:23]
ds_read_b64_tr_b16 a[142:143], v234 offset:32456
v_mfma_f32_16x16x32_bf16 v[24:27], a[116:119], a[104:107], v[24:27]
v_mfma_f32_16x16x32_bf16 v[28:31], a[116:119], a[108:111], v[28:31]
s_waitcnt lgkmcnt(10)
v_mfma_f32_16x16x32_bf16 v[32:35], a[120:123], a[96:99], v[32:35]
ds_read_b64_tr_b16 a[144:145], v233 offset:9216
v_mfma_f32_16x16x32_bf16 v[36:39], a[120:123], a[100:103], v[36:39]
ds_read_b64_tr_b16 a[146:147], v233 offset:13824
s_add_u32 m0, m0, 1024
v_mfma_f32_16x16x32_bf16 v[40:43], a[120:123], a[104:107], v[40:43]
buffer_load_dwordx4 v230, s[40:43], s64 offen lds
v_mfma_f32_16x16x32_bf16 v[44:47], a[120:123], a[108:111], v[44:47]
s_waitcnt lgkmcnt(10)
v_mfma_f32_16x16x32_bf16 v[48:51], a[124:127], a[96:99], v[48:51]
ds_read_b64_tr_b16 a[148:149], v233 offset:9248
v_mfma_f32_16x16x32_bf16 v[52:55], a[124:127], a[100:103], v[52:55]
ds_read_b64_tr_b16 a[150:151], v233 offset:13856
v_mfma_f32_16x16x32_bf16 v[56:59], a[124:127], a[104:107], v[56:59]
v_mfma_f32_16x16x32_bf16 v[60:63], a[124:127], a[108:111], v[60:63]
buffer_load_dwordx4 a[84:87], v235, s[44:47], s71 offen offset:1024 sc1
s_waitcnt lgkmcnt(10)
v_mfma_f32_16x16x32_bf16 v[64:67], a[128:131], a[96:99], v[64:67]
ds_read_b64_tr_b16 a[152:153], v233 offset:9280
)"
R"(
v_mfma_f32_16x16x32_bf16 v[68:71], a[128:131], a[100:103], v[68:71]
ds_read_b64_tr_b16 a[154:155], v233 offset:13888
v_mfma_f32_16x16x32_bf16 v[72:75], a[128:131], a[104:107], v[72:75]
v_mfma_f32_16x16x32_bf16 v[76:79], a[128:131], a[108:111], v[76:79]
s_waitcnt lgkmcnt(10)
v_mfma_f32_16x16x32_bf16 v[80:83], a[132:135], a[96:99], v[80:83]
ds_read_b64_tr_b16 a[156:157], v233 offset:9312
v_mfma_f32_16x16x32_bf16 v[84:87], a[132:135], a[100:103], v[84:87]
ds_read_b64_tr_b16 a[158:159], v233 offset:13920
s_add_u32 m0, m0, 1024
v_mfma_f32_16x16x32_bf16 v[88:91], a[132:135], a[104:107], v[88:91]
buffer_load_dwordx4 v231, s[40:43], s64 offen lds
v_mfma_f32_16x16x32_bf16 v[92:95], a[132:135], a[108:111], v[92:95]
s_waitcnt lgkmcnt(10)
v_mfma_f32_16x16x32_bf16 v[96:99], a[136:139], a[96:99], v[96:99]
ds_read_b64_tr_b16 a[160:161], v233 offset:9344
v_mfma_f32_16x16x32_bf16 v[100:103], a[136:139], a[100:103], v[100:103]
ds_read_b64_tr_b16 a[162:163], v233 offset:13952
v_mfma_f32_16x16x32_bf16 v[104:107], a[136:139], a[104:107], v[104:107]
v_mfma_f32_16x16x32_bf16 v[108:111], a[136:139], a[108:111], v[108:111]
buffer_load_dwordx4 a[88:91], v235, s[44:47], s71 offen offset:2048 sc1
s_waitcnt lgkmcnt(10)
v_mfma_f32_16x16x32_bf16 v[112:115], a[140:143], a[96:99], v[112:115]
ds_read_b64_tr_b16 a[164:165], v233 offset:9376
v_mfma_f32_16x16x32_bf16 v[116:119], a[140:143], a[100:103], v[116:119]
ds_read_b64_tr_b16 a[166:167], v233 offset:13984
v_mfma_f32_16x16x32_bf16 v[120:123], a[140:143], a[104:107], v[120:123]
v_mfma_f32_16x16x32_bf16 v[124:127], a[140:143], a[108:111], v[124:127]
s_waitcnt lgkmcnt(10)
v_mfma_f32_16x16x32_bf16 v[128:131], a[144:147], a[96:99], v[128:131]
ds_read_b64_tr_b16 a[168:169], v233 offset:9408
v_mfma_f32_16x16x32_bf16 v[132:135], a[144:147], a[100:103], v[132:135]
ds_read_b64_tr_b16 a[170:171], v233 offset:14016
s_add_u32 m0, m0, 1024
v_mfma_f32_16x16x32_bf16 v[136:139], a[144:147], a[104:107], v[136:139]
buffer_load_dwordx4 v232, s[40:43], s64 offen lds
v_mfma_f32_16x16x32_bf16 v[140:143], a[144:147], a[108:111], v[140:143]
s_waitcnt lgkmcnt(10)
v_mfma_f32_16x16x32_bf16 v[144:147], a[148:151], a[96:99], v[144:147]
ds_read_b64_tr_b16 a[172:173], v233 offset:9440
v_mfma_f32_16x16x32_bf16 v[148:151], a[148:151], a[100:103], v[148:151]
ds_read_b64_tr_b16 a[174:175], v233 offset:14048
v_mfma_f32_16x16x32_bf16 v[152:155], a[148:151], a[104:107], v[152:155]
v_mfma_f32_16x16x32_bf16 v[156:159], a[148:151], a[108:111], v[156:159]
s_add_u32 s64, s64, s65
s_add_u32 s69, s69, 36864
s_cmp_eq_u32 s69, 147456
s_cselect_b32 s69, 0, s69
buffer_load_dwordx4 a[92:95], v235, s[44:47], s71 offen offset:3072 sc1
s_add_u32 s71, s71, 4096
s_waitcnt lgkmcnt(10)
v_mfma_f32_16x16x32_bf16 v[160:163], a[152:155], a[96:99], v[160:163]
ds_read_b64_tr_b16 a[176:177], v233 offset:9472
v_mfma_f32_16x16x32_bf16 v[164:167], a[152:155], a[100:103], v[164:167]
ds_read_b64_tr_b16 a[178:179], v233 offset:14080
v_mfma_f32_16x16x32_bf16 v[168:171], a[152:155], a[104:107], v[168:171]
v_mfma_f32_16x16x32_bf16 v[172:175], a[152:155], a[108:111], v[172:175]
s_waitcnt lgkmcnt(10)
v_mfma_f32_16x16x32_bf16 v[176:179], a[156:159], a[96:99], v[176:179]
s_add_u32 s67, s67, 36864
s_cmp_eq_u32 s67, 147456
s_cselect_b32 s66, s76, 36864
s_cselect_b32 s67, 0, s67
v_mfma_f32_16x16x32_bf16 v[180:183], a[156:159], a[100:103], v[180:183]
v_mfma_f32_16x16x32_bf16 v[184:187], a[156:159], a[104:107], v[184:187]
v_mfma_f32_16x16x32_bf16 v[188:191], a[156:159], a[108:111], v[188:191]
s_waitcnt lgkmcnt(8)
v_mfma_f32_16x16x32_bf16 v[192:195], a[160:163], a[96:99], v[192:195]
v_add_u32 v233, s66, v233
v_mfma_f32_16x16x32_bf16 v[196:199], a[160:163], a[100:103], v[196:199]
v_add_u32 v234, s66, v234
v_mfma_f32_16x16x32_bf16 v[200:203], a[160:163], a[104:107], v[200:203]
v_mfma_f32_16x16x32_bf16 v[204:207], a[160:163], a[108:111], v[204:207]
s_waitcnt vmcnt(39) lgkmcnt(0)
s_barrier
v_mfma_f32_16x16x32_bf16 v[208:211], a[164:167], a[96:99], v[208:211]
ds_read_b64_tr_b16 a[112:113], v234 offset:18432
ds_read_b64_tr_b16 a[114:115], v234 offset:23040
v_mfma_f32_16x16x32_bf16 v[212:215], a[164:167], a[100:103], v[212:215]
ds_read_b64_tr_b16 a[116:117], v234 offset:18440
ds_read_b64_tr_b16 a[118:119], v234 offset:23048
v_mfma_f32_16x16x32_bf16 v[216:219], a[164:167], a[104:107], v[216:219]
v_mfma_f32_16x16x32_bf16 v[220:223], a[164:167], a[108:111], v[220:223]
s_waitcnt lgkmcnt(8)
v_mfma_f32_16x16x32_bf16 a[0:3], a[168:171], a[96:99], a[0:3]
ds_read_b64_tr_b16 a[120:121], v234 offset:18496
ds_read_b64_tr_b16 a[122:123], v234 offset:23104
v_mfma_f32_16x16x32_bf16 a[4:7], a[168:171], a[100:103], a[4:7]
ds_read_b64_tr_b16 a[124:125], v234 offset:18504
ds_read_b64_tr_b16 a[126:127], v234 offset:23112
v_mfma_f32_16x16x32_bf16 a[8:11], a[168:171], a[104:107], a[8:11]
v_mfma_f32_16x16x32_bf16 a[12:15], a[168:171], a[108:111], a[12:15]
s_waitcnt lgkmcnt(10)
v_mfma_f32_16x16x32_bf16 a[16:19], a[172:175], a[96:99], a[16:19]
ds_read_b64_tr_b16 a[128:129], v234 offset:18560
v_mfma_f32_16x16x32_bf16 a[20:23], a[172:175], a[100:103], a[20:23]
ds_read_b64_tr_b16 a[130:131], v234 offset:23168
v_mfma_f32_16x16x32_bf16 a[24:27], a[172:175], a[104:107], a[24:27]
v_mfma_f32_16x16x32_bf16 a[28:31], a[172:175], a[108:111], a[28:31]
s_waitcnt lgkmcnt(10)
v_mfma_f32_16x16x32_bf16 a[32:35], a[176:179], a[96:99], a[32:35]
ds_read_b64_tr_b16 a[132:133], v234 offset:18568
v_mfma_f32_16x16x32_bf16 a[36:39], a[176:179], a[100:103], a[36:39]
ds_read_b64_tr_b16 a[134:135], v234 offset:23176
v_mfma_f32_16x16x32_bf16 a[40:43], a[176:179], a[104:107], a[40:43]
v_mfma_f32_16x16x32_bf16 a[44:47], a[176:179], a[108:111], a[44:47]
s_waitcnt vmcnt(38) lgkmcnt(10)
v_mfma_f32_16x16x32_bf16 v[0:3], a[112:115], a[192:195], v[0:3]
ds_read_b64_tr_b16 a[136:137], v234 offset:18624
v_accvgpr_read_b32 v224, a183
v_accvgpr_read_b32 v225, a184
v_mfma_f32_16x16x32_bf16 v[4:7], a[112:115], a[196:199], v[4:7]
ds_read_b64_tr_b16 a[138:139], v234 offset:23232
v_accvgpr_read_b32 v226, a185
v_accvgpr_read_b32 v227, a186
v_mfma_f32_16x16x32_bf16 v[8:11], a[112:115], a[200:203], v[8:11]
v_accvgpr_read_b32 v228, a187
v_accvgpr_read_b32 v229, a188
v_mfma_f32_16x16x32_bf16 v[12:15], a[112:115], a[204:207], v[12:15]
v_accvgpr_read_b32 v230, a189
v_accvgpr_read_b32 v231, a190
v_accvgpr_read_b32 v232, a191
s_mov_b64 s[40:41], s[28:29]
s_and_b32 s41, s41, 65535
s_mov_b32 s42, 131072
s_and_b32 s64, %[wid], 1
s_lshl_b32 s64, s64, 13
s_mov_b32 s65, 16384
buffer_load_dwordx4 a[96:99], v235, s[44:47], s71 offen offset:0 sc1
s_waitcnt lgkmcnt(10)
v_mfma_f32_16x16x32_bf16 v[16:19], a[116:119], a[192:195], v[16:19]
ds_read_b64_tr_b16 a[140:141], v234 offset:18632
v_mfma_f32_16x16x32_bf16 v[20:23], a[116:119], a[196:199], v[20:23]
ds_read_b64_tr_b16 a[142:143], v234 offset:23240
s_add_u32 m0, s70, s69
v_mfma_f32_16x16x32_bf16 v[24:27], a[116:119], a[200:203], v[24:27]
buffer_load_dwordx4 v224, s[40:43], s64 offen lds
v_mfma_f32_16x16x32_bf16 v[28:31], a[116:119], a[204:207], v[28:31]
s_waitcnt lgkmcnt(10)
v_mfma_f32_16x16x32_bf16 v[32:35], a[120:123], a[192:195], v[32:35]
ds_read_b64_tr_b16 a[144:145], v233 offset:0
v_mfma_f32_16x16x32_bf16 v[36:39], a[120:123], a[196:199], v[36:39]
ds_read_b64_tr_b16 a[146:147], v233 offset:4608
v_mfma_f32_16x16x32_bf16 v[40:43], a[120:123], a[200:203], v[40:43]
v_mfma_f32_16x16x32_bf16 v[44:47], a[120:123], a[204:207], v[44:47]
s_waitcnt lgkmcnt(10)
v_mfma_f32_16x16x32_bf16 v[48:51], a[124:127], a[192:195], v[48:51]
ds_read_b64_tr_b16 a[148:149], v233 offset:32
v_mfma_f32_16x16x32_bf16 v[52:55], a[124:127], a[196:199], v[52:55]
ds_read_b64_tr_b16 a[150:151], v233 offset:4640
v_mfma_f32_16x16x32_bf16 v[56:59], a[124:127], a[200:203], v[56:59]
v_mfma_f32_16x16x32_bf16 v[60:63], a[124:127], a[204:207], v[60:63]
buffer_load_dwordx4 a[100:103], v235, s[44:47], s71 offen offset:1024 sc1
s_waitcnt lgkmcnt(10)
v_mfma_f32_16x16x32_bf16 v[64:67], a[128:131], a[192:195], v[64:67]
ds_read_b64_tr_b16 a[152:153], v233 offset:64
v_mfma_f32_16x16x32_bf16 v[68:71], a[128:131], a[196:199], v[68:71]
ds_read_b64_tr_b16 a[154:155], v233 offset:4672
s_add_u32 m0, m0, 1024
v_mfma_f32_16x16x32_bf16 v[72:75], a[128:131], a[200:203], v[72:75]
buffer_load_dwordx4 v225, s[40:43], s64 offen lds
v_mfma_f32_16x16x32_bf16 v[76:79], a[128:131], a[204:207], v[76:79]
s_waitcnt lgkmcnt(10)
v_mfma_f32_16x16x32_bf16 v[80:83], a[132:135], a[192:195], v[80:83]
ds_read_b64_tr_b16 a[156:157], v233 offset:96
v_mfma_f32_16x16x32_bf16 v[84:87], a[132:135], a[196:199], v[84:87]
ds_read_b64_tr_b16 a[158:159], v233 offset:4704
v_mfma_f32_16x16x32_bf16 v[88:91], a[132:135], a[200:203], v[88:91]
v_mfma_f32_16x16x32_bf16 v[92:95], a[132:135], a[204:207], v[92:95]
s_waitcnt lgkmcnt(10)
v_mfma_f32_16x16x32_bf16 v[96:99], a[136:139], a[192:195], v[96:99]
ds_read_b64_tr_b16 a[160:161], v233 offset:128
v_mfma_f32_16x16x32_bf16 v[100:103], a[136:139], a[196:199], v[100:103]
ds_read_b64_tr_b16 a[162:163], v233 offset:4736
v_mfma_f32_16x16x32_bf16 v[104:107], a[136:139], a[200:203], v[104:107]
v_mfma_f32_16x16x32_bf16 v[108:111], a[136:139], a[204:207], v[108:111]
buffer_load_dwordx4 a[104:107], v235, s[44:47], s71 offen offset:2048 sc1
s_waitcnt lgkmcnt(10)
v_mfma_f32_16x16x32_bf16 v[112:115], a[140:143], a[192:195], v[112:115]
ds_read_b64_tr_b16 a[164:165], v233 offset:160
v_mfma_f32_16x16x32_bf16 v[116:119], a[140:143], a[196:199], v[116:119]
ds_read_b64_tr_b16 a[166:167], v233 offset:4768
s_add_u32 m0, m0, 1024
v_mfma_f32_16x16x32_bf16 v[120:123], a[140:143], a[200:203], v[120:123]
buffer_load_dwordx4 v226, s[40:43], s64 offen lds
v_mfma_f32_16x16x32_bf16 v[124:127], a[140:143], a[204:207], v[124:127]
s_waitcnt lgkmcnt(10)
v_mfma_f32_16x16x32_bf16 v[128:131], a[144:147], a[192:195], v[128:131]
ds_read_b64_tr_b16 a[168:169], v233 offset:192
v_mfma_f32_16x16x32_bf16 v[132:135], a[144:147], a[196:199], v[132:135]
ds_read_b64_tr_b16 a[170:171], v233 offset:4800
v_mfma_f32_16x16x32_bf16 v[136:139], a[144:147], a[200:203], v[136:139]
v_mfma_f32_16x16x32_bf16 v[140:143], a[144:147], a[204:207], v[140:143]
s_waitcnt lgkmcnt(10)
v_mfma_f32_16x16x32_bf16 v[144:147], a[148:151], a[192:195], v[144:147]
ds_read_b64_tr_b16 a[172:173], v233 offset:224
v_mfma_f32_16x16x32_bf16 v[148:151], a[148:151], a[196:199], v[148:151]
ds_read_b64_tr_b16 a[174:175], v233 offset:4832
v_mfma_f32_16x16x32_bf16 v[152:155], a[148:151], a[200:203], v[152:155]
v_mfma_f32_16x16x32_bf16 v[156:159], a[148:151], a[204:207], v[156:159]
buffer_load_dwordx4 a[108:111], v235, s[44:47], s71 offen offset:3072 sc1
s_add_u32 s71, s71, 4096
s_waitcnt lgkmcnt(10)
v_mfma_f32_16x16x32_bf16 v[160:163], a[152:155], a[192:195], v[160:163]
ds_read_b64_tr_b16 a[176:177], v233 offset:256
v_mfma_f32_16x16x32_bf16 v[164:167], a[152:155], a[196:199], v[164:167]
ds_read_b64_tr_b16 a[178:179], v233 offset:4864
s_add_u32 m0, m0, 1024
v_mfma_f32_16x16x32_bf16 v[168:171], a[152:155], a[200:203], v[168:171]
buffer_load_dwordx4 v227, s[40:43], s64 offen lds
v_mfma_f32_16x16x32_bf16 v[172:175], a[152:155], a[204:207], v[172:175]
s_waitcnt lgkmcnt(10)
v_mfma_f32_16x16x32_bf16 v[176:179], a[156:159], a[192:195], v[176:179]
ds_read_b64_tr_b16 a[112:113], v234 offset:27648
v_mfma_f32_16x16x32_bf16 v[180:183], a[156:159], a[196:199], v[180:183]
ds_read_b64_tr_b16 a[114:115], v234 offset:32256
v_mfma_f32_16x16x32_bf16 v[184:187], a[156:159], a[200:203], v[184:187]
v_mfma_f32_16x16x32_bf16 v[188:191], a[156:159], a[204:207], v[188:191]
s_waitcnt lgkmcnt(10)
v_mfma_f32_16x16x32_bf16 v[192:195], a[160:163], a[192:195], v[192:195]
ds_read_b64_tr_b16 a[116:117], v234 offset:27656
v_mfma_f32_16x16x32_bf16 v[196:199], a[160:163], a[196:199], v[196:199]
ds_read_b64_tr_b16 a[118:119], v234 offset:32264
v_mfma_f32_16x16x32_bf16 v[200:203], a[160:163], a[200:203], v[200:203]
v_mfma_f32_16x16x32_bf16 v[204:207], a[160:163], a[204:207], v[204:207]
s_waitcnt lgkmcnt(10)
v_mfma_f32_16x16x32_bf16 v[208:211], a[164:167], a[192:195], v[208:211]
ds_read_b64_tr_b16 a[120:121], v234 offset:27712
v_mfma_f32_16x16x32_bf16 v[212:215], a[164:167], a[196:199], v[212:215]
ds_read_b64_tr_b16 a[122:123], v234 offset:32320
s_add_u32 m0, m0, 1024
v_mfma_f32_16x16x32_bf16 v[216:219], a[164:167], a[200:203], v[216:219]
buffer_load_dwordx4 v228, s[40:43], s64 offen lds
v_mfma_f32_16x16x32_bf16 v[220:223], a[164:167], a[204:207], v[220:223]
s_waitcnt lgkmcnt(10)
v_mfma_f32_16x16x32_bf16 a[0:3], a[168:171], a[192:195], a[0:3]
ds_read_b64_tr_b16 a[124:125], v234 offset:27720
v_mfma_f32_16x16x32_bf16 a[4:7], a[168:171], a[196:199], a[4:7]
ds_read_b64_tr_b16 a[126:127], v234 offset:32328
v_mfma_f32_16x16x32_bf16 a[8:11], a[168:171], a[200:203], a[8:11]
v_mfma_f32_16x16x32_bf16 a[12:15], a[168:171], a[204:207], a[12:15]
s_waitcnt lgkmcnt(10)
v_mfma_f32_16x16x32_bf16 a[16:19], a[172:175], a[192:195], a[16:19]
ds_read_b64_tr_b16 a[128:129], v234 offset:27776
v_mfma_f32_16x16x32_bf16 a[20:23], a[172:175], a[196:199], a[20:23]
ds_read_b64_tr_b16 a[130:131], v234 offset:32384
v_mfma_f32_16x16x32_bf16 a[24:27], a[172:175], a[200:203], a[24:27]
v_mfma_f32_16x16x32_bf16 a[28:31], a[172:175], a[204:207], a[28:31]
s_waitcnt lgkmcnt(10)
v_mfma_f32_16x16x32_bf16 a[32:35], a[176:179], a[192:195], a[32:35]
ds_read_b64_tr_b16 a[132:133], v234 offset:27784
v_mfma_f32_16x16x32_bf16 a[36:39], a[176:179], a[196:199], a[36:39]
ds_read_b64_tr_b16 a[134:135], v234 offset:32392
s_add_u32 m0, m0, 1024
v_mfma_f32_16x16x32_bf16 a[40:43], a[176:179], a[200:203], a[40:43]
buffer_load_dwordx4 v229, s[40:43], s64 offen lds
v_mfma_f32_16x16x32_bf16 a[44:47], a[176:179], a[204:207], a[44:47]
s_waitcnt vmcnt(40) lgkmcnt(10)
v_mfma_f32_16x16x32_bf16 v[0:3], a[112:115], a[208:211], v[0:3]
ds_read_b64_tr_b16 a[136:137], v234 offset:27840
v_mfma_f32_16x16x32_bf16 v[4:7], a[112:115], a[212:215], v[4:7]
ds_read_b64_tr_b16 a[138:139], v234 offset:32448
v_mfma_f32_16x16x32_bf16 v[8:11], a[112:115], a[216:219], v[8:11]
v_mfma_f32_16x16x32_bf16 v[12:15], a[112:115], a[220:223], v[12:15]
s_waitcnt lgkmcnt(10)
v_mfma_f32_16x16x32_bf16 v[16:19], a[116:119], a[208:211], v[16:19]
ds_read_b64_tr_b16 a[140:141], v234 offset:27848
v_mfma_f32_16x16x32_bf16 v[20:23], a[116:119], a[212:215], v[20:23]
ds_read_b64_tr_b16 a[142:143], v234 offset:32456
v_mfma_f32_16x16x32_bf16 v[24:27], a[116:119], a[216:219], v[24:27]
v_mfma_f32_16x16x32_bf16 v[28:31], a[116:119], a[220:223], v[28:31]
s_waitcnt lgkmcnt(10)
v_mfma_f32_16x16x32_bf16 v[32:35], a[120:123], a[208:211], v[32:35]
ds_read_b64_tr_b16 a[144:145], v233 offset:9216
v_mfma_f32_16x16x32_bf16 v[36:39], a[120:123], a[212:215], v[36:39]
ds_read_b64_tr_b16 a[146:147], v233 offset:13824
s_add_u32 m0, m0, 1024
v_mfma_f32_16x16x32_bf16 v[40:43], a[120:123], a[216:219], v[40:43]
buffer_load_dwordx4 v230, s[40:43], s64 offen lds
v_mfma_f32_16x16x32_bf16 v[44:47], a[120:123], a[220:223], v[44:47]
s_waitcnt lgkmcnt(10)
v_mfma_f32_16x16x32_bf16 v[48:51], a[124:127], a[208:211], v[48:51]
ds_read_b64_tr_b16 a[148:149], v233 offset:9248
v_mfma_f32_16x16x32_bf16 v[52:55], a[124:127], a[212:215], v[52:55]
ds_read_b64_tr_b16 a[150:151], v233 offset:13856
v_mfma_f32_16x16x32_bf16 v[56:59], a[124:127], a[216:219], v[56:59]
v_mfma_f32_16x16x32_bf16 v[60:63], a[124:127], a[220:223], v[60:63]
s_waitcnt lgkmcnt(10)
v_mfma_f32_16x16x32_bf16 v[64:67], a[128:131], a[208:211], v[64:67]
ds_read_b64_tr_b16 a[152:153], v233 offset:9280
v_mfma_f32_16x16x32_bf16 v[68:71], a[128:131], a[212:215], v[68:71]
ds_read_b64_tr_b16 a[154:155], v233 offset:13888
v_mfma_f32_16x16x32_bf16 v[72:75], a[128:131], a[216:219], v[72:75]
v_mfma_f32_16x16x32_bf16 v[76:79], a[128:131], a[220:223], v[76:79]
s_waitcnt lgkmcnt(10)
v_mfma_f32_16x16x32_bf16 v[80:83], a[132:135], a[208:211], v[80:83]
ds_read_b64_tr_b16 a[156:157], v233 offset:9312
v_mfma_f32_16x16x32_bf16 v[84:87], a[132:135], a[212:215], v[84:87]
ds_read_b64_tr_b16 a[158:159], v233 offset:13920
s_add_u32 m0, m0, 1024
v_mfma_f32_16x16x32_bf16 v[88:91], a[132:135], a[216:219], v[88:91]
buffer_load_dwordx4 v231, s[40:43], s64 offen lds
v_mfma_f32_16x16x32_bf16 v[92:95], a[132:135], a[220:223], v[92:95]
s_waitcnt lgkmcnt(10)
v_mfma_f32_16x16x32_bf16 v[96:99], a[136:139], a[208:211], v[96:99]
ds_read_b64_tr_b16 a[160:161], v233 offset:9344
v_mfma_f32_16x16x32_bf16 v[100:103], a[136:139], a[212:215], v[100:103]
ds_read_b64_tr_b16 a[162:163], v233 offset:13952
v_mfma_f32_16x16x32_bf16 v[104:107], a[136:139], a[216:219], v[104:107]
v_mfma_f32_16x16x32_bf16 v[108:111], a[136:139], a[220:223], v[108:111]
s_waitcnt lgkmcnt(10)
v_mfma_f32_16x16x32_bf16 v[112:115], a[140:143], a[208:211], v[112:115]
ds_read_b64_tr_b16 a[164:165], v233 offset:9376
v_mfma_f32_16x16x32_bf16 v[116:119], a[140:143], a[212:215], v[116:119]
ds_read_b64_tr_b16 a[166:167], v233 offset:13984
v_mfma_f32_16x16x32_bf16 v[120:123], a[140:143], a[216:219], v[120:123]
v_mfma_f32_16x16x32_bf16 v[124:127], a[140:143], a[220:223], v[124:127]
s_waitcnt lgkmcnt(10)
v_mfma_f32_16x16x32_bf16 v[128:131], a[144:147], a[208:211], v[128:131]
ds_read_b64_tr_b16 a[168:169], v233 offset:9408
v_mfma_f32_16x16x32_bf16 v[132:135], a[144:147], a[212:215], v[132:135]
ds_read_b64_tr_b16 a[170:171], v233 offset:14016
s_add_u32 m0, m0, 1024
v_mfma_f32_16x16x32_bf16 v[136:139], a[144:147], a[216:219], v[136:139]
buffer_load_dwordx4 v232, s[40:43], s64 offen lds
v_mfma_f32_16x16x32_bf16 v[140:143], a[144:147], a[220:223], v[140:143]
s_waitcnt lgkmcnt(10)
v_mfma_f32_16x16x32_bf16 v[144:147], a[148:151], a[208:211], v[144:147]
ds_read_b64_tr_b16 a[172:173], v233 offset:9440
v_mfma_f32_16x16x32_bf16 v[148:151], a[148:151], a[212:215], v[148:151]
ds_read_b64_tr_b16 a[174:175], v233 offset:14048
v_mfma_f32_16x16x32_bf16 v[152:155], a[148:151], a[216:219], v[152:155]
v_mfma_f32_16x16x32_bf16 v[156:159], a[148:151], a[220:223], v[156:159]
s_add_u32 s64, s64, s65
s_add_u32 s69, s69, 36864
s_cmp_eq_u32 s69, 147456
s_cselect_b32 s69, 0, s69
s_waitcnt lgkmcnt(10)
v_mfma_f32_16x16x32_bf16 v[160:163], a[152:155], a[208:211], v[160:163]
ds_read_b64_tr_b16 a[176:177], v233 offset:9472
v_mfma_f32_16x16x32_bf16 v[164:167], a[152:155], a[212:215], v[164:167]
ds_read_b64_tr_b16 a[178:179], v233 offset:14080
v_mfma_f32_16x16x32_bf16 v[168:171], a[152:155], a[216:219], v[168:171]
v_mfma_f32_16x16x32_bf16 v[172:175], a[152:155], a[220:223], v[172:175]
s_waitcnt lgkmcnt(10)
v_mfma_f32_16x16x32_bf16 v[176:179], a[156:159], a[208:211], v[176:179]
s_add_u32 s67, s67, 36864
s_cmp_eq_u32 s67, 147456
s_cselect_b32 s66, s76, 36864
s_cselect_b32 s67, 0, s67
v_mfma_f32_16x16x32_bf16 v[180:183], a[156:159], a[212:215], v[180:183]
v_mfma_f32_16x16x32_bf16 v[184:187], a[156:159], a[216:219], v[184:187]
v_mfma_f32_16x16x32_bf16 v[188:191], a[156:159], a[220:223], v[188:191]
s_waitcnt lgkmcnt(8)
v_mfma_f32_16x16x32_bf16 v[192:195], a[160:163], a[208:211], v[192:195]
v_add_u32 v233, s66, v233
v_mfma_f32_16x16x32_bf16 v[196:199], a[160:163], a[212:215], v[196:199]
v_add_u32 v234, s66, v234
v_mfma_f32_16x16x32_bf16 v[200:203], a[160:163], a[216:219], v[200:203]
v_mfma_f32_16x16x32_bf16 v[204:207], a[160:163], a[220:223], v[204:207]
s_waitcnt vmcnt(33) lgkmcnt(0)
s_barrier
v_mfma_f32_16x16x32_bf16 v[208:211], a[164:167], a[208:211], v[208:211]
ds_read_b64_tr_b16 a[112:113], v233 offset:0
ds_read_b64_tr_b16 a[114:115], v233 offset:4608
v_mfma_f32_16x16x32_bf16 v[212:215], a[164:167], a[212:215], v[212:215]
ds_read_b64_tr_b16 a[116:117], v233 offset:9216
ds_read_b64_tr_b16 a[118:119], v233 offset:13824
v_mfma_f32_16x16x32_bf16 v[216:219], a[164:167], a[216:219], v[216:219]
s_mov_b32 s56, s84
s_mov_b32 s57, s85
s_mov_b32 s58, s94
s_mov_b32 s80, s95
v_mfma_f32_16x16x32_bf16 v[220:223], a[164:167], a[220:223], v[220:223]
s_waitcnt lgkmcnt(8)
v_mfma_f32_16x16x32_bf16 a[0:3], a[168:171], a[208:211], a[0:3]
ds_read_b64_tr_b16 a[120:121], v233 offset:32
ds_read_b64_tr_b16 a[122:123], v233 offset:4640
v_mfma_f32_16x16x32_bf16 a[4:7], a[168:171], a[212:215], a[4:7]
ds_read_b64_tr_b16 a[124:125], v233 offset:9248
ds_read_b64_tr_b16 a[126:127], v233 offset:13856
v_mfma_f32_16x16x32_bf16 a[8:11], a[168:171], a[216:219], a[8:11]
v_mfma_f32_16x16x32_bf16 a[12:15], a[168:171], a[220:223], a[12:15]
s_waitcnt lgkmcnt(10)
v_mfma_f32_16x16x32_bf16 a[16:19], a[172:175], a[208:211], a[16:19]
ds_read_b64_tr_b16 a[128:129], v233 offset:64
v_mfma_f32_16x16x32_bf16 a[20:23], a[172:175], a[212:215], a[20:23]
ds_read_b64_tr_b16 a[130:131], v233 offset:4672
v_mfma_f32_16x16x32_bf16 a[24:27], a[172:175], a[216:219], a[24:27]
v_mfma_f32_16x16x32_bf16 a[28:31], a[172:175], a[220:223], a[28:31]
s_waitcnt lgkmcnt(10)
v_mfma_f32_16x16x32_bf16 a[32:35], a[176:179], a[208:211], a[32:35]
ds_read_b64_tr_b16 a[132:133], v233 offset:9280
v_mfma_f32_16x16x32_bf16 a[36:39], a[176:179], a[212:215], a[36:39]
ds_read_b64_tr_b16 a[134:135], v233 offset:13888
v_mfma_f32_16x16x32_bf16 a[40:43], a[176:179], a[216:219], a[40:43]
v_mfma_f32_16x16x32_bf16 a[44:47], a[176:179], a[220:223], a[44:47]
s_waitcnt vmcnt(32) lgkmcnt(10)
v_mfma_f32_16x16x32_bf16 v[128:131], a[112:115], a[48:51], v[128:131]
ds_read_b64_tr_b16 a[136:137], v233 offset:96
)"
R"(
s_add_u32 s67, s67, 36864
s_cmp_eq_u32 s67, 147456
s_cselect_b32 s66, s76, 36864
s_cselect_b32 s67, 0, s67
v_mfma_f32_16x16x32_bf16 v[132:135], a[112:115], a[52:55], v[132:135]
ds_read_b64_tr_b16 a[138:139], v233 offset:4704
v_mfma_f32_16x16x32_bf16 v[136:139], a[112:115], a[56:59], v[136:139]
v_add_u32 v238, s66, v233
v_add_u32 v239, s66, v234
v_mfma_f32_16x16x32_bf16 v[140:143], a[112:115], a[60:63], v[140:143]
s_waitcnt vmcnt(24) lgkmcnt(10)
v_mfma_f32_16x16x32_bf16 v[128:131], a[116:119], a[64:67], v[128:131]
ds_read_b64_tr_b16 a[140:141], v233 offset:9312
v_mfma_f32_16x16x32_bf16 v[132:135], a[116:119], a[68:71], v[132:135]
ds_read_b64_tr_b16 a[142:143], v233 offset:13920
v_mfma_f32_16x16x32_bf16 v[136:139], a[116:119], a[72:75], v[136:139]
s_waitcnt vmcnt(14)
s_barrier
v_mfma_f32_16x16x32_bf16 v[140:143], a[116:119], a[76:79], v[140:143]
s_waitcnt lgkmcnt(10)
v_mfma_f32_16x16x32_bf16 v[144:147], a[120:123], a[48:51], v[144:147]
ds_read_b64_tr_b16 a[144:145], v238 offset:0
v_mfma_f32_16x16x32_bf16 v[148:151], a[120:123], a[52:55], v[148:151]
ds_read_b64_tr_b16 a[146:147], v238 offset:4608
v_mfma_f32_16x16x32_bf16 v[152:155], a[120:123], a[56:59], v[152:155]
v_mfma_f32_16x16x32_bf16 v[156:159], a[120:123], a[60:63], v[156:159]
s_mul_i32 s75, s6, 9
s_add_u32 s75, s75, 257
s_lshr_b32 s75, s75, 1
s_lshl_b32 s75, s75, 12
s_and_b32 s77, s35, 1
s_cselect_b32 s38, -4088, s77
s_cselect_b32 s77, 0, 16384
s_add_u32 s38, s38, s77
s_add_u32 s38, s38, s75
s_waitcnt lgkmcnt(10)
v_mfma_f32_16x16x32_bf16 v[144:147], a[124:127], a[64:67], v[144:147]
ds_read_b64_tr_b16 a[148:149], v238 offset:9216
v_mfma_f32_16x16x32_bf16 v[148:151], a[124:127], a[68:71], v[148:151]
ds_read_b64_tr_b16 a[150:151], v238 offset:13824
s_add_u32 m0, s70, s69
v_mfma_f32_16x16x32_bf16 v[152:155], a[124:127], a[72:75], v[152:155]
buffer_load_dwordx4 v224, s[40:43], s64 offen lds
v_mfma_f32_16x16x32_bf16 v[156:159], a[124:127], a[76:79], v[156:159]
s_waitcnt lgkmcnt(10)
v_mfma_f32_16x16x32_bf16 v[160:163], a[128:131], a[48:51], v[160:163]
ds_read_b64_tr_b16 a[152:153], v238 offset:32
v_mfma_f32_16x16x32_bf16 v[164:167], a[128:131], a[52:55], v[164:167]
ds_read_b64_tr_b16 a[154:155], v238 offset:4640
s_add_u32 m0, m0, 1024
v_mfma_f32_16x16x32_bf16 v[168:171], a[128:131], a[56:59], v[168:171]
buffer_load_dwordx4 v225, s[40:43], s64 offen lds
v_mfma_f32_16x16x32_bf16 v[172:175], a[128:131], a[60:63], v[172:175]
s_waitcnt lgkmcnt(10)
v_mfma_f32_16x16x32_bf16 v[160:163], a[132:135], a[64:67], v[160:163]
ds_read_b64_tr_b16 a[156:157], v238 offset:9248
v_mfma_f32_16x16x32_bf16 v[164:167], a[132:135], a[68:71], v[164:167]
ds_read_b64_tr_b16 a[158:159], v238 offset:13856
s_add_u32 m0, m0, 1024
v_mfma_f32_16x16x32_bf16 v[168:171], a[132:135], a[72:75], v[168:171]
buffer_load_dwordx4 v226, s[40:43], s64 offen lds
v_mfma_f32_16x16x32_bf16 v[172:175], a[132:135], a[76:79], v[172:175]
s_waitcnt lgkmcnt(10)
v_mfma_f32_16x16x32_bf16 v[176:179], a[136:139], a[48:51], v[176:179]
ds_read_b64_tr_b16 a[160:161], v238 offset:64
v_mfma_f32_16x16x32_bf16 v[180:183], a[136:139], a[52:55], v[180:183]
ds_read_b64_tr_b16 a[162:163], v238 offset:4672
s_add_u32 m0, m0, 1024
v_mfma_f32_16x16x32_bf16 v[184:187], a[136:139], a[56:59], v[184:187]
buffer_load_dwordx4 v227, s[40:43], s64 offen lds
v_mfma_f32_16x16x32_bf16 v[188:191], a[136:139], a[60:63], v[188:191]
s_waitcnt lgkmcnt(10)
v_mfma_f32_16x16x32_bf16 v[176:179], a[140:143], a[64:67], v[176:179]
ds_read_b64_tr_b16 a[164:165], v238 offset:9280
v_mfma_f32_16x16x32_bf16 v[180:183], a[140:143], a[68:71], v[180:183]
ds_read_b64_tr_b16 a[166:167], v238 offset:13888
s_add_u32 m0, m0, 1024
v_mfma_f32_16x16x32_bf16 v[184:187], a[140:143], a[72:75], v[184:187]
buffer_load_dwordx4 v228, s[40:43], s64 offen lds
v_mfma_f32_16x16x32_bf16 v[188:191], a[140:143], a[76:79], v[188:191]
s_waitcnt vmcnt(18) lgkmcnt(10)
v_mfma_f32_16x16x32_bf16 v[128:131], a[144:147], a[80:83], v[128:131]
ds_read_b64_tr_b16 a[168:169], v238 offset:96
v_mfma_f32_16x16x32_bf16 v[132:135], a[144:147], a[84:87], v[132:135]
ds_read_b64_tr_b16 a[170:171], v238 offset:4704
s_add_u32 m0, m0, 1024
v_mfma_f32_16x16x32_bf16 v[136:139], a[144:147], a[88:91], v[136:139]
buffer_load_dwordx4 v229, s[40:43], s64 offen lds
v_mfma_f32_16x16x32_bf16 v[140:143], a[144:147], a[92:95], v[140:143]
s_waitcnt vmcnt(12) lgkmcnt(10)
v_mfma_f32_16x16x32_bf16 v[128:131], a[148:151], a[96:99], v[128:131]
ds_read_b64_tr_b16 a[172:173], v238 offset:9312
v_mfma_f32_16x16x32_bf16 v[132:135], a[148:151], a[100:103], v[132:135]
ds_read_b64_tr_b16 a[174:175], v238 offset:13920
s_add_u32 m0, m0, 1024
v_mfma_f32_16x16x32_bf16 v[136:139], a[148:151], a[104:107], v[136:139]
buffer_load_dwordx4 v230, s[40:43], s64 offen lds
v_mfma_f32_16x16x32_bf16 v[140:143], a[148:151], a[108:111], v[140:143]
s_waitcnt lgkmcnt(10)
v_mfma_f32_16x16x32_bf16 v[144:147], a[152:155], a[80:83], v[144:147]
ds_read_b64_tr_b16 a[176:177], v233 offset:128
v_mfma_f32_16x16x32_bf16 v[148:151], a[152:155], a[84:87], v[148:151]
ds_read_b64_tr_b16 a[178:179], v233 offset:4736
s_add_u32 m0, m0, 1024
v_mfma_f32_16x16x32_bf16 v[152:155], a[152:155], a[88:91], v[152:155]
buffer_load_dwordx4 v231, s[40:43], s64 offen lds
v_mfma_f32_16x16x32_bf16 v[156:159], a[152:155], a[92:95], v[156:159]
s_waitcnt lgkmcnt(10)
v_mfma_f32_16x16x32_bf16 v[144:147], a[156:159], a[96:99], v[144:147]
ds_read_b64_tr_b16 a[112:113], v233 offset:9344
v_mfma_f32_16x16x32_bf16 v[148:151], a[156:159], a[100:103], v[148:151]
ds_read_b64_tr_b16 a[114:115], v233 offset:13952
s_add_u32 m0, m0, 1024
v_mfma_f32_16x16x32_bf16 v[152:155], a[156:159], a[104:107], v[152:155]
buffer_load_dwordx4 v232, s[40:43], s64 offen lds
v_mfma_f32_16x16x32_bf16 v[156:159], a[156:159], a[108:111], v[156:159]
s_waitcnt lgkmcnt(10)
v_mfma_f32_16x16x32_bf16 v[160:163], a[160:163], a[80:83], v[160:163]
ds_read_b64_tr_b16 a[116:117], v238 offset:128
v_mfma_f32_16x16x32_bf16 v[164:167], a[160:163], a[84:87], v[164:167]
ds_read_b64_tr_b16 a[118:119], v238 offset:4736
v_mfma_f32_16x16x32_bf16 v[168:171], a[160:163], a[88:91], v[168:171]
s_add_u32 s64, s64, s65
s_add_u32 s69, s69, 36864
s_cmp_eq_u32 s69, 147456
s_cselect_b32 s69, 0, s69
v_mfma_f32_16x16x32_bf16 v[172:175], a[160:163], a[92:95], v[172:175]
s_waitcnt lgkmcnt(10)
v_mfma_f32_16x16x32_bf16 v[160:163], a[164:167], a[96:99], v[160:163]
ds_read_b64_tr_b16 a[120:121], v238 offset:9344
v_cvt_pk_bf16_f32 v128, v128, v129
v_mfma_f32_16x16x32_bf16 v[164:167], a[164:167], a[100:103], v[164:167]
ds_read_b64_tr_b16 a[122:123], v238 offset:13952
v_cvt_pk_bf16_f32 v129, v130, v131
v_mfma_f32_16x16x32_bf16 v[168:171], a[164:167], a[104:107], v[168:171]
v_cvt_pk_bf16_f32 v130, v144, v145
v_cvt_pk_bf16_f32 v131, v146, v147
v_mfma_f32_16x16x32_bf16 v[172:175], a[164:167], a[108:111], v[172:175]
v_pk_max_i16 v128, v128, 0
v_pk_max_i16 v129, v129, 0
s_waitcnt lgkmcnt(10)
v_mfma_f32_16x16x32_bf16 v[176:179], a[168:171], a[80:83], v[176:179]
ds_read_b64_tr_b16 a[124:125], v233 offset:160
v_pk_max_i16 v130, v130, 0
v_mfma_f32_16x16x32_bf16 v[180:183], a[168:171], a[84:87], v[180:183]
ds_read_b64_tr_b16 a[126:127], v233 offset:4768
v_pk_max_i16 v131, v131, 0
v_mfma_f32_16x16x32_bf16 v[184:187], a[168:171], a[88:91], v[184:187]
v_cvt_pk_bf16_f32 v132, v132, v133
v_cvt_pk_bf16_f32 v133, v134, v135
v_mfma_f32_16x16x32_bf16 v[188:191], a[168:171], a[92:95], v[188:191]
v_cvt_pk_bf16_f32 v134, v148, v149
v_cvt_pk_bf16_f32 v135, v150, v151
s_waitcnt lgkmcnt(10)
v_mfma_f32_16x16x32_bf16 v[176:179], a[172:175], a[96:99], v[176:179]
ds_read_b64_tr_b16 a[128:129], v233 offset:9376
v_pk_max_i16 v132, v132, 0
v_mfma_f32_16x16x32_bf16 v[180:183], a[172:175], a[100:103], v[180:183]
ds_read_b64_tr_b16 a[130:131], v233 offset:13984
v_pk_max_i16 v133, v133, 0
v_mfma_f32_16x16x32_bf16 v[184:187], a[172:175], a[104:107], v[184:187]
v_pk_max_i16 v134, v134, 0
v_pk_max_i16 v135, v135, 0
v_mfma_f32_16x16x32_bf16 v[188:191], a[172:175], a[108:111], v[188:191]
v_cvt_pk_bf16_f32 v136, v136, v137
v_cvt_pk_bf16_f32 v137, v138, v139
s_waitcnt lgkmcnt(10)
v_mfma_f32_16x16x32_bf16 v[192:195], a[176:179], a[48:51], v[192:195]
ds_read_b64_tr_b16 a[132:133], v238 offset:160
v_cvt_pk_bf16_f32 v138, v152, v153
v_mfma_f32_16x16x32_bf16 v[196:199], a[176:179], a[52:55], v[196:199]
ds_read_b64_tr_b16 a[134:135], v238 offset:4768
v_cvt_pk_bf16_f32 v139, v154, v155
v_mfma_f32_16x16x32_bf16 v[200:203], a[176:179], a[56:59], v[200:203]
v_pk_max_i16 v136, v136, 0
v_pk_max_i16 v137, v137, 0
v_mfma_f32_16x16x32_bf16 v[204:207], a[176:179], a[60:63], v[204:207]
v_pk_max_i16 v138, v138, 0
v_pk_max_i16 v139, v139, 0
s_waitcnt lgkmcnt(10)
v_mfma_f32_16x16x32_bf16 v[192:195], a[112:115], a[64:67], v[192:195]
ds_read_b64_tr_b16 a[136:137], v238 offset:9376
v_cvt_pk_bf16_f32 v140, v140, v141
v_mfma_f32_16x16x32_bf16 v[196:199], a[112:115], a[68:71], v[196:199]
ds_read_b64_tr_b16 a[138:139], v238 offset:13984
v_cvt_pk_bf16_f32 v141, v142, v143
v_mfma_f32_16x16x32_bf16 v[200:203], a[112:115], a[72:75], v[200:203]
v_cvt_pk_bf16_f32 v142, v156, v157
v_cvt_pk_bf16_f32 v143, v158, v159
v_mfma_f32_16x16x32_bf16 v[204:207], a[112:115], a[76:79], v[204:207]
v_pk_max_i16 v140, v140, 0
v_pk_max_i16 v141, v141, 0
s_waitcnt lgkmcnt(10)
v_mfma_f32_16x16x32_bf16 v[192:195], a[116:119], a[80:83], v[192:195]
ds_read_b64_tr_b16 a[140:141], v233 offset:192
v_pk_max_i16 v142, v142, 0
v_mfma_f32_16x16x32_bf16 v[196:199], a[116:119], a[84:87], v[196:199]
ds_read_b64_tr_b16 a[142:143], v233 offset:4800
v_pk_max_i16 v143, v143, 0
v_mfma_f32_16x16x32_bf16 v[200:203], a[116:119], a[88:91], v[200:203]
v_cvt_pk_bf16_f32 v160, v160, v161
v_cvt_pk_bf16_f32 v161, v162, v163
v_mfma_f32_16x16x32_bf16 v[204:207], a[116:119], a[92:95], v[204:207]
v_cvt_pk_bf16_f32 v162, v176, v177
v_cvt_pk_bf16_f32 v163, v178, v179
s_waitcnt lgkmcnt(10)
v_mfma_f32_16x16x32_bf16 v[192:195], a[120:123], a[96:99], v[192:195]
ds_read_b64_tr_b16 a[144:145], v233 offset:9408
v_pk_max_i16 v160, v160, 0
v_mfma_f32_16x16x32_bf16 v[196:199], a[120:123], a[100:103], v[196:199]
ds_read_b64_tr_b16 a[146:147], v233 offset:14016
v_pk_max_i16 v161, v161, 0
v_mfma_f32_16x16x32_bf16 v[200:203], a[120:123], a[104:107], v[200:203]
v_pk_max_i16 v162, v162, 0
v_pk_max_i16 v163, v163, 0
v_mfma_f32_16x16x32_bf16 v[204:207], a[120:123], a[108:111], v[204:207]
v_cvt_pk_bf16_f32 v164, v164, v165
v_cvt_pk_bf16_f32 v165, v166, v167
s_waitcnt lgkmcnt(10)
v_mfma_f32_16x16x32_bf16 v[208:211], a[124:127], a[48:51], v[208:211]
ds_read_b64_tr_b16 a[148:149], v238 offset:192
v_cvt_pk_bf16_f32 v166, v180, v181
v_mfma_f32_16x16x32_bf16 v[212:215], a[124:127], a[52:55], v[212:215]
ds_read_b64_tr_b16 a[150:151], v238 offset:4800
v_cvt_pk_bf16_f32 v167, v182, v183
v_mfma_f32_16x16x32_bf16 v[216:219], a[124:127], a[56:59], v[216:219]
v_pk_max_i16 v164, v164, 0
v_pk_max_i16 v165, v165, 0
v_mfma_f32_16x16x32_bf16 v[220:223], a[124:127], a[60:63], v[220:223]
v_pk_max_i16 v166, v166, 0
v_pk_max_i16 v167, v167, 0
s_waitcnt lgkmcnt(10)
v_mfma_f32_16x16x32_bf16 v[208:211], a[128:131], a[64:67], v[208:211]
ds_read_b64_tr_b16 a[152:153], v238 offset:9408
v_cvt_pk_bf16_f32 v168, v168, v169
v_mfma_f32_16x16x32_bf16 v[212:215], a[128:131], a[68:71], v[212:215]
ds_read_b64_tr_b16 a[154:155], v238 offset:14016
v_cvt_pk_bf16_f32 v169, v170, v171
v_mfma_f32_16x16x32_bf16 v[216:219], a[128:131], a[72:75], v[216:219]
buffer_store_dwordx4 v[128:131], v235, s[48:51], s75 offen offset:0 sc1
v_mfma_f32_16x16x32_bf16 v[220:223], a[128:131], a[76:79], v[220:223]
v_cvt_pk_bf16_f32 v170, v184, v185
v_cvt_pk_bf16_f32 v171, v186, v187
s_waitcnt lgkmcnt(10)
v_mfma_f32_16x16x32_bf16 v[208:211], a[132:135], a[80:83], v[208:211]
ds_read_b64_tr_b16 a[156:157], v233 offset:224
v_pk_max_i16 v168, v168, 0
v_mfma_f32_16x16x32_bf16 v[212:215], a[132:135], a[84:87], v[212:215]
ds_read_b64_tr_b16 a[158:159], v233 offset:4832
v_pk_max_i16 v169, v169, 0
v_mfma_f32_16x16x32_bf16 v[216:219], a[132:135], a[88:91], v[216:219]
v_pk_max_i16 v170, v170, 0
v_pk_max_i16 v171, v171, 0
v_mfma_f32_16x16x32_bf16 v[220:223], a[132:135], a[92:95], v[220:223]
v_cvt_pk_bf16_f32 v172, v172, v173
v_cvt_pk_bf16_f32 v173, v174, v175
s_waitcnt lgkmcnt(10)
v_mfma_f32_16x16x32_bf16 v[208:211], a[136:139], a[96:99], v[208:211]
ds_read_b64_tr_b16 a[160:161], v233 offset:9440
v_cvt_pk_bf16_f32 v174, v188, v189
v_mfma_f32_16x16x32_bf16 v[212:215], a[136:139], a[100:103], v[212:215]
ds_read_b64_tr_b16 a[162:163], v233 offset:14048
v_cvt_pk_bf16_f32 v175, v190, v191
v_mfma_f32_16x16x32_bf16 v[216:219], a[136:139], a[104:107], v[216:219]
v_pk_max_i16 v172, v172, 0
v_pk_max_i16 v173, v173, 0
v_mfma_f32_16x16x32_bf16 v[220:223], a[136:139], a[108:111], v[220:223]
v_pk_max_i16 v174, v174, 0
v_pk_max_i16 v175, v175, 0
s_waitcnt lgkmcnt(10)
v_mfma_f32_16x16x32_bf16 a[0:3], a[140:143], a[48:51], a[0:3]
ds_read_b64_tr_b16 a[164:165], v238 offset:224
v_mfma_f32_16x16x32_bf16 a[4:7], a[140:143], a[52:55], a[4:7]
ds_read_b64_tr_b16 a[166:167], v238 offset:4832
v_mfma_f32_16x16x32_bf16 a[8:11], a[140:143], a[56:59], a[8:11]
v_mfma_f32_16x16x32_bf16 a[12:15], a[140:143], a[60:63], a[12:15]
s_waitcnt lgkmcnt(10)
v_mfma_f32_16x16x32_bf16 a[0:3], a[144:147], a[64:67], a[0:3]
ds_read_b64_tr_b16 a[168:169], v238 offset:9440
v_cvt_pk_bf16_f32 v192, v192, v193
v_mfma_f32_16x16x32_bf16 a[4:7], a[144:147], a[68:71], a[4:7]
ds_read_b64_tr_b16 a[170:171], v238 offset:14048
v_cvt_pk_bf16_f32 v193, v194, v195
v_mfma_f32_16x16x32_bf16 a[8:11], a[144:147], a[72:75], a[8:11]
v_cvt_pk_bf16_f32 v194, v208, v209
v_cvt_pk_bf16_f32 v195, v210, v211
v_mfma_f32_16x16x32_bf16 a[12:15], a[144:147], a[76:79], a[12:15]
v_pk_max_i16 v192, v192, 0
v_pk_max_i16 v193, v193, 0
s_waitcnt lgkmcnt(10)
v_mfma_f32_16x16x32_bf16 a[0:3], a[148:151], a[80:83], a[0:3]
ds_read_b64_tr_b16 a[172:173], v233 offset:256
v_pk_max_i16 v194, v194, 0
v_mfma_f32_16x16x32_bf16 a[4:7], a[148:151], a[84:87], a[4:7]
ds_read_b64_tr_b16 a[174:175], v233 offset:4864
v_pk_max_i16 v195, v195, 0
v_mfma_f32_16x16x32_bf16 a[8:11], a[148:151], a[88:91], a[8:11]
v_cvt_pk_bf16_f32 v196, v196, v197
v_cvt_pk_bf16_f32 v197, v198, v199
v_mfma_f32_16x16x32_bf16 a[12:15], a[148:151], a[92:95], a[12:15]
v_cvt_pk_bf16_f32 v198, v212, v213
v_cvt_pk_bf16_f32 v199, v214, v215
s_waitcnt lgkmcnt(10)
v_mfma_f32_16x16x32_bf16 a[0:3], a[152:155], a[96:99], a[0:3]
ds_read_b64_tr_b16 a[176:177], v233 offset:9472
v_pk_max_i16 v196, v196, 0
v_mfma_f32_16x16x32_bf16 a[4:7], a[152:155], a[100:103], a[4:7]
ds_read_b64_tr_b16 a[178:179], v233 offset:14080
v_pk_max_i16 v197, v197, 0
v_mfma_f32_16x16x32_bf16 a[8:11], a[152:155], a[104:107], a[8:11]
buffer_store_dwordx4 v[132:135], v235, s[48:51], s75 offen offset:1024 sc1
v_mfma_f32_16x16x32_bf16 a[12:15], a[152:155], a[108:111], a[12:15]
v_pk_max_i16 v198, v198, 0
v_pk_max_i16 v199, v199, 0
s_waitcnt lgkmcnt(10)
v_mfma_f32_16x16x32_bf16 a[16:19], a[156:159], a[48:51], a[16:19]
ds_read_b64_tr_b16 a[112:113], v238 offset:256
v_cvt_pk_bf16_f32 v200, v200, v201
v_mfma_f32_16x16x32_bf16 a[20:23], a[156:159], a[52:55], a[20:23]
ds_read_b64_tr_b16 a[114:115], v238 offset:4864
v_cvt_pk_bf16_f32 v201, v202, v203
v_mfma_f32_16x16x32_bf16 a[24:27], a[156:159], a[56:59], a[24:27]
v_cvt_pk_bf16_f32 v202, v216, v217
v_cvt_pk_bf16_f32 v203, v218, v219
v_mfma_f32_16x16x32_bf16 a[28:31], a[156:159], a[60:63], a[28:31]
v_pk_max_i16 v200, v200, 0
v_pk_max_i16 v201, v201, 0
s_waitcnt lgkmcnt(10)
v_mfma_f32_16x16x32_bf16 a[16:19], a[160:163], a[64:67], a[16:19]
ds_read_b64_tr_b16 a[116:117], v238 offset:9472
v_pk_max_i16 v202, v202, 0
v_mfma_f32_16x16x32_bf16 a[20:23], a[160:163], a[68:71], a[20:23]
ds_read_b64_tr_b16 a[118:119], v238 offset:14080
v_pk_max_i16 v203, v203, 0
v_mfma_f32_16x16x32_bf16 a[24:27], a[160:163], a[72:75], a[24:27]
v_cvt_pk_bf16_f32 v204, v204, v205
v_cvt_pk_bf16_f32 v205, v206, v207
v_mfma_f32_16x16x32_bf16 a[28:31], a[160:163], a[76:79], a[28:31]
v_cvt_pk_bf16_f32 v206, v220, v221
v_cvt_pk_bf16_f32 v207, v222, v223
s_waitcnt lgkmcnt(10)
v_mfma_f32_16x16x32_bf16 a[16:19], a[164:167], a[80:83], a[16:19]
ds_read_b64_tr_b16 a[120:121], v234 offset:18432
v_pk_max_i16 v204, v204, 0
v_mfma_f32_16x16x32_bf16 a[20:23], a[164:167], a[84:87], a[20:23]
ds_read_b64_tr_b16 a[122:123], v234 offset:23040
v_pk_max_i16 v205, v205, 0
v_mfma_f32_16x16x32_bf16 a[24:27], a[164:167], a[88:91], a[24:27]
v_pk_max_i16 v206, v206, 0
v_pk_max_i16 v207, v207, 0
v_mfma_f32_16x16x32_bf16 a[28:31], a[164:167], a[92:95], a[28:31]
s_waitcnt lgkmcnt(10)
v_mfma_f32_16x16x32_bf16 a[16:19], a[168:171], a[96:99], a[16:19]
ds_read_b64_tr_b16 a[124:125], v234 offset:27648
v_mfma_f32_16x16x32_bf16 a[20:23], a[168:171], a[100:103], a[20:23]
ds_read_b64_tr_b16 a[126:127], v234 offset:32256
v_mfma_f32_16x16x32_bf16 a[24:27], a[168:171], a[104:107], a[24:27]
v_mfma_f32_16x16x32_bf16 a[28:31], a[168:171], a[108:111], a[28:31]
s_waitcnt lgkmcnt(10)
v_mfma_f32_16x16x32_bf16 a[32:35], a[172:175], a[48:51], a[32:35]
ds_read_b64_tr_b16 a[128:129], v239 offset:18432
v_mfma_f32_16x16x32_bf16 a[36:39], a[172:175], a[52:55], a[36:39]
ds_read_b64_tr_b16 a[130:131], v239 offset:23040
v_mfma_f32_16x16x32_bf16 a[40:43], a[172:175], a[56:59], a[40:43]
v_mfma_f32_16x16x32_bf16 a[44:47], a[172:175], a[60:63], a[44:47]
s_waitcnt lgkmcnt(10)
v_mfma_f32_16x16x32_bf16 a[32:35], a[176:179], a[64:67], a[32:35]
ds_read_b64_tr_b16 a[132:133], v239 offset:27648
v_accvgpr_read_b32 v144, a0
v_mfma_f32_16x16x32_bf16 a[36:39], a[176:179], a[68:71], a[36:39]
ds_read_b64_tr_b16 a[134:135], v239 offset:32256
v_accvgpr_read_b32 v145, a1
v_mfma_f32_16x16x32_bf16 a[40:43], a[176:179], a[72:75], a[40:43]
buffer_store_dwordx4 v[136:139], v235, s[48:51], s75 offen offset:2048 sc1
v_mfma_f32_16x16x32_bf16 a[44:47], a[176:179], a[76:79], a[44:47]
v_accvgpr_read_b32 v146, a2
v_accvgpr_read_b32 v147, a3
s_waitcnt lgkmcnt(10)
v_mfma_f32_16x16x32_bf16 a[32:35], a[112:115], a[80:83], a[32:35]
ds_read_b64_tr_b16 a[136:137], v234 offset:18440
v_accvgpr_read_b32 v176, a16
v_mfma_f32_16x16x32_bf16 a[36:39], a[112:115], a[84:87], a[36:39]
ds_read_b64_tr_b16 a[138:139], v234 offset:23048
v_accvgpr_read_b32 v177, a17
v_mfma_f32_16x16x32_bf16 a[40:43], a[112:115], a[88:91], a[40:43]
v_accvgpr_read_b32 v178, a18
v_accvgpr_read_b32 v179, a19
v_mfma_f32_16x16x32_bf16 a[44:47], a[112:115], a[92:95], a[44:47]
v_cvt_pk_bf16_f32 v144, v144, v145
v_cvt_pk_bf16_f32 v145, v146, v147
s_waitcnt lgkmcnt(10)
v_mfma_f32_16x16x32_bf16 a[32:35], a[116:119], a[96:99], a[32:35]
ds_read_b64_tr_b16 a[140:141], v234 offset:27656
v_cvt_pk_bf16_f32 v146, v176, v177
v_mfma_f32_16x16x32_bf16 a[36:39], a[116:119], a[100:103], a[36:39]
ds_read_b64_tr_b16 a[142:143], v234 offset:32264
v_cvt_pk_bf16_f32 v147, v178, v179
v_mfma_f32_16x16x32_bf16 a[40:43], a[116:119], a[104:107], a[40:43]
v_pk_max_i16 v144, v144, 0
v_pk_max_i16 v145, v145, 0
)"
R"(
v_mfma_f32_16x16x32_bf16 a[44:47], a[116:119], a[108:111], a[44:47]
v_pk_max_i16 v146, v146, 0
v_pk_max_i16 v147, v147, 0
s_waitcnt lgkmcnt(10)
v_mfma_f32_16x16x32_bf16 v[0:3], a[120:123], a[48:51], v[0:3]
ds_read_b64_tr_b16 a[144:145], v239 offset:18440
v_accvgpr_write_b32 a0, v144
v_mfma_f32_16x16x32_bf16 v[4:7], a[120:123], a[52:55], v[4:7]
ds_read_b64_tr_b16 a[146:147], v239 offset:23048
v_accvgpr_write_b32 a1, v145
v_mfma_f32_16x16x32_bf16 v[8:11], a[120:123], a[56:59], v[8:11]
v_accvgpr_write_b32 a2, v146
v_accvgpr_write_b32 a3, v147
v_mfma_f32_16x16x32_bf16 v[12:15], a[120:123], a[60:63], v[12:15]
v_accvgpr_read_b32 v148, a4
v_accvgpr_read_b32 v149, a5
s_waitcnt lgkmcnt(10)
v_mfma_f32_16x16x32_bf16 v[0:3], a[124:127], a[64:67], v[0:3]
ds_read_b64_tr_b16 a[148:149], v239 offset:27656
v_accvgpr_read_b32 v150, a6
v_mfma_f32_16x16x32_bf16 v[4:7], a[124:127], a[68:71], v[4:7]
ds_read_b64_tr_b16 a[150:151], v239 offset:32264
v_accvgpr_read_b32 v151, a7
v_mfma_f32_16x16x32_bf16 v[8:11], a[124:127], a[72:75], v[8:11]
v_accvgpr_read_b32 v180, a20
v_accvgpr_read_b32 v181, a21
v_mfma_f32_16x16x32_bf16 v[12:15], a[124:127], a[76:79], v[12:15]
v_accvgpr_read_b32 v182, a22
v_accvgpr_read_b32 v183, a23
s_waitcnt lgkmcnt(10)
v_mfma_f32_16x16x32_bf16 v[0:3], a[128:131], a[80:83], v[0:3]
ds_read_b64_tr_b16 a[152:153], v234 offset:18496
v_cvt_pk_bf16_f32 v148, v148, v149
v_mfma_f32_16x16x32_bf16 v[4:7], a[128:131], a[84:87], v[4:7]
ds_read_b64_tr_b16 a[154:155], v234 offset:23104
v_cvt_pk_bf16_f32 v149, v150, v151
v_mfma_f32_16x16x32_bf16 v[8:11], a[128:131], a[88:91], v[8:11]
v_cvt_pk_bf16_f32 v150, v180, v181
v_cvt_pk_bf16_f32 v151, v182, v183
v_mfma_f32_16x16x32_bf16 v[12:15], a[128:131], a[92:95], v[12:15]
v_pk_max_i16 v148, v148, 0
v_pk_max_i16 v149, v149, 0
s_waitcnt lgkmcnt(10)
v_mfma_f32_16x16x32_bf16 v[0:3], a[132:135], a[96:99], v[0:3]
ds_read_b64_tr_b16 a[156:157], v234 offset:27712
v_pk_max_i16 v150, v150, 0
v_mfma_f32_16x16x32_bf16 v[4:7], a[132:135], a[100:103], v[4:7]
ds_read_b64_tr_b16 a[158:159], v234 offset:32320
v_pk_max_i16 v151, v151, 0
v_mfma_f32_16x16x32_bf16 v[8:11], a[132:135], a[104:107], v[8:11]
buffer_store_dwordx4 v[140:143], v235, s[48:51], s75 offen offset:3072 sc1
s_add_u32 s75, s75, 4096
v_mfma_f32_16x16x32_bf16 v[12:15], a[132:135], a[108:111], v[12:15]
v_accvgpr_write_b32 a4, v148
v_accvgpr_write_b32 a5, v149
s_waitcnt lgkmcnt(10)
v_mfma_f32_16x16x32_bf16 v[16:19], a[136:139], a[48:51], v[16:19]
ds_read_b64_tr_b16 a[160:161], v239 offset:18496
v_accvgpr_write_b32 a6, v150
v_mfma_f32_16x16x32_bf16 v[20:23], a[136:139], a[52:55], v[20:23]
ds_read_b64_tr_b16 a[162:163], v239 offset:23104
v_accvgpr_write_b32 a7, v151
v_mfma_f32_16x16x32_bf16 v[24:27], a[136:139], a[56:59], v[24:27]
v_accvgpr_read_b32 v152, a8
v_accvgpr_read_b32 v153, a9
v_mfma_f32_16x16x32_bf16 v[28:31], a[136:139], a[60:63], v[28:31]
v_accvgpr_read_b32 v154, a10
v_accvgpr_read_b32 v155, a11
s_waitcnt lgkmcnt(10)
v_mfma_f32_16x16x32_bf16 v[16:19], a[140:143], a[64:67], v[16:19]
ds_read_b64_tr_b16 a[164:165], v239 offset:27712
v_accvgpr_read_b32 v184, a24
v_mfma_f32_16x16x32_bf16 v[20:23], a[140:143], a[68:71], v[20:23]
ds_read_b64_tr_b16 a[166:167], v239 offset:32320
v_accvgpr_read_b32 v185, a25
v_mfma_f32_16x16x32_bf16 v[24:27], a[140:143], a[72:75], v[24:27]
v_accvgpr_read_b32 v186, a26
v_accvgpr_read_b32 v187, a27
v_mfma_f32_16x16x32_bf16 v[28:31], a[140:143], a[76:79], v[28:31]
v_cvt_pk_bf16_f32 v152, v152, v153
v_cvt_pk_bf16_f32 v153, v154, v155
s_waitcnt lgkmcnt(10)
v_mfma_f32_16x16x32_bf16 v[16:19], a[144:147], a[80:83], v[16:19]
ds_read_b64_tr_b16 a[168:169], v234 offset:18504
v_cvt_pk_bf16_f32 v154, v184, v185
v_mfma_f32_16x16x32_bf16 v[20:23], a[144:147], a[84:87], v[20:23]
ds_read_b64_tr_b16 a[170:171], v234 offset:23112
v_cvt_pk_bf16_f32 v155, v186, v187
v_mfma_f32_16x16x32_bf16 v[24:27], a[144:147], a[88:91], v[24:27]
v_pk_max_i16 v152, v152, 0
v_pk_max_i16 v153, v153, 0
v_mfma_f32_16x16x32_bf16 v[28:31], a[144:147], a[92:95], v[28:31]
v_pk_max_i16 v154, v154, 0
v_pk_max_i16 v155, v155, 0
s_waitcnt lgkmcnt(10)
v_mfma_f32_16x16x32_bf16 v[16:19], a[148:151], a[96:99], v[16:19]
ds_read_b64_tr_b16 a[172:173], v234 offset:27720
v_accvgpr_write_b32 a8, v152
v_mfma_f32_16x16x32_bf16 v[20:23], a[148:151], a[100:103], v[20:23]
ds_read_b64_tr_b16 a[174:175], v234 offset:32328
v_accvgpr_write_b32 a9, v153
v_mfma_f32_16x16x32_bf16 v[24:27], a[148:151], a[104:107], v[24:27]
v_accvgpr_write_b32 a10, v154
v_accvgpr_write_b32 a11, v155
v_mfma_f32_16x16x32_bf16 v[28:31], a[148:151], a[108:111], v[28:31]
v_accvgpr_read_b32 v156, a12
v_accvgpr_read_b32 v157, a13
s_waitcnt lgkmcnt(10)
v_mfma_f32_16x16x32_bf16 v[32:35], a[152:155], a[48:51], v[32:35]
ds_read_b64_tr_b16 a[176:177], v239 offset:18504
v_accvgpr_read_b32 v158, a14
v_mfma_f32_16x16x32_bf16 v[36:39], a[152:155], a[52:55], v[36:39]
ds_read_b64_tr_b16 a[178:179], v239 offset:23112
v_accvgpr_read_b32 v159, a15
v_mfma_f32_16x16x32_bf16 v[40:43], a[152:155], a[56:59], v[40:43]
v_accvgpr_read_b32 v188, a28
v_accvgpr_read_b32 v189, a29
v_mfma_f32_16x16x32_bf16 v[44:47], a[152:155], a[60:63], v[44:47]
v_accvgpr_read_b32 v190, a30
v_accvgpr_read_b32 v191, a31
s_waitcnt lgkmcnt(10)
v_mfma_f32_16x16x32_bf16 v[32:35], a[156:159], a[64:67], v[32:35]
ds_read_b64_tr_b16 a[112:113], v239 offset:27720
v_cvt_pk_bf16_f32 v156, v156, v157
v_mfma_f32_16x16x32_bf16 v[36:39], a[156:159], a[68:71], v[36:39]
ds_read_b64_tr_b16 a[114:115], v239 offset:32328
v_cvt_pk_bf16_f32 v157, v158, v159
v_mfma_f32_16x16x32_bf16 v[40:43], a[156:159], a[72:75], v[40:43]
buffer_store_dwordx4 v[160:163], v235, s[48:51], s75 offen offset:0 sc1
v_mfma_f32_16x16x32_bf16 v[44:47], a[156:159], a[76:79], v[44:47]
v_cvt_pk_bf16_f32 v158, v188, v189
v_cvt_pk_bf16_f32 v159, v190, v191
s_waitcnt lgkmcnt(10)
v_mfma_f32_16x16x32_bf16 v[32:35], a[160:163], a[80:83], v[32:35]
ds_read_b64_tr_b16 a[116:117], v234 offset:18560
v_pk_max_i16 v156, v156, 0
v_mfma_f32_16x16x32_bf16 v[36:39], a[160:163], a[84:87], v[36:39]
ds_read_b64_tr_b16 a[118:119], v234 offset:23168
v_pk_max_i16 v157, v157, 0
v_mfma_f32_16x16x32_bf16 v[40:43], a[160:163], a[88:91], v[40:43]
v_pk_max_i16 v158, v158, 0
v_pk_max_i16 v159, v159, 0
v_mfma_f32_16x16x32_bf16 v[44:47], a[160:163], a[92:95], v[44:47]
v_accvgpr_write_b32 a12, v156
v_accvgpr_write_b32 a13, v157
s_waitcnt lgkmcnt(10)
v_mfma_f32_16x16x32_bf16 v[32:35], a[164:167], a[96:99], v[32:35]
ds_read_b64_tr_b16 a[120:121], v234 offset:27776
v_accvgpr_write_b32 a14, v158
v_mfma_f32_16x16x32_bf16 v[36:39], a[164:167], a[100:103], v[36:39]
ds_read_b64_tr_b16 a[122:123], v234 offset:32384
v_accvgpr_write_b32 a15, v159
v_mfma_f32_16x16x32_bf16 v[40:43], a[164:167], a[104:107], v[40:43]
v_accvgpr_read_b32 v208, a32
v_accvgpr_read_b32 v209, a33
v_mfma_f32_16x16x32_bf16 v[44:47], a[164:167], a[108:111], v[44:47]
v_accvgpr_read_b32 v210, a34
v_accvgpr_read_b32 v211, a35
s_waitcnt lgkmcnt(10)
v_mfma_f32_16x16x32_bf16 v[48:51], a[168:171], a[48:51], v[48:51]
ds_read_b64_tr_b16 a[124:125], v239 offset:18560
v_cvt_pk_bf16_f32 v184, v208, v209
v_mfma_f32_16x16x32_bf16 v[52:55], a[168:171], a[52:55], v[52:55]
ds_read_b64_tr_b16 a[126:127], v239 offset:23168
v_cvt_pk_bf16_f32 v185, v210, v211
v_mfma_f32_16x16x32_bf16 v[56:59], a[168:171], a[56:59], v[56:59]
v_pk_max_i16 v184, v184, 0
v_pk_max_i16 v185, v185, 0
v_mfma_f32_16x16x32_bf16 v[60:63], a[168:171], a[60:63], v[60:63]
v_accvgpr_read_b32 v212, a36
v_accvgpr_read_b32 v213, a37
s_waitcnt lgkmcnt(10)
v_mfma_f32_16x16x32_bf16 v[48:51], a[172:175], a[64:67], v[48:51]
ds_read_b64_tr_b16 a[128:129], v239 offset:27776
v_accvgpr_read_b32 v214, a38
v_mfma_f32_16x16x32_bf16 v[52:55], a[172:175], a[68:71], v[52:55]
ds_read_b64_tr_b16 a[130:131], v239 offset:32384
v_accvgpr_read_b32 v215, a39
v_mfma_f32_16x16x32_bf16 v[56:59], a[172:175], a[72:75], v[56:59]
v_cvt_pk_bf16_f32 v186, v212, v213
v_cvt_pk_bf16_f32 v187, v214, v215
v_mfma_f32_16x16x32_bf16 v[60:63], a[172:175], a[76:79], v[60:63]
v_pk_max_i16 v186, v186, 0
v_pk_max_i16 v187, v187, 0
s_waitcnt lgkmcnt(10)
v_mfma_f32_16x16x32_bf16 v[48:51], a[176:179], a[80:83], v[48:51]
ds_read_b64_tr_b16 a[132:133], v234 offset:18568
v_accvgpr_read_b32 v216, a40
v_mfma_f32_16x16x32_bf16 v[52:55], a[176:179], a[84:87], v[52:55]
ds_read_b64_tr_b16 a[134:135], v234 offset:23176
v_accvgpr_read_b32 v217, a41
v_mfma_f32_16x16x32_bf16 v[56:59], a[176:179], a[88:91], v[56:59]
v_accvgpr_read_b32 v218, a42
v_accvgpr_read_b32 v219, a43
v_mfma_f32_16x16x32_bf16 v[60:63], a[176:179], a[92:95], v[60:63]
v_cvt_pk_bf16_f32 v188, v216, v217
v_cvt_pk_bf16_f32 v189, v218, v219
s_waitcnt lgkmcnt(10)
v_mfma_f32_16x16x32_bf16 v[48:51], a[112:115], a[96:99], v[48:51]
ds_read_b64_tr_b16 a[136:137], v234 offset:27784
v_pk_max_i16 v188, v188, 0
v_mfma_f32_16x16x32_bf16 v[52:55], a[112:115], a[100:103], v[52:55]
ds_read_b64_tr_b16 a[138:139], v234 offset:32392
v_pk_max_i16 v189, v189, 0
v_mfma_f32_16x16x32_bf16 v[56:59], a[112:115], a[104:107], v[56:59]
buffer_store_dwordx4 v[164:167], v235, s[48:51], s75 offen offset:1024 sc1
v_mfma_f32_16x16x32_bf16 v[60:63], a[112:115], a[108:111], v[60:63]
v_accvgpr_read_b32 v220, a44
v_accvgpr_read_b32 v221, a45
s_waitcnt lgkmcnt(10)
v_mfma_f32_16x16x32_bf16 v[64:67], a[116:119], a[48:51], v[64:67]
ds_read_b64_tr_b16 a[140:141], v239 offset:18568
v_accvgpr_read_b32 v222, a46
v_mfma_f32_16x16x32_bf16 v[68:71], a[116:119], a[52:55], v[68:71]
ds_read_b64_tr_b16 a[142:143], v239 offset:23176
v_accvgpr_read_b32 v223, a47
v_mfma_f32_16x16x32_bf16 v[72:75], a[116:119], a[56:59], v[72:75]
v_cvt_pk_bf16_f32 v190, v220, v221
v_cvt_pk_bf16_f32 v191, v222, v223
v_mfma_f32_16x16x32_bf16 v[76:79], a[116:119], a[60:63], v[76:79]
v_pk_max_i16 v190, v190, 0
v_pk_max_i16 v191, v191, 0
s_waitcnt lgkmcnt(10)
v_mfma_f32_16x16x32_bf16 v[64:67], a[120:123], a[64:67], v[64:67]
ds_read_b64_tr_b16 a[144:145], v239 offset:27784
v_cvt_pk_bf16_f32 v0, v0, v1
v_mfma_f32_16x16x32_bf16 v[68:71], a[120:123], a[68:71], v[68:71]
ds_read_b64_tr_b16 a[146:147], v239 offset:32392
v_cvt_pk_bf16_f32 v1, v2, v3
v_mfma_f32_16x16x32_bf16 v[72:75], a[120:123], a[72:75], v[72:75]
v_cvt_pk_bf16_f32 v2, v16, v17
v_cvt_pk_bf16_f32 v3, v18, v19
v_mfma_f32_16x16x32_bf16 v[76:79], a[120:123], a[76:79], v[76:79]
v_accvgpr_write_b32 a192, v0
v_accvgpr_write_b32 a193, v1
s_waitcnt lgkmcnt(10)
v_mfma_f32_16x16x32_bf16 v[64:67], a[124:127], a[80:83], v[64:67]
ds_read_b64_tr_b16 a[148:149], v234 offset:18624
v_accvgpr_write_b32 a194, v2
v_mfma_f32_16x16x32_bf16 v[68:71], a[124:127], a[84:87], v[68:71]
ds_read_b64_tr_b16 a[150:151], v234 offset:23232
v_accvgpr_write_b32 a195, v3
v_mfma_f32_16x16x32_bf16 v[72:75], a[124:127], a[88:91], v[72:75]
v_cvt_pk_bf16_f32 v4, v4, v5
v_cvt_pk_bf16_f32 v5, v6, v7
v_mfma_f32_16x16x32_bf16 v[76:79], a[124:127], a[92:95], v[76:79]
v_cvt_pk_bf16_f32 v6, v20, v21
v_cvt_pk_bf16_f32 v7, v22, v23
s_waitcnt lgkmcnt(10)
v_mfma_f32_16x16x32_bf16 v[64:67], a[128:131], a[96:99], v[64:67]
ds_read_b64_tr_b16 a[152:153], v234 offset:27840
v_accvgpr_write_b32 a196, v4
v_mfma_f32_16x16x32_bf16 v[68:71], a[128:131], a[100:103], v[68:71]
ds_read_b64_tr_b16 a[154:155], v234 offset:32448
v_accvgpr_write_b32 a197, v5
v_mfma_f32_16x16x32_bf16 v[72:75], a[128:131], a[104:107], v[72:75]
v_accvgpr_write_b32 a198, v6
v_accvgpr_write_b32 a199, v7
v_mfma_f32_16x16x32_bf16 v[76:79], a[128:131], a[108:111], v[76:79]
v_cvt_pk_bf16_f32 v8, v8, v9
v_cvt_pk_bf16_f32 v9, v10, v11
s_waitcnt lgkmcnt(10)
v_mfma_f32_16x16x32_bf16 v[80:83], a[132:135], a[48:51], v[80:83]
ds_read_b64_tr_b16 a[156:157], v239 offset:18624
v_cvt_pk_bf16_f32 v10, v24, v25
v_mfma_f32_16x16x32_bf16 v[84:87], a[132:135], a[52:55], v[84:87]
ds_read_b64_tr_b16 a[158:159], v239 offset:23232
v_cvt_pk_bf16_f32 v11, v26, v27
v_mfma_f32_16x16x32_bf16 v[88:91], a[132:135], a[56:59], v[88:91]
v_accvgpr_write_b32 a200, v8
v_accvgpr_write_b32 a201, v9
v_mfma_f32_16x16x32_bf16 v[92:95], a[132:135], a[60:63], v[92:95]
v_accvgpr_write_b32 a202, v10
v_accvgpr_write_b32 a203, v11
s_waitcnt lgkmcnt(10)
v_mfma_f32_16x16x32_bf16 v[80:83], a[136:139], a[64:67], v[80:83]
ds_read_b64_tr_b16 a[160:161], v239 offset:27840
v_cvt_pk_bf16_f32 v12, v12, v13
v_mfma_f32_16x16x32_bf16 v[84:87], a[136:139], a[68:71], v[84:87]
ds_read_b64_tr_b16 a[162:163], v239 offset:32448
v_cvt_pk_bf16_f32 v13, v14, v15
v_mfma_f32_16x16x32_bf16 v[88:91], a[136:139], a[72:75], v[88:91]
buffer_store_dwordx4 v[168:171], v235, s[48:51], s75 offen offset:2048 sc1
v_mfma_f32_16x16x32_bf16 v[92:95], a[136:139], a[76:79], v[92:95]
v_cvt_pk_bf16_f32 v14, v28, v29
v_cvt_pk_bf16_f32 v15, v30, v31
s_waitcnt lgkmcnt(10)
v_mfma_f32_16x16x32_bf16 v[80:83], a[140:143], a[80:83], v[80:83]
ds_read_b64_tr_b16 a[164:165], v234 offset:18632
v_accvgpr_write_b32 a204, v12
v_mfma_f32_16x16x32_bf16 v[84:87], a[140:143], a[84:87], v[84:87]
ds_read_b64_tr_b16 a[166:167], v234 offset:23240
v_accvgpr_write_b32 a205, v13
v_mfma_f32_16x16x32_bf16 v[88:91], a[140:143], a[88:91], v[88:91]
v_accvgpr_write_b32 a206, v14
v_accvgpr_write_b32 a207, v15
v_mfma_f32_16x16x32_bf16 v[92:95], a[140:143], a[92:95], v[92:95]
v_cvt_pk_bf16_f32 v32, v32, v33
v_cvt_pk_bf16_f32 v33, v34, v35
s_waitcnt lgkmcnt(10)
v_mfma_f32_16x16x32_bf16 v[80:83], a[144:147], a[96:99], v[80:83]
ds_read_b64_tr_b16 a[168:169], v234 offset:27848
v_cvt_pk_bf16_f32 v34, v48, v49
v_mfma_f32_16x16x32_bf16 v[84:87], a[144:147], a[100:103], v[84:87]
ds_read_b64_tr_b16 a[170:171], v234 offset:32456
v_cvt_pk_bf16_f32 v35, v50, v51
v_mfma_f32_16x16x32_bf16 v[88:91], a[144:147], a[104:107], v[88:91]
v_accvgpr_write_b32 a208, v32
v_accvgpr_write_b32 a209, v33
v_mfma_f32_16x16x32_bf16 v[92:95], a[144:147], a[108:111], v[92:95]
v_accvgpr_write_b32 a210, v34
v_accvgpr_write_b32 a211, v35
s_waitcnt lgkmcnt(10)
v_mfma_f32_16x16x32_bf16 v[96:99], a[148:151], a[48:51], v[96:99]
ds_read_b64_tr_b16 a[172:173], v239 offset:18632
v_cvt_pk_bf16_f32 v36, v36, v37
v_mfma_f32_16x16x32_bf16 v[100:103], a[148:151], a[52:55], v[100:103]
ds_read_b64_tr_b16 a[174:175], v239 offset:23240
v_cvt_pk_bf16_f32 v37, v38, v39
v_mfma_f32_16x16x32_bf16 v[104:107], a[148:151], a[56:59], v[104:107]
v_cvt_pk_bf16_f32 v38, v52, v53
v_cvt_pk_bf16_f32 v39, v54, v55
v_mfma_f32_16x16x32_bf16 v[108:111], a[148:151], a[60:63], v[108:111]
v_accvgpr_write_b32 a212, v36
v_accvgpr_write_b32 a213, v37
s_waitcnt lgkmcnt(10)
v_mfma_f32_16x16x32_bf16 v[96:99], a[152:155], a[64:67], v[96:99]
ds_read_b64_tr_b16 a[176:177], v239 offset:27848
v_accvgpr_write_b32 a214, v38
v_mfma_f32_16x16x32_bf16 v[100:103], a[152:155], a[68:71], v[100:103]
ds_read_b64_tr_b16 a[178:179], v239 offset:32456
v_accvgpr_write_b32 a215, v39
v_mfma_f32_16x16x32_bf16 v[104:107], a[152:155], a[72:75], v[104:107]
v_cvt_pk_bf16_f32 v40, v40, v41
v_cvt_pk_bf16_f32 v41, v42, v43
v_mfma_f32_16x16x32_bf16 v[108:111], a[152:155], a[76:79], v[108:111]
v_cvt_pk_bf16_f32 v42, v56, v57
v_cvt_pk_bf16_f32 v43, v58, v59
s_waitcnt lgkmcnt(10)
v_mfma_f32_16x16x32_bf16 v[96:99], a[156:159], a[80:83], v[96:99]
v_and_b32 v253, 15, v236
v_lshrrev_b32 v249, 4, v236
s_add_u32 s67, s67, 36864
s_cmp_eq_u32 s67, 147456
s_cselect_b32 s66, s76, 36864
s_cselect_b32 s67, 0, s67
v_mfma_f32_16x16x32_bf16 v[100:103], a[156:159], a[84:87], v[100:103]
v_mul_u32_u24 v253, 288, v253
v_add_u32 v233, s66, v238
v_mfma_f32_16x16x32_bf16 v[104:107], a[156:159], a[88:91], v[104:107]
v_accvgpr_write_b32 a216, v40
v_accvgpr_write_b32 a217, v41
v_mfma_f32_16x16x32_bf16 v[108:111], a[156:159], a[92:95], v[108:111]
v_accvgpr_write_b32 a218, v42
v_accvgpr_write_b32 a219, v43
s_waitcnt lgkmcnt(8)
v_mfma_f32_16x16x32_bf16 v[96:99], a[160:163], a[96:99], v[96:99]
s_add_u32 s77, %[lds], s67
v_lshl_add_u32 v253, v249, 4, v253
v_mfma_f32_16x16x32_bf16 v[100:103], a[160:163], a[100:103], v[100:103]
v_add_u32 v234, s77, v253
v_mfma_f32_16x16x32_bf16 v[104:107], a[160:163], a[104:107], v[104:107]
buffer_store_dwordx4 v[172:175], v235, s[48:51], s75 offen offset:3072 sc1
s_add_u32 s75, s75, 4096
v_mfma_f32_16x16x32_bf16 v[108:111], a[160:163], a[108:111], v[108:111]
v_cvt_pk_bf16_f32 v44, v44, v45
v_cvt_pk_bf16_f32 v45, v46, v47
s_waitcnt lgkmcnt(6)
v_mfma_f32_16x16x32_bf16 v[112:115], a[164:167], a[48:51], v[112:115]
s_waitcnt vmcnt(17) lgkmcnt(0)
s_barrier
ds_read_b128 a[112:115], v234 offset:0
v_cvt_pk_bf16_f32 v46, v60, v61
v_mfma_f32_16x16x32_bf16 v[116:119], a[164:167], a[52:55], v[116:119]
ds_read_b128 a[116:119], v234 offset:64
v_cvt_pk_bf16_f32 v47, v62, v63
v_mfma_f32_16x16x32_bf16 v[120:123], a[164:167], a[56:59], v[120:123]
v_accvgpr_write_b32 a220, v44
v_accvgpr_write_b32 a221, v45
v_mfma_f32_16x16x32_bf16 v[124:127], a[164:167], a[60:63], v[124:127]
v_accvgpr_write_b32 a222, v46
v_accvgpr_write_b32 a223, v47
s_waitcnt lgkmcnt(6)
v_mfma_f32_16x16x32_bf16 v[112:115], a[168:171], a[64:67], v[112:115]
ds_read_b128 a[120:123], v234 offset:128
v_cvt_pk_bf16_f32 v64, v64, v65
v_mfma_f32_16x16x32_bf16 v[116:119], a[168:171], a[68:71], v[116:119]
ds_read_b128 a[124:127], v234 offset:192
v_cvt_pk_bf16_f32 v65, v66, v67
v_mfma_f32_16x16x32_bf16 v[120:123], a[168:171], a[72:75], v[120:123]
v_cvt_pk_bf16_f32 v66, v80, v81
v_cvt_pk_bf16_f32 v67, v82, v83
v_mfma_f32_16x16x32_bf16 v[124:127], a[168:171], a[76:79], v[124:127]
v_accvgpr_write_b32 a224, v64
v_accvgpr_write_b32 a225, v65
s_waitcnt lgkmcnt(6)
v_mfma_f32_16x16x32_bf16 v[112:115], a[172:175], a[80:83], v[112:115]
ds_read_b128 a[128:131], v234 offset:4608
v_accvgpr_write_b32 a226, v66
v_mfma_f32_16x16x32_bf16 v[116:119], a[172:175], a[84:87], v[116:119]
)"
R"(
ds_read_b128 a[132:135], v234 offset:4672
v_accvgpr_write_b32 a227, v67
v_mfma_f32_16x16x32_bf16 v[120:123], a[172:175], a[88:91], v[120:123]
v_cvt_pk_bf16_f32 v68, v68, v69
v_cvt_pk_bf16_f32 v69, v70, v71
v_mfma_f32_16x16x32_bf16 v[124:127], a[172:175], a[92:95], v[124:127]
v_cvt_pk_bf16_f32 v70, v84, v85
v_cvt_pk_bf16_f32 v71, v86, v87
s_waitcnt lgkmcnt(6)
v_mfma_f32_16x16x32_bf16 v[112:115], a[176:179], a[96:99], v[112:115]
ds_read_b128 a[136:139], v234 offset:4736
v_accvgpr_write_b32 a228, v68
v_mfma_f32_16x16x32_bf16 v[116:119], a[176:179], a[100:103], v[116:119]
ds_read_b128 a[140:143], v234 offset:4800
v_accvgpr_write_b32 a229, v69
v_mfma_f32_16x16x32_bf16 v[120:123], a[176:179], a[104:107], v[120:123]
v_accvgpr_write_b32 a230, v70
v_accvgpr_write_b32 a231, v71
v_mfma_f32_16x16x32_bf16 v[124:127], a[176:179], a[108:111], v[124:127]
v_cvt_pk_bf16_f32 v72, v72, v73
v_cvt_pk_bf16_f32 v73, v74, v75
EP1:
v_cvt_pk_bf16_f32 v74, v88, v89
v_cvt_pk_bf16_f32 v75, v90, v91
v_accvgpr_write_b32 a232, v72
v_accvgpr_write_b32 a233, v73
v_accvgpr_write_b32 a234, v74
v_accvgpr_write_b32 a235, v75
v_cvt_pk_bf16_f32 v76, v76, v77
v_cvt_pk_bf16_f32 v77, v78, v79
v_cvt_pk_bf16_f32 v78, v92, v93
v_cvt_pk_bf16_f32 v79, v94, v95
v_accvgpr_write_b32 a236, v76
v_accvgpr_write_b32 a237, v77
v_accvgpr_write_b32 a238, v78
v_accvgpr_write_b32 a239, v79
v_cvt_pk_bf16_f32 v96, v96, v97
v_cvt_pk_bf16_f32 v97, v98, v99
v_cvt_pk_bf16_f32 v98, v112, v113
v_cvt_pk_bf16_f32 v99, v114, v115
v_accvgpr_write_b32 a240, v96
v_accvgpr_write_b32 a241, v97
v_accvgpr_write_b32 a242, v98
v_accvgpr_write_b32 a243, v99
v_cvt_pk_bf16_f32 v100, v100, v101
v_cvt_pk_bf16_f32 v101, v102, v103
v_cvt_pk_bf16_f32 v102, v116, v117
v_cvt_pk_bf16_f32 v103, v118, v119
v_accvgpr_write_b32 a244, v100
v_accvgpr_write_b32 a245, v101
v_accvgpr_write_b32 a246, v102
v_accvgpr_write_b32 a247, v103
v_cvt_pk_bf16_f32 v104, v104, v105
v_cvt_pk_bf16_f32 v105, v106, v107
v_cvt_pk_bf16_f32 v106, v120, v121
v_cvt_pk_bf16_f32 v107, v122, v123
v_accvgpr_write_b32 a248, v104
v_accvgpr_write_b32 a249, v105
v_accvgpr_write_b32 a250, v106
v_accvgpr_write_b32 a251, v107
v_cvt_pk_bf16_f32 v108, v108, v109
v_cvt_pk_bf16_f32 v109, v110, v111
v_cvt_pk_bf16_f32 v110, v124, v125
v_cvt_pk_bf16_f32 v111, v126, v127
v_accvgpr_write_b32 a252, v108
v_accvgpr_write_b32 a253, v109
v_accvgpr_write_b32 a254, v110
v_accvgpr_write_b32 a255, v111
s_mov_b32 s87, 1073758208
v_mov_b32 v180, 1065369472
v_mov_b32 v181, 1065369472
v_mov_b32 v182, 1065369472
v_mov_b32 v183, 1065369472
s_lshl_b32 s92, s34, 8
s_lshl_b32 s77, %[wid], 3
s_add_u32 s92, s92, s77
s_and_b32 s92, s92, 1023
s_add_u32 s92, s92, 1024
s_waitcnt lgkmcnt(0)
v_mfma_f32_16x16x32_bf16 v[128:131], a[112:115], a[192:195], 0
ds_read_b64_tr_b16 a[48:49], v233 offset:18432
ds_read_b64_tr_b16 a[50:51], v233 offset:23040
s_add_u32 m0, s70, s69
v_mfma_f32_16x16x32_bf16 v[132:135], a[128:131], a[192:195], 0
buffer_load_dwordx4 v224, s[40:43], s64 offen lds
v_mfma_f32_16x16x32_bf16 v[128:131], a[116:119], a[208:211], v[128:131]
ds_read_b64_tr_b16 a[52:53], v233 offset:18464
ds_read_b64_tr_b16 a[54:55], v233 offset:23072
s_add_u32 m0, m0, 1024
v_mfma_f32_16x16x32_bf16 v[132:135], a[132:135], a[208:211], v[132:135]
buffer_load_dwordx4 v225, s[40:43], s64 offen lds
v_mfma_f32_16x16x32_bf16 v[128:131], a[120:123], a[224:227], v[128:131]
ds_read_b64_tr_b16 a[56:57], v233 offset:18496
ds_read_b64_tr_b16 a[58:59], v233 offset:23104
s_add_u32 m0, m0, 1024
v_mfma_f32_16x16x32_bf16 v[132:135], a[136:139], a[224:227], v[132:135]
buffer_load_dwordx4 v226, s[40:43], s64 offen lds
v_mfma_f32_16x16x32_bf16 v[128:131], a[124:127], a[240:243], v[128:131]
ds_read_b64_tr_b16 a[60:61], v233 offset:18528
ds_read_b64_tr_b16 a[62:63], v233 offset:23136
s_add_u32 m0, m0, 1024
v_mfma_f32_16x16x32_bf16 v[132:135], a[140:143], a[240:243], v[132:135]
buffer_load_dwordx4 v227, s[40:43], s64 offen lds
v_mfma_f32_16x16x32_bf16 v[136:139], a[112:115], a[196:199], 0
ds_read_b64_tr_b16 a[64:65], v233 offset:18560
s_nop 0
v_max3_f32 v177, v128, v129, v130
s_nop 2
v_max3_f32 v178, v131, v132, v133
v_max3_f32 v177, v177, v134, v135
v_max_f32 v177, v177, v178
v_mov_b32 v178, v177
v_mfma_f32_16x16x32_bf16 v[140:143], a[128:131], a[196:199], 0
ds_read_b64_tr_b16 a[66:67], v233 offset:23168
v_permlane16_swap_b32 v177, v178
v_max_f32 v177, v177, v178
v_mov_b32 v178, v177
s_nop 1
v_permlane32_swap_b32 v177, v178
v_mfma_f32_16x16x32_bf16 v[136:139], a[116:119], a[212:215], v[136:139]
ds_read_b64_tr_b16 a[68:69], v233 offset:18592
v_max_f32 v177, v177, v178
v_add_f32 v177, 0x41500000, v177
v_sub_f32 v128, v128, v177
v_sub_f32 v129, v129, v177
v_sub_f32 v130, v130, v177
v_mfma_f32_16x16x32_bf16 v[140:143], a[132:135], a[212:215], v[140:143]
ds_read_b64_tr_b16 a[70:71], v233 offset:23200
v_sub_f32 v131, v131, v177
v_sub_f32 v132, v132, v177
v_sub_f32 v133, v133, v177
v_sub_f32 v134, v134, v177
s_waitcnt lgkmcnt(6)
v_mfma_f32_16x16x32_bf16 v[136:139], a[120:123], a[228:231], v[136:139]
ds_read_b64_tr_b16 a[72:73], v233 offset:18624
v_sub_f32 v135, v135, v177
v_sub_f32 v160, 0, v177
v_sub_f32 v161, 0, v177
v_sub_f32 v162, 0, v177
v_mfma_f32_16x16x32_bf16 v[140:143], a[136:139], a[228:231], v[140:143]
ds_read_b64_tr_b16 a[74:75], v233 offset:23232
v_sub_f32 v163, 0, v177
v_exp_f32 v144, v128
v_exp_f32 v145, v129
v_exp_f32 v146, v130
v_exp_f32 v147, v131
v_mfma_f32_16x16x32_bf16 v[136:139], a[124:127], a[244:247], v[136:139]
ds_read_b64_tr_b16 a[76:77], v233 offset:18656
v_exp_f32 v148, v132
v_exp_f32 v149, v133
v_exp_f32 v150, v134
v_exp_f32 v151, v135
v_mfma_f32_16x16x32_bf16 v[140:143], a[140:143], a[244:247], v[140:143]
ds_read_b64_tr_b16 a[78:79], v233 offset:23264
v_cvt_pk_bf16_f32 v152, v144, v145
v_cvt_pk_bf16_f32 v153, v146, v147
v_cvt_pk_bf16_f32 v154, v148, v149
v_cvt_pk_bf16_f32 v155, v150, v151
v_mfma_f32_16x16x32_bf16 v[128:131], a[112:115], a[200:203], 0
s_add_u32 m0, m0, 1024
v_max3_f32 v177, v136, v137, v138
v_max3_f32 v178, v139, v140, v141
v_max3_f32 v177, v177, v142, v143
v_mfma_f32_16x16x32_bf16 v[132:135], a[128:131], a[200:203], 0
buffer_load_dwordx4 v228, s[40:43], s64 offen lds
v_max_f32 v177, v177, v178
v_mov_b32 v178, v177
v_mfma_f32_16x16x32_bf16 v[128:131], a[116:119], a[216:219], v[128:131]
s_nop 0
v_permlane16_swap_b32 v177, v178
v_max_f32 v177, v177, v178
v_mfma_f32_16x16x32_bf16 v[132:135], a[132:135], a[216:219], v[132:135]
s_add_u32 m0, m0, 1024
v_mov_b32 v178, v177
s_nop 1
v_permlane32_swap_b32 v177, v178
v_mfma_f32_16x16x32_bf16 v[128:131], a[120:123], a[232:235], v[128:131]
buffer_load_dwordx4 v229, s[40:43], s64 offen lds
v_max_f32 v177, v177, v178
v_add_f32 v177, 0x41500000, v177
v_mfma_f32_16x16x32_bf16 v[132:135], a[136:139], a[232:235], v[132:135]
v_sub_f32 v136, v136, v177
v_sub_f32 v137, v137, v177
v_mfma_f32_16x16x32_bf16 v[128:131], a[124:127], a[248:251], v[128:131]
s_add_u32 m0, m0, 1024
v_sub_f32 v138, v138, v177
v_sub_f32 v139, v139, v177
v_mfma_f32_16x16x32_bf16 v[132:135], a[140:143], a[248:251], v[132:135]
buffer_load_dwordx4 v230, s[40:43], s64 offen lds
v_sub_f32 v140, v140, v177
v_sub_f32 v141, v141, v177
s_waitcnt lgkmcnt(0)
v_mfma_f32_16x16x32_bf16 v[0:3], a[48:51], v[152:155], 0
v_sub_f32 v142, v142, v177
v_sub_f32 v143, v143, v177
v_mfma_f32_16x16x32_bf16 v[16:19], a[52:55], v[152:155], 0
ds_read_b128 a[144:147], v234 offset:9216
v_sub_f32 v164, 0, v177
v_sub_f32 v165, 0, v177
v_mfma_f32_16x16x32_bf16 v[32:35], a[56:59], v[152:155], 0
ds_read_b128 a[148:151], v234 offset:9280
v_sub_f32 v166, 0, v177
v_sub_f32 v167, 0, v177
v_mfma_f32_16x16x32_bf16 v[48:51], a[60:63], v[152:155], 0
v_exp_f32 v144, v136
v_exp_f32 v145, v137
v_mfma_f32_16x16x32_bf16 v[64:67], a[64:67], v[152:155], 0
ds_read_b128 a[152:155], v234 offset:9344
v_exp_f32 v146, v138
v_exp_f32 v147, v139
v_mfma_f32_16x16x32_bf16 v[80:83], a[68:71], v[152:155], 0
ds_read_b128 a[156:159], v234 offset:9408
v_exp_f32 v148, v140
v_exp_f32 v149, v141
v_mfma_f32_16x16x32_bf16 v[96:99], a[72:75], v[152:155], 0
v_exp_f32 v150, v142
v_exp_f32 v151, v143
v_mfma_f32_16x16x32_bf16 v[112:115], a[76:79], v[152:155], 0
ds_read_b128 a[160:163], v234 offset:13824
v_cvt_pk_bf16_f32 v156, v144, v145
v_cvt_pk_bf16_f32 v157, v146, v147
v_mfma_f32_16x16x32_bf16 v[208:211], v[180:183], v[152:155], 0
v_cvt_pk_bf16_f32 v158, v148, v149
v_cvt_pk_bf16_f32 v159, v150, v151
v_mfma_f32_16x16x32_bf16 v[136:139], a[112:115], a[204:207], 0
s_add_u32 m0, m0, 1024
v_max3_f32 v177, v128, v129, v130
v_max3_f32 v178, v131, v132, v133
v_max3_f32 v177, v177, v134, v135
v_mfma_f32_16x16x32_bf16 v[140:143], a[128:131], a[204:207], 0
buffer_load_dwordx4 v231, s[40:43], s64 offen lds
v_max_f32 v177, v177, v178
v_mov_b32 v178, v177
v_mfma_f32_16x16x32_bf16 v[136:139], a[116:119], a[220:223], v[136:139]
s_nop 0
v_permlane16_swap_b32 v177, v178
v_max_f32 v177, v177, v178
v_mfma_f32_16x16x32_bf16 v[140:143], a[132:135], a[220:223], v[140:143]
s_add_u32 m0, m0, 1024
v_mov_b32 v178, v177
s_nop 1
v_permlane32_swap_b32 v177, v178
v_mfma_f32_16x16x32_bf16 v[136:139], a[120:123], a[236:239], v[136:139]
buffer_load_dwordx4 v232, s[40:43], s64 offen lds
s_add_u32 s64, s64, s65
s_add_u32 s69, s69, 36864
s_cmp_eq_u32 s69, 147456
s_cselect_b32 s69, 0, s69
v_max_f32 v177, v177, v178
v_add_f32 v177, 0x41500000, v177
v_mfma_f32_16x16x32_bf16 v[140:143], a[136:139], a[236:239], v[140:143]
v_sub_f32 v128, v128, v177
v_sub_f32 v129, v129, v177
v_mfma_f32_16x16x32_bf16 v[136:139], a[124:127], a[252:255], v[136:139]
s_add_u32 m0, s70, s69
v_sub_f32 v130, v130, v177
v_sub_f32 v131, v131, v177
v_mfma_f32_16x16x32_bf16 v[140:143], a[140:143], a[252:255], v[140:143]
buffer_load_dwordx4 v224, s[40:43], s64 offen lds
v_sub_f32 v132, v132, v177
v_sub_f32 v133, v133, v177
v_mfma_f32_16x16x32_bf16 v[4:7], a[48:51], v[156:159], 0
buffer_store_dwordx4 v[192:195], v235, s[48:51], s75 offen offset:0 sc1
v_sub_f32 v134, v134, v177
v_sub_f32 v135, v135, v177
v_mfma_f32_16x16x32_bf16 v[20:23], a[52:55], v[156:159], 0
ds_read_b128 a[164:167], v234 offset:13888
v_sub_f32 v168, 0, v177
v_sub_f32 v169, 0, v177
v_mfma_f32_16x16x32_bf16 v[36:39], a[56:59], v[156:159], 0
ds_read_b128 a[168:171], v234 offset:13952
v_sub_f32 v170, 0, v177
v_sub_f32 v171, 0, v177
v_mfma_f32_16x16x32_bf16 v[52:55], a[60:63], v[156:159], 0
ds_read_b128 a[172:175], v234 offset:14016
v_exp_f32 v144, v128
v_exp_f32 v145, v129
v_mfma_f32_16x16x32_bf16 v[68:71], a[64:67], v[156:159], 0
ds_read_b64_tr_b16 a[80:81], v233 offset:27648
v_exp_f32 v146, v130
v_exp_f32 v147, v131
v_mfma_f32_16x16x32_bf16 v[84:87], a[68:71], v[156:159], 0
ds_read_b64_tr_b16 a[82:83], v233 offset:32256
v_exp_f32 v148, v132
v_exp_f32 v149, v133
v_mfma_f32_16x16x32_bf16 v[100:103], a[72:75], v[156:159], 0
ds_read_b64_tr_b16 a[84:85], v233 offset:27680
v_exp_f32 v150, v134
v_exp_f32 v151, v135
v_mfma_f32_16x16x32_bf16 v[116:119], a[76:79], v[156:159], 0
ds_read_b64_tr_b16 a[86:87], v233 offset:32288
v_cvt_pk_bf16_f32 v152, v144, v145
v_cvt_pk_bf16_f32 v153, v146, v147
v_mfma_f32_16x16x32_bf16 v[212:215], v[180:183], v[156:159], 0
v_cvt_pk_bf16_f32 v154, v148, v149
v_cvt_pk_bf16_f32 v155, v150, v151
s_waitcnt lgkmcnt(4)
v_mfma_f32_16x16x32_bf16 v[128:131], a[144:147], a[192:195], v[160:163]
s_add_u32 m0, m0, 1024
v_max3_f32 v177, v136, v137, v138
v_max3_f32 v178, v139, v140, v141
v_max3_f32 v177, v177, v142, v143
v_mfma_f32_16x16x32_bf16 v[132:135], a[160:163], a[192:195], v[160:163]
buffer_load_dwordx4 v225, s[40:43], s64 offen lds
v_max_f32 v177, v177, v178
v_mov_b32 v178, v177
v_mfma_f32_16x16x32_bf16 v[128:131], a[148:151], a[208:211], v[128:131]
s_nop 0
v_permlane16_swap_b32 v177, v178
v_max_f32 v177, v177, v178
v_mfma_f32_16x16x32_bf16 v[132:135], a[164:167], a[208:211], v[132:135]
s_add_u32 m0, m0, 1024
v_mov_b32 v178, v177
s_nop 1
v_permlane32_swap_b32 v177, v178
v_mfma_f32_16x16x32_bf16 v[128:131], a[152:155], a[224:227], v[128:131]
buffer_load_dwordx4 v226, s[40:43], s64 offen lds
v_max_f32 v177, v177, v178
v_add_f32 v177, 0x41500000, v177
v_mfma_f32_16x16x32_bf16 v[132:135], a[168:171], a[224:227], v[132:135]
v_sub_f32 v136, v136, v177
v_sub_f32 v137, v137, v177
v_mfma_f32_16x16x32_bf16 v[128:131], a[156:159], a[240:243], v[128:131]
s_add_u32 m0, m0, 1024
v_sub_f32 v138, v138, v177
v_sub_f32 v139, v139, v177
v_mfma_f32_16x16x32_bf16 v[132:135], a[172:175], a[240:243], v[132:135]
buffer_load_dwordx4 v227, s[40:43], s64 offen lds
v_sub_f32 v140, v140, v177
v_sub_f32 v141, v141, v177
v_mfma_f32_16x16x32_bf16 v[8:11], a[48:51], v[152:155], 0
v_sub_f32 v142, v142, v177
v_sub_f32 v143, v143, v177
v_mfma_f32_16x16x32_bf16 v[24:27], a[52:55], v[152:155], 0
ds_read_b64_tr_b16 a[88:89], v233 offset:27712
v_sub_f32 v172, 0, v177
v_sub_f32 v173, 0, v177
v_mfma_f32_16x16x32_bf16 v[40:43], a[56:59], v[152:155], 0
ds_read_b64_tr_b16 a[90:91], v233 offset:32320
v_sub_f32 v174, 0, v177
v_sub_f32 v175, 0, v177
v_mfma_f32_16x16x32_bf16 v[56:59], a[60:63], v[152:155], 0
ds_read_b64_tr_b16 a[92:93], v233 offset:27744
v_exp_f32 v144, v136
v_exp_f32 v145, v137
v_mfma_f32_16x16x32_bf16 v[72:75], a[64:67], v[152:155], 0
v_exp_f32 v146, v138
v_exp_f32 v147, v139
v_mfma_f32_16x16x32_bf16 v[88:91], a[68:71], v[152:155], 0
ds_read_b64_tr_b16 a[94:95], v233 offset:32352
v_exp_f32 v148, v140
v_exp_f32 v149, v141
v_mfma_f32_16x16x32_bf16 v[104:107], a[72:75], v[152:155], 0
ds_read_b64_tr_b16 a[96:97], v233 offset:27776
v_exp_f32 v150, v142
v_exp_f32 v151, v143
v_mfma_f32_16x16x32_bf16 v[120:123], a[76:79], v[152:155], 0
ds_read_b64_tr_b16 a[98:99], v233 offset:32384
v_cvt_pk_bf16_f32 v156, v144, v145
v_cvt_pk_bf16_f32 v157, v146, v147
v_mfma_f32_16x16x32_bf16 v[216:219], v[180:183], v[152:155], 0
v_cvt_pk_bf16_f32 v158, v148, v149
v_cvt_pk_bf16_f32 v159, v150, v151
v_mfma_f32_16x16x32_bf16 v[136:139], a[144:147], a[196:199], v[164:167]
v_exp_f32 v144, v128
v_mfma_f32_16x16x32_bf16 v[140:143], a[160:163], a[196:199], v[164:167]
v_exp_f32 v145, v129
s_waitcnt lgkmcnt(6)
v_mfma_f32_16x16x32_bf16 v[136:139], a[148:151], a[212:215], v[136:139]
ds_read_b64_tr_b16 a[100:101], v233 offset:27808
v_cvt_pk_bf16_f32 v152, v144, v145
v_mfma_f32_16x16x32_bf16 v[140:143], a[164:167], a[212:215], v[140:143]
v_exp_f32 v146, v130
v_mfma_f32_16x16x32_bf16 v[136:139], a[152:155], a[228:231], v[136:139]
v_exp_f32 v147, v131
v_mfma_f32_16x16x32_bf16 v[140:143], a[168:171], a[228:231], v[140:143]
ds_read_b64_tr_b16 a[102:103], v233 offset:32416
v_cvt_pk_bf16_f32 v153, v146, v147
v_mfma_f32_16x16x32_bf16 v[136:139], a[156:159], a[244:247], v[136:139]
v_exp_f32 v148, v132
v_mfma_f32_16x16x32_bf16 v[140:143], a[172:175], a[244:247], v[140:143]
v_exp_f32 v149, v133
v_mfma_f32_16x16x32_bf16 v[12:15], a[48:51], v[156:159], 0
ds_read_b64_tr_b16 a[104:105], v233 offset:27840
v_cvt_pk_bf16_f32 v154, v148, v149
v_mfma_f32_16x16x32_bf16 v[28:31], a[52:55], v[156:159], 0
v_exp_f32 v150, v134
v_mfma_f32_16x16x32_bf16 v[44:47], a[56:59], v[156:159], 0
v_exp_f32 v151, v135
v_mfma_f32_16x16x32_bf16 v[60:63], a[60:63], v[156:159], 0
v_or3_b32 v176, v152, v153, v154
v_cvt_pk_bf16_f32 v155, v150, v151
v_mfma_f32_16x16x32_bf16 v[76:79], a[64:67], v[156:159], 0
ds_read_b64_tr_b16 a[106:107], v233 offset:32448
v_bitop3_b32 v176, v176, v155, s87 bitop3:0xa8
v_mfma_f32_16x16x32_bf16 v[92:95], a[68:71], v[156:159], 0
ds_read_b64_tr_b16 a[108:109], v233 offset:27872
s_add_u32 m0, m0, 1024
v_cmp_ne_u32 vcc, 0, v176
v_mfma_f32_16x16x32_bf16 v[108:111], a[72:75], v[156:159], 0
)"
R"(
buffer_load_dwordx4 v228, s[40:43], s64 offen lds
s_add_u32 s67, s67, 36864
s_cmp_eq_u32 s67, 147456
s_cselect_b32 s66, s76, 36864
s_cselect_b32 s67, 0, s67
v_mfma_f32_16x16x32_bf16 v[124:127], a[76:79], v[156:159], 0
buffer_store_dwordx4 v[196:199], v235, s[48:51], s75 offen offset:1024 sc1
s_cmp_lg_u64 vcc, 0
v_mfma_f32_16x16x32_bf16 v[220:223], v[180:183], v[156:159], 0
ds_read_b64_tr_b16 a[110:111], v233 offset:32480
s_cbranch_scc1 SLOW4
SLOWR4:
v_mfma_f32_16x16x32_bf16 v[128:131], a[144:147], a[200:203], v[168:171]
v_exp_f32 v144, v136
v_mfma_f32_16x16x32_bf16 v[132:135], a[160:163], a[200:203], v[168:171]
v_exp_f32 v145, v137
v_mfma_f32_16x16x32_bf16 v[128:131], a[148:151], a[216:219], v[128:131]
v_cvt_pk_bf16_f32 v156, v144, v145
v_mfma_f32_16x16x32_bf16 v[132:135], a[164:167], a[216:219], v[132:135]
v_exp_f32 v146, v138
v_mfma_f32_16x16x32_bf16 v[128:131], a[152:155], a[232:235], v[128:131]
v_exp_f32 v147, v139
v_mfma_f32_16x16x32_bf16 v[132:135], a[168:171], a[232:235], v[132:135]
s_waitcnt vmcnt(24) lgkmcnt(0)
s_barrier
v_cvt_pk_bf16_f32 v157, v146, v147
v_add_u32 v234, s66, v234
v_mfma_f32_16x16x32_bf16 v[128:131], a[156:159], a[248:251], v[128:131]
v_exp_f32 v148, v140
v_mfma_f32_16x16x32_bf16 v[132:135], a[172:175], a[248:251], v[132:135]
v_exp_f32 v149, v141
v_mfma_f32_16x16x32_bf16 v[0:3], a[80:83], v[152:155], v[0:3]
ds_read_b128 a[112:115], v234 offset:0
v_cvt_pk_bf16_f32 v158, v148, v149
v_mfma_f32_16x16x32_bf16 v[16:19], a[84:87], v[152:155], v[16:19]
v_exp_f32 v150, v142
v_mfma_f32_16x16x32_bf16 v[32:35], a[88:91], v[152:155], v[32:35]
v_exp_f32 v151, v143
v_mfma_f32_16x16x32_bf16 v[48:51], a[92:95], v[152:155], v[48:51]
v_or3_b32 v176, v156, v157, v158
v_cvt_pk_bf16_f32 v159, v150, v151
v_mfma_f32_16x16x32_bf16 v[64:67], a[96:99], v[152:155], v[64:67]
ds_read_b128 a[116:119], v234 offset:64
v_bitop3_b32 v176, v176, v159, s87 bitop3:0xa8
v_mfma_f32_16x16x32_bf16 v[80:83], a[100:103], v[152:155], v[80:83]
ds_read_b128 a[120:123], v234 offset:128
s_add_u32 m0, m0, 1024
v_cmp_ne_u32 vcc, 0, v176
v_mfma_f32_16x16x32_bf16 v[96:99], a[104:107], v[152:155], v[96:99]
buffer_load_dwordx4 v229, s[40:43], s64 offen lds
v_mfma_f32_16x16x32_bf16 v[112:115], a[108:111], v[152:155], v[112:115]
ds_read_b128 a[124:127], v234 offset:192
v_add_u32 v233, s66, v233
s_cmp_lg_u64 vcc, 0
v_mfma_f32_16x16x32_bf16 v[208:211], v[180:183], v[152:155], v[208:211]
ds_read_b128 a[128:131], v234 offset:4608
s_cbranch_scc1 SLOW5
SLOWR5:
v_mfma_f32_16x16x32_bf16 v[136:139], a[144:147], a[204:207], v[172:175]
v_exp_f32 v144, v128
v_mfma_f32_16x16x32_bf16 v[140:143], a[160:163], a[204:207], v[172:175]
v_exp_f32 v145, v129
v_mfma_f32_16x16x32_bf16 v[136:139], a[148:151], a[220:223], v[136:139]
ds_read_b128 a[132:135], v234 offset:4672
v_cvt_pk_bf16_f32 v152, v144, v145
v_mfma_f32_16x16x32_bf16 v[140:143], a[164:167], a[220:223], v[140:143]
v_exp_f32 v146, v130
v_mfma_f32_16x16x32_bf16 v[136:139], a[152:155], a[236:239], v[136:139]
v_exp_f32 v147, v131
v_mfma_f32_16x16x32_bf16 v[140:143], a[168:171], a[236:239], v[140:143]
ds_read_b128 a[136:139], v234 offset:4736
v_cvt_pk_bf16_f32 v153, v146, v147
v_mfma_f32_16x16x32_bf16 v[136:139], a[156:159], a[252:255], v[136:139]
v_exp_f32 v148, v132
v_mfma_f32_16x16x32_bf16 v[140:143], a[172:175], a[252:255], v[140:143]
v_exp_f32 v149, v133
v_mfma_f32_16x16x32_bf16 v[4:7], a[80:83], v[156:159], v[4:7]
ds_read_b128 a[140:143], v234 offset:4800
v_cvt_pk_bf16_f32 v154, v148, v149
v_mfma_f32_16x16x32_bf16 v[20:23], a[84:87], v[156:159], v[20:23]
v_exp_f32 v150, v134
v_mfma_f32_16x16x32_bf16 v[36:39], a[88:91], v[156:159], v[36:39]
v_exp_f32 v151, v135
v_mfma_f32_16x16x32_bf16 v[52:55], a[92:95], v[156:159], v[52:55]
v_or3_b32 v176, v152, v153, v154
v_cvt_pk_bf16_f32 v155, v150, v151
v_mfma_f32_16x16x32_bf16 v[68:71], a[96:99], v[156:159], v[68:71]
ds_read_b64_tr_b16 a[48:49], v233 offset:18432
v_bitop3_b32 v176, v176, v155, s87 bitop3:0xa8
v_mfma_f32_16x16x32_bf16 v[84:87], a[100:103], v[156:159], v[84:87]
ds_read_b64_tr_b16 a[50:51], v233 offset:23040
s_add_u32 m0, m0, 1024
v_cmp_ne_u32 vcc, 0, v176
v_mfma_f32_16x16x32_bf16 v[100:103], a[104:107], v[156:159], v[100:103]
buffer_load_dwordx4 v230, s[40:43], s64 offen lds
v_mfma_f32_16x16x32_bf16 v[116:119], a[108:111], v[156:159], v[116:119]
buffer_store_dwordx4 v[200:203], v235, s[48:51], s75 offen offset:2048 sc1
s_cmp_lg_u64 vcc, 0
v_mfma_f32_16x16x32_bf16 v[212:215], v[180:183], v[156:159], v[212:215]
ds_read_b64_tr_b16 a[52:53], v233 offset:18464
s_cbranch_scc1 SLOW6
SLOWR6:
s_waitcnt lgkmcnt(3)
v_mfma_f32_16x16x32_bf16 v[128:131], a[112:115], a[192:195], v[160:163]
v_exp_f32 v144, v136
v_mfma_f32_16x16x32_bf16 v[132:135], a[128:131], a[192:195], v[160:163]
v_exp_f32 v145, v137
v_mfma_f32_16x16x32_bf16 v[128:131], a[116:119], a[208:211], v[128:131]
ds_read_b64_tr_b16 a[54:55], v233 offset:23072
v_cvt_pk_bf16_f32 v156, v144, v145
v_mfma_f32_16x16x32_bf16 v[132:135], a[132:135], a[208:211], v[132:135]
v_exp_f32 v146, v138
v_mfma_f32_16x16x32_bf16 v[128:131], a[120:123], a[224:227], v[128:131]
v_exp_f32 v147, v139
v_mfma_f32_16x16x32_bf16 v[132:135], a[136:139], a[224:227], v[132:135]
ds_read_b64_tr_b16 a[56:57], v233 offset:18496
v_cvt_pk_bf16_f32 v157, v146, v147
v_mfma_f32_16x16x32_bf16 v[128:131], a[124:127], a[240:243], v[128:131]
v_exp_f32 v148, v140
v_mfma_f32_16x16x32_bf16 v[132:135], a[140:143], a[240:243], v[132:135]
v_exp_f32 v149, v141
v_mfma_f32_16x16x32_bf16 v[8:11], a[80:83], v[152:155], v[8:11]
ds_read_b64_tr_b16 a[58:59], v233 offset:23104
v_cvt_pk_bf16_f32 v158, v148, v149
v_mfma_f32_16x16x32_bf16 v[24:27], a[84:87], v[152:155], v[24:27]
v_exp_f32 v150, v142
v_mfma_f32_16x16x32_bf16 v[40:43], a[88:91], v[152:155], v[40:43]
v_exp_f32 v151, v143
v_mfma_f32_16x16x32_bf16 v[56:59], a[92:95], v[152:155], v[56:59]
v_or3_b32 v176, v156, v157, v158
v_cvt_pk_bf16_f32 v159, v150, v151
v_mfma_f32_16x16x32_bf16 v[72:75], a[96:99], v[152:155], v[72:75]
ds_read_b64_tr_b16 a[60:61], v233 offset:18528
v_bitop3_b32 v176, v176, v159, s87 bitop3:0xa8
v_mfma_f32_16x16x32_bf16 v[88:91], a[100:103], v[152:155], v[88:91]
ds_read_b64_tr_b16 a[62:63], v233 offset:23136
s_add_u32 m0, m0, 1024
v_cmp_ne_u32 vcc, 0, v176
v_mfma_f32_16x16x32_bf16 v[104:107], a[104:107], v[152:155], v[104:107]
buffer_load_dwordx4 v231, s[40:43], s64 offen lds
s_add_u32 m0, m0, 1024
v_mfma_f32_16x16x32_bf16 v[120:123], a[108:111], v[152:155], v[120:123]
buffer_load_dwordx4 v232, s[40:43], s64 offen lds
s_add_u32 s64, s64, s65
s_add_u32 s69, s69, 36864
s_cmp_eq_u32 s69, 147456
s_cselect_b32 s69, 0, s69
s_cmp_lg_u64 vcc, 0
v_mfma_f32_16x16x32_bf16 v[216:219], v[180:183], v[152:155], v[216:219]
ds_read_b64_tr_b16 a[64:65], v233 offset:18560
s_cbranch_scc1 SLOW7
SLOWR7:
v_mfma_f32_16x16x32_bf16 v[136:139], a[112:115], a[196:199], v[164:167]
v_exp_f32 v144, v128
v_mfma_f32_16x16x32_bf16 v[140:143], a[128:131], a[196:199], v[164:167]
v_exp_f32 v145, v129
s_waitcnt lgkmcnt(3)
v_mfma_f32_16x16x32_bf16 v[136:139], a[116:119], a[212:215], v[136:139]
ds_read_b64_tr_b16 a[66:67], v233 offset:23168
v_cvt_pk_bf16_f32 v152, v144, v145
v_mfma_f32_16x16x32_bf16 v[140:143], a[132:135], a[212:215], v[140:143]
v_exp_f32 v146, v130
v_mfma_f32_16x16x32_bf16 v[136:139], a[120:123], a[228:231], v[136:139]
v_exp_f32 v147, v131
v_mfma_f32_16x16x32_bf16 v[140:143], a[136:139], a[228:231], v[140:143]
ds_read_b64_tr_b16 a[68:69], v233 offset:18592
v_cvt_pk_bf16_f32 v153, v146, v147
v_mfma_f32_16x16x32_bf16 v[136:139], a[124:127], a[244:247], v[136:139]
v_exp_f32 v148, v132
v_mfma_f32_16x16x32_bf16 v[140:143], a[140:143], a[244:247], v[140:143]
v_exp_f32 v149, v133
v_mfma_f32_16x16x32_bf16 v[12:15], a[80:83], v[156:159], v[12:15]
ds_read_b64_tr_b16 a[70:71], v233 offset:23200
v_cvt_pk_bf16_f32 v154, v148, v149
v_mfma_f32_16x16x32_bf16 v[28:31], a[84:87], v[156:159], v[28:31]
v_exp_f32 v150, v134
v_mfma_f32_16x16x32_bf16 v[44:47], a[88:91], v[156:159], v[44:47]
v_exp_f32 v151, v135
v_mfma_f32_16x16x32_bf16 v[60:63], a[92:95], v[156:159], v[60:63]
v_or3_b32 v176, v152, v153, v154
v_cvt_pk_bf16_f32 v155, v150, v151
v_mfma_f32_16x16x32_bf16 v[76:79], a[96:99], v[156:159], v[76:79]
ds_read_b64_tr_b16 a[72:73], v233 offset:18624
v_bitop3_b32 v176, v176, v155, s87 bitop3:0xa8
v_mfma_f32_16x16x32_bf16 v[92:95], a[100:103], v[156:159], v[92:95]
ds_read_b64_tr_b16 a[74:75], v233 offset:23232
s_add_u32 m0, s70, s69
v_cmp_ne_u32 vcc, 0, v176
v_mfma_f32_16x16x32_bf16 v[108:111], a[104:107], v[156:159], v[108:111]
buffer_load_dwordx4 v224, s[40:43], s64 offen lds
v_mfma_f32_16x16x32_bf16 v[124:127], a[108:111], v[156:159], v[124:127]
buffer_store_dwordx4 v[204:207], v235, s[48:51], s75 offen offset:3072 sc1
s_add_u32 s75, s75, 4096
s_cmp_lg_u64 vcc, 0
v_mfma_f32_16x16x32_bf16 v[220:223], v[180:183], v[156:159], v[220:223]
ds_read_b64_tr_b16 a[76:77], v233 offset:18656
s_cbranch_scc1 SLOW8
SLOWR8:
v_mfma_f32_16x16x32_bf16 v[128:131], a[112:115], a[200:203], v[168:171]
v_exp_f32 v144, v136
v_mfma_f32_16x16x32_bf16 v[132:135], a[128:131], a[200:203], v[168:171]
v_exp_f32 v145, v137
v_mfma_f32_16x16x32_bf16 v[128:131], a[116:119], a[216:219], v[128:131]
ds_read_b64_tr_b16 a[78:79], v233 offset:23264
v_cvt_pk_bf16_f32 v156, v144, v145
v_mfma_f32_16x16x32_bf16 v[132:135], a[132:135], a[216:219], v[132:135]
v_exp_f32 v146, v138
v_mfma_f32_16x16x32_bf16 v[128:131], a[120:123], a[232:235], v[128:131]
v_exp_f32 v147, v139
v_mfma_f32_16x16x32_bf16 v[132:135], a[136:139], a[232:235], v[132:135]
ds_read_b128 a[144:147], v234 offset:9216
v_cvt_pk_bf16_f32 v157, v146, v147
v_mfma_f32_16x16x32_bf16 v[128:131], a[124:127], a[248:251], v[128:131]
v_exp_f32 v148, v140
v_mfma_f32_16x16x32_bf16 v[132:135], a[140:143], a[248:251], v[132:135]
v_exp_f32 v149, v141
s_waitcnt lgkmcnt(1)
v_mfma_f32_16x16x32_bf16 v[0:3], a[48:51], v[152:155], v[0:3]
ds_read_b128 a[148:151], v234 offset:9280
v_cvt_pk_bf16_f32 v158, v148, v149
v_mfma_f32_16x16x32_bf16 v[16:19], a[52:55], v[152:155], v[16:19]
v_exp_f32 v150, v142
v_mfma_f32_16x16x32_bf16 v[32:35], a[56:59], v[152:155], v[32:35]
v_exp_f32 v151, v143
v_mfma_f32_16x16x32_bf16 v[48:51], a[60:63], v[152:155], v[48:51]
v_or3_b32 v176, v156, v157, v158
v_cvt_pk_bf16_f32 v159, v150, v151
v_mfma_f32_16x16x32_bf16 v[64:67], a[64:67], v[152:155], v[64:67]
ds_read_b128 a[152:155], v234 offset:9344
v_bitop3_b32 v176, v176, v159, s87 bitop3:0xa8
v_mfma_f32_16x16x32_bf16 v[80:83], a[68:71], v[152:155], v[80:83]
ds_read_b128 a[156:159], v234 offset:9408
s_add_u32 m0, m0, 1024
v_cmp_ne_u32 vcc, 0, v176
v_mfma_f32_16x16x32_bf16 v[96:99], a[72:75], v[152:155], v[96:99]
buffer_load_dwordx4 v225, s[40:43], s64 offen lds
s_add_u32 m0, m0, 1024
v_mfma_f32_16x16x32_bf16 v[112:115], a[76:79], v[152:155], v[112:115]
buffer_load_dwordx4 v226, s[40:43], s64 offen lds
s_cmp_lg_u64 vcc, 0
v_mfma_f32_16x16x32_bf16 v[208:211], v[180:183], v[152:155], v[208:211]
ds_read_b128 a[160:163], v234 offset:13824
s_cbranch_scc1 SLOW9
SLOWR9:
v_mfma_f32_16x16x32_bf16 v[136:139], a[112:115], a[204:207], v[172:175]
v_exp_f32 v144, v128
v_mfma_f32_16x16x32_bf16 v[140:143], a[128:131], a[204:207], v[172:175]
v_exp_f32 v145, v129
v_mfma_f32_16x16x32_bf16 v[136:139], a[116:119], a[220:223], v[136:139]
ds_read_b128 a[164:167], v234 offset:13888
v_cvt_pk_bf16_f32 v152, v144, v145
v_mfma_f32_16x16x32_bf16 v[140:143], a[132:135], a[220:223], v[140:143]
v_exp_f32 v146, v130
v_mfma_f32_16x16x32_bf16 v[136:139], a[120:123], a[236:239], v[136:139]
v_exp_f32 v147, v131
v_mfma_f32_16x16x32_bf16 v[140:143], a[136:139], a[236:239], v[140:143]
ds_read_b128 a[168:171], v234 offset:13952
v_cvt_pk_bf16_f32 v153, v146, v147
v_mfma_f32_16x16x32_bf16 v[136:139], a[124:127], a[252:255], v[136:139]
v_exp_f32 v148, v132
v_mfma_f32_16x16x32_bf16 v[140:143], a[140:143], a[252:255], v[140:143]
v_exp_f32 v149, v133
v_mfma_f32_16x16x32_bf16 v[4:7], a[48:51], v[156:159], v[4:7]
ds_read_b128 a[172:175], v234 offset:14016
v_cvt_pk_bf16_f32 v154, v148, v149
v_mfma_f32_16x16x32_bf16 v[20:23], a[52:55], v[156:159], v[20:23]
v_exp_f32 v150, v134
v_mfma_f32_16x16x32_bf16 v[36:39], a[56:59], v[156:159], v[36:39]
v_exp_f32 v151, v135
v_mfma_f32_16x16x32_bf16 v[52:55], a[60:63], v[156:159], v[52:55]
v_or3_b32 v176, v152, v153, v154
v_cvt_pk_bf16_f32 v155, v150, v151
v_mfma_f32_16x16x32_bf16 v[68:71], a[64:67], v[156:159], v[68:71]
ds_read_b64_tr_b16 a[80:81], v233 offset:27648
v_bitop3_b32 v176, v176, v155, s87 bitop3:0xa8
v_mfma_f32_16x16x32_bf16 v[84:87], a[68:71], v[156:159], v[84:87]
ds_read_b64_tr_b16 a[82:83], v233 offset:32256
s_add_u32 m0, m0, 1024
v_cmp_ne_u32 vcc, 0, v176
v_mfma_f32_16x16x32_bf16 v[100:103], a[72:75], v[156:159], v[100:103]
buffer_load_dwordx4 v227, s[40:43], s64 offen lds
v_mfma_f32_16x16x32_bf16 v[116:119], a[76:79], v[156:159], v[116:119]
buffer_store_dwordx4 a[0:3], v235, s[48:51], s75 offen offset:0 sc1
s_cmp_lg_u64 vcc, 0
v_mfma_f32_16x16x32_bf16 v[212:215], v[180:183], v[156:159], v[212:215]
ds_read_b64_tr_b16 a[84:85], v233 offset:27680
ds_read_b64_tr_b16 a[86:87], v233 offset:32288
s_cbranch_scc1 SLOW10
SLOWR10:
s_waitcnt lgkmcnt(4)
v_mfma_f32_16x16x32_bf16 v[128:131], a[144:147], a[192:195], v[160:163]
v_exp_f32 v144, v136
v_mfma_f32_16x16x32_bf16 v[132:135], a[160:163], a[192:195], v[160:163]
v_exp_f32 v145, v137
v_mfma_f32_16x16x32_bf16 v[128:131], a[148:151], a[208:211], v[128:131]
ds_read_b64_tr_b16 a[88:89], v233 offset:27712
v_cvt_pk_bf16_f32 v156, v144, v145
v_mfma_f32_16x16x32_bf16 v[132:135], a[164:167], a[208:211], v[132:135]
v_exp_f32 v146, v138
v_mfma_f32_16x16x32_bf16 v[128:131], a[152:155], a[224:227], v[128:131]
v_exp_f32 v147, v139
v_mfma_f32_16x16x32_bf16 v[132:135], a[168:171], a[224:227], v[132:135]
ds_read_b64_tr_b16 a[90:91], v233 offset:32320
v_cvt_pk_bf16_f32 v157, v146, v147
v_mfma_f32_16x16x32_bf16 v[128:131], a[156:159], a[240:243], v[128:131]
v_exp_f32 v148, v140
v_mfma_f32_16x16x32_bf16 v[132:135], a[172:175], a[240:243], v[132:135]
v_exp_f32 v149, v141
v_mfma_f32_16x16x32_bf16 v[8:11], a[48:51], v[152:155], v[8:11]
ds_read_b64_tr_b16 a[92:93], v233 offset:27744
v_cvt_pk_bf16_f32 v158, v148, v149
v_mfma_f32_16x16x32_bf16 v[24:27], a[52:55], v[152:155], v[24:27]
v_exp_f32 v150, v142
v_mfma_f32_16x16x32_bf16 v[40:43], a[56:59], v[152:155], v[40:43]
v_exp_f32 v151, v143
v_mfma_f32_16x16x32_bf16 v[56:59], a[60:63], v[152:155], v[56:59]
v_or3_b32 v176, v156, v157, v158
v_cvt_pk_bf16_f32 v159, v150, v151
v_mfma_f32_16x16x32_bf16 v[72:75], a[64:67], v[152:155], v[72:75]
ds_read_b64_tr_b16 a[94:95], v233 offset:32352
v_bitop3_b32 v176, v176, v159, s87 bitop3:0xa8
v_mfma_f32_16x16x32_bf16 v[88:91], a[68:71], v[152:155], v[88:91]
ds_read_b64_tr_b16 a[96:97], v233 offset:27776
s_add_u32 m0, m0, 1024
v_cmp_ne_u32 vcc, 0, v176
v_mfma_f32_16x16x32_bf16 v[104:107], a[72:75], v[152:155], v[104:107]
buffer_load_dwordx4 v228, s[40:43], s64 offen lds
s_add_u32 m0, m0, 1024
v_mfma_f32_16x16x32_bf16 v[120:123], a[76:79], v[152:155], v[120:123]
buffer_load_dwordx4 v229, s[40:43], s64 offen lds
s_cmp_lg_u64 vcc, 0
v_mfma_f32_16x16x32_bf16 v[216:219], v[180:183], v[152:155], v[216:219]
ds_read_b64_tr_b16 a[98:99], v233 offset:32384
s_cbranch_scc1 SLOW11
SLOWR11:
v_mfma_f32_16x16x32_bf16 v[136:139], a[144:147], a[196:199], v[164:167]
v_exp_f32 v144, v128
v_mfma_f32_16x16x32_bf16 v[140:143], a[160:163], a[196:199], v[164:167]
v_exp_f32 v145, v129
s_waitcnt lgkmcnt(3)
v_mfma_f32_16x16x32_bf16 v[136:139], a[148:151], a[212:215], v[136:139]
ds_read_b64_tr_b16 a[100:101], v233 offset:27808
v_cvt_pk_bf16_f32 v152, v144, v145
v_mfma_f32_16x16x32_bf16 v[140:143], a[164:167], a[212:215], v[140:143]
v_exp_f32 v146, v130
v_mfma_f32_16x16x32_bf16 v[136:139], a[152:155], a[228:231], v[136:139]
v_exp_f32 v147, v131
v_mfma_f32_16x16x32_bf16 v[140:143], a[168:171], a[228:231], v[140:143]
ds_read_b64_tr_b16 a[102:103], v233 offset:32416
v_cvt_pk_bf16_f32 v153, v146, v147
v_mfma_f32_16x16x32_bf16 v[136:139], a[156:159], a[244:247], v[136:139]
v_exp_f32 v148, v132
v_mfma_f32_16x16x32_bf16 v[140:143], a[172:175], a[244:247], v[140:143]
v_exp_f32 v149, v133
v_mfma_f32_16x16x32_bf16 v[12:15], a[48:51], v[156:159], v[12:15]
ds_read_b64_tr_b16 a[104:105], v233 offset:27840
v_cvt_pk_bf16_f32 v154, v148, v149
v_mfma_f32_16x16x32_bf16 v[28:31], a[52:55], v[156:159], v[28:31]
v_exp_f32 v150, v134
v_mfma_f32_16x16x32_bf16 v[44:47], a[56:59], v[156:159], v[44:47]
v_exp_f32 v151, v135
v_mfma_f32_16x16x32_bf16 v[60:63], a[60:63], v[156:159], v[60:63]
v_or3_b32 v176, v152, v153, v154
v_cvt_pk_bf16_f32 v155, v150, v151
v_mfma_f32_16x16x32_bf16 v[76:79], a[64:67], v[156:159], v[76:79]
ds_read_b64_tr_b16 a[106:107], v233 offset:32448
v_bitop3_b32 v176, v176, v155, s87 bitop3:0xa8
v_mfma_f32_16x16x32_bf16 v[92:95], a[68:71], v[156:159], v[92:95]
ds_read_b64_tr_b16 a[108:109], v233 offset:27872
s_add_u32 m0, m0, 1024
v_cmp_ne_u32 vcc, 0, v176
v_mfma_f32_16x16x32_bf16 v[108:111], a[72:75], v[156:159], v[108:111]
buffer_load_dwordx4 v230, s[40:43], s64 offen lds
s_add_u32 s67, s67, 36864
s_cmp_eq_u32 s67, 147456
s_cselect_b32 s66, s76, 36864
s_cselect_b32 s67, 0, s67
v_mfma_f32_16x16x32_bf16 v[124:127], a[76:79], v[156:159], v[124:127]
buffer_store_dwordx4 a[4:7], v235, s[48:51], s75 offen offset:1024 sc1
s_cmp_lg_u64 vcc, 0
v_mfma_f32_16x16x32_bf16 v[220:223], v[180:183], v[156:159], v[220:223]
ds_read_b64_tr_b16 a[110:111], v233 offset:32480
s_cbranch_scc1 SLOW12
SLOWR12:
v_mfma_f32_16x16x32_bf16 v[128:131], a[144:147], a[200:203], v[168:171]
v_exp_f32 v144, v136
v_mfma_f32_16x16x32_bf16 v[132:135], a[160:163], a[200:203], v[168:171]
v_exp_f32 v145, v137
v_mfma_f32_16x16x32_bf16 v[128:131], a[148:151], a[216:219], v[128:131]
v_cvt_pk_bf16_f32 v156, v144, v145
v_mfma_f32_16x16x32_bf16 v[132:135], a[164:167], a[216:219], v[132:135]
v_exp_f32 v146, v138
v_mfma_f32_16x16x32_bf16 v[128:131], a[152:155], a[232:235], v[128:131]
v_exp_f32 v147, v139
v_mfma_f32_16x16x32_bf16 v[132:135], a[168:171], a[232:235], v[132:135]
s_waitcnt vmcnt(22) lgkmcnt(0)
s_barrier
v_cvt_pk_bf16_f32 v157, v146, v147
v_add_u32 v234, s66, v234
v_mfma_f32_16x16x32_bf16 v[128:131], a[156:159], a[248:251], v[128:131]
)"
R"(
v_exp_f32 v148, v140
v_mfma_f32_16x16x32_bf16 v[132:135], a[172:175], a[248:251], v[132:135]
v_exp_f32 v149, v141
v_mfma_f32_16x16x32_bf16 v[0:3], a[80:83], v[152:155], v[0:3]
ds_read_b128 a[112:115], v234 offset:0
v_cvt_pk_bf16_f32 v158, v148, v149
v_mfma_f32_16x16x32_bf16 v[16:19], a[84:87], v[152:155], v[16:19]
v_exp_f32 v150, v142
v_mfma_f32_16x16x32_bf16 v[32:35], a[88:91], v[152:155], v[32:35]
v_exp_f32 v151, v143
v_mfma_f32_16x16x32_bf16 v[48:51], a[92:95], v[152:155], v[48:51]
v_or3_b32 v176, v156, v157, v158
v_cvt_pk_bf16_f32 v159, v150, v151
v_mfma_f32_16x16x32_bf16 v[64:67], a[96:99], v[152:155], v[64:67]
ds_read_b128 a[116:119], v234 offset:64
v_bitop3_b32 v176, v176, v159, s87 bitop3:0xa8
v_mfma_f32_16x16x32_bf16 v[80:83], a[100:103], v[152:155], v[80:83]
ds_read_b128 a[120:123], v234 offset:128
s_add_u32 m0, m0, 1024
v_cmp_ne_u32 vcc, 0, v176
v_mfma_f32_16x16x32_bf16 v[96:99], a[104:107], v[152:155], v[96:99]
buffer_load_dwordx4 v231, s[40:43], s64 offen lds
v_mfma_f32_16x16x32_bf16 v[112:115], a[108:111], v[152:155], v[112:115]
ds_read_b128 a[124:127], v234 offset:192
v_add_u32 v233, s66, v233
s_cmp_lg_u64 vcc, 0
v_mfma_f32_16x16x32_bf16 v[208:211], v[180:183], v[152:155], v[208:211]
ds_read_b128 a[128:131], v234 offset:4608
s_cbranch_scc1 SLOW13
SLOWR13:
v_mfma_f32_16x16x32_bf16 v[136:139], a[144:147], a[204:207], v[172:175]
v_exp_f32 v144, v128
v_mfma_f32_16x16x32_bf16 v[140:143], a[160:163], a[204:207], v[172:175]
v_exp_f32 v145, v129
v_mfma_f32_16x16x32_bf16 v[136:139], a[148:151], a[220:223], v[136:139]
ds_read_b128 a[132:135], v234 offset:4672
v_cvt_pk_bf16_f32 v152, v144, v145
v_mfma_f32_16x16x32_bf16 v[140:143], a[164:167], a[220:223], v[140:143]
v_exp_f32 v146, v130
v_mfma_f32_16x16x32_bf16 v[136:139], a[152:155], a[236:239], v[136:139]
v_exp_f32 v147, v131
v_mfma_f32_16x16x32_bf16 v[140:143], a[168:171], a[236:239], v[140:143]
ds_read_b128 a[136:139], v234 offset:4736
v_cvt_pk_bf16_f32 v153, v146, v147
v_mfma_f32_16x16x32_bf16 v[136:139], a[156:159], a[252:255], v[136:139]
v_exp_f32 v148, v132
v_mfma_f32_16x16x32_bf16 v[140:143], a[172:175], a[252:255], v[140:143]
v_exp_f32 v149, v133
v_mfma_f32_16x16x32_bf16 v[4:7], a[80:83], v[156:159], v[4:7]
ds_read_b128 a[140:143], v234 offset:4800
v_cvt_pk_bf16_f32 v154, v148, v149
v_mfma_f32_16x16x32_bf16 v[20:23], a[84:87], v[156:159], v[20:23]
v_exp_f32 v150, v134
v_mfma_f32_16x16x32_bf16 v[36:39], a[88:91], v[156:159], v[36:39]
v_exp_f32 v151, v135
v_mfma_f32_16x16x32_bf16 v[52:55], a[92:95], v[156:159], v[52:55]
v_or3_b32 v176, v152, v153, v154
v_cvt_pk_bf16_f32 v155, v150, v151
v_mfma_f32_16x16x32_bf16 v[68:71], a[96:99], v[156:159], v[68:71]
ds_read_b64_tr_b16 a[48:49], v233 offset:18432
v_bitop3_b32 v176, v176, v155, s87 bitop3:0xa8
v_mfma_f32_16x16x32_bf16 v[84:87], a[100:103], v[156:159], v[84:87]
ds_read_b64_tr_b16 a[50:51], v233 offset:23040
s_add_u32 m0, m0, 1024
v_cmp_ne_u32 vcc, 0, v176
v_mfma_f32_16x16x32_bf16 v[100:103], a[104:107], v[156:159], v[100:103]
buffer_load_dwordx4 v232, s[40:43], s64 offen lds
s_add_u32 s64, s64, s65
s_add_u32 s69, s69, 36864
s_cmp_eq_u32 s69, 147456
s_cselect_b32 s69, 0, s69
v_mfma_f32_16x16x32_bf16 v[116:119], a[108:111], v[156:159], v[116:119]
buffer_store_dwordx4 a[8:11], v235, s[48:51], s75 offen offset:2048 sc1
s_cmp_lg_u64 vcc, 0
v_mfma_f32_16x16x32_bf16 v[212:215], v[180:183], v[156:159], v[212:215]
ds_read_b64_tr_b16 a[52:53], v233 offset:18464
s_cbranch_scc1 SLOW14
SLOWR14:
s_waitcnt lgkmcnt(3)
v_mfma_f32_16x16x32_bf16 v[128:131], a[112:115], a[192:195], v[160:163]
v_exp_f32 v144, v136
v_mfma_f32_16x16x32_bf16 v[132:135], a[128:131], a[192:195], v[160:163]
v_exp_f32 v145, v137
v_mfma_f32_16x16x32_bf16 v[128:131], a[116:119], a[208:211], v[128:131]
ds_read_b64_tr_b16 a[54:55], v233 offset:23072
v_cvt_pk_bf16_f32 v156, v144, v145
v_mfma_f32_16x16x32_bf16 v[132:135], a[132:135], a[208:211], v[132:135]
v_exp_f32 v146, v138
v_mfma_f32_16x16x32_bf16 v[128:131], a[120:123], a[224:227], v[128:131]
v_exp_f32 v147, v139
v_mfma_f32_16x16x32_bf16 v[132:135], a[136:139], a[224:227], v[132:135]
ds_read_b64_tr_b16 a[56:57], v233 offset:18496
v_cvt_pk_bf16_f32 v157, v146, v147
v_mfma_f32_16x16x32_bf16 v[128:131], a[124:127], a[240:243], v[128:131]
v_exp_f32 v148, v140
v_mfma_f32_16x16x32_bf16 v[132:135], a[140:143], a[240:243], v[132:135]
v_exp_f32 v149, v141
v_mfma_f32_16x16x32_bf16 v[8:11], a[80:83], v[152:155], v[8:11]
ds_read_b64_tr_b16 a[58:59], v233 offset:23104
v_cvt_pk_bf16_f32 v158, v148, v149
v_mfma_f32_16x16x32_bf16 v[24:27], a[84:87], v[152:155], v[24:27]
v_exp_f32 v150, v142
v_mfma_f32_16x16x32_bf16 v[40:43], a[88:91], v[152:155], v[40:43]
v_exp_f32 v151, v143
v_mfma_f32_16x16x32_bf16 v[56:59], a[92:95], v[152:155], v[56:59]
v_or3_b32 v176, v156, v157, v158
v_cvt_pk_bf16_f32 v159, v150, v151
v_mfma_f32_16x16x32_bf16 v[72:75], a[96:99], v[152:155], v[72:75]
ds_read_b64_tr_b16 a[60:61], v233 offset:18528
v_bitop3_b32 v176, v176, v159, s87 bitop3:0xa8
v_mfma_f32_16x16x32_bf16 v[88:91], a[100:103], v[152:155], v[88:91]
ds_read_b64_tr_b16 a[62:63], v233 offset:23136
s_add_u32 m0, s70, s69
v_cmp_ne_u32 vcc, 0, v176
v_mfma_f32_16x16x32_bf16 v[104:107], a[104:107], v[152:155], v[104:107]
buffer_load_dwordx4 v224, s[40:43], s64 offen lds
s_add_u32 m0, m0, 1024
v_mfma_f32_16x16x32_bf16 v[120:123], a[108:111], v[152:155], v[120:123]
buffer_load_dwordx4 v225, s[40:43], s64 offen lds
s_cmp_lg_u64 vcc, 0
v_mfma_f32_16x16x32_bf16 v[216:219], v[180:183], v[152:155], v[216:219]
ds_read_b64_tr_b16 a[64:65], v233 offset:18560
s_cbranch_scc1 SLOW15
SLOWR15:
v_mfma_f32_16x16x32_bf16 v[136:139], a[112:115], a[196:199], v[164:167]
v_exp_f32 v144, v128
v_mfma_f32_16x16x32_bf16 v[140:143], a[128:131], a[196:199], v[164:167]
v_exp_f32 v145, v129
s_waitcnt lgkmcnt(3)
v_mfma_f32_16x16x32_bf16 v[136:139], a[116:119], a[212:215], v[136:139]
ds_read_b64_tr_b16 a[66:67], v233 offset:23168
v_cvt_pk_bf16_f32 v152, v144, v145
v_mfma_f32_16x16x32_bf16 v[140:143], a[132:135], a[212:215], v[140:143]
v_exp_f32 v146, v130
v_mfma_f32_16x16x32_bf16 v[136:139], a[120:123], a[228:231], v[136:139]
v_exp_f32 v147, v131
v_mfma_f32_16x16x32_bf16 v[140:143], a[136:139], a[228:231], v[140:143]
ds_read_b64_tr_b16 a[68:69], v233 offset:18592
v_cvt_pk_bf16_f32 v153, v146, v147
v_mfma_f32_16x16x32_bf16 v[136:139], a[124:127], a[244:247], v[136:139]
v_exp_f32 v148, v132
v_mfma_f32_16x16x32_bf16 v[140:143], a[140:143], a[244:247], v[140:143]
v_exp_f32 v149, v133
v_mfma_f32_16x16x32_bf16 v[12:15], a[80:83], v[156:159], v[12:15]
ds_read_b64_tr_b16 a[70:71], v233 offset:23200
v_cvt_pk_bf16_f32 v154, v148, v149
v_mfma_f32_16x16x32_bf16 v[28:31], a[84:87], v[156:159], v[28:31]
v_exp_f32 v150, v134
v_mfma_f32_16x16x32_bf16 v[44:47], a[88:91], v[156:159], v[44:47]
v_exp_f32 v151, v135
v_mfma_f32_16x16x32_bf16 v[60:63], a[92:95], v[156:159], v[60:63]
v_or3_b32 v176, v152, v153, v154
v_cvt_pk_bf16_f32 v155, v150, v151
v_mfma_f32_16x16x32_bf16 v[76:79], a[96:99], v[156:159], v[76:79]
ds_read_b64_tr_b16 a[72:73], v233 offset:18624
v_bitop3_b32 v176, v176, v155, s87 bitop3:0xa8
v_mfma_f32_16x16x32_bf16 v[92:95], a[100:103], v[156:159], v[92:95]
ds_read_b64_tr_b16 a[74:75], v233 offset:23232
s_add_u32 m0, m0, 1024
v_cmp_ne_u32 vcc, 0, v176
v_mfma_f32_16x16x32_bf16 v[108:111], a[104:107], v[156:159], v[108:111]
buffer_load_dwordx4 v226, s[40:43], s64 offen lds
v_mfma_f32_16x16x32_bf16 v[124:127], a[108:111], v[156:159], v[124:127]
buffer_store_dwordx4 a[12:15], v235, s[48:51], s75 offen offset:3072 sc1
s_add_u32 s75, s75, 4096
s_cmp_lg_u64 vcc, 0
v_mfma_f32_16x16x32_bf16 v[220:223], v[180:183], v[156:159], v[220:223]
ds_read_b64_tr_b16 a[76:77], v233 offset:18656
s_cbranch_scc1 SLOW16
SLOWR16:
v_mfma_f32_16x16x32_bf16 v[128:131], a[112:115], a[200:203], v[168:171]
v_exp_f32 v144, v136
v_mfma_f32_16x16x32_bf16 v[132:135], a[128:131], a[200:203], v[168:171]
v_exp_f32 v145, v137
v_mfma_f32_16x16x32_bf16 v[128:131], a[116:119], a[216:219], v[128:131]
ds_read_b64_tr_b16 a[78:79], v233 offset:23264
v_cvt_pk_bf16_f32 v156, v144, v145
v_mfma_f32_16x16x32_bf16 v[132:135], a[132:135], a[216:219], v[132:135]
v_exp_f32 v146, v138
v_mfma_f32_16x16x32_bf16 v[128:131], a[120:123], a[232:235], v[128:131]
v_exp_f32 v147, v139
v_mfma_f32_16x16x32_bf16 v[132:135], a[136:139], a[232:235], v[132:135]
ds_read_b128 a[144:147], v234 offset:9216
v_cvt_pk_bf16_f32 v157, v146, v147
v_mfma_f32_16x16x32_bf16 v[128:131], a[124:127], a[248:251], v[128:131]
v_exp_f32 v148, v140
v_mfma_f32_16x16x32_bf16 v[132:135], a[140:143], a[248:251], v[132:135]
v_exp_f32 v149, v141
s_waitcnt lgkmcnt(1)
v_mfma_f32_16x16x32_bf16 v[0:3], a[48:51], v[152:155], v[0:3]
ds_read_b128 a[148:151], v234 offset:9280
v_cvt_pk_bf16_f32 v158, v148, v149
v_mfma_f32_16x16x32_bf16 v[16:19], a[52:55], v[152:155], v[16:19]
v_exp_f32 v150, v142
v_mfma_f32_16x16x32_bf16 v[32:35], a[56:59], v[152:155], v[32:35]
v_exp_f32 v151, v143
v_mfma_f32_16x16x32_bf16 v[48:51], a[60:63], v[152:155], v[48:51]
v_or3_b32 v176, v156, v157, v158
v_cvt_pk_bf16_f32 v159, v150, v151
v_mfma_f32_16x16x32_bf16 v[64:67], a[64:67], v[152:155], v[64:67]
ds_read_b128 a[152:155], v234 offset:9344
v_bitop3_b32 v176, v176, v159, s87 bitop3:0xa8
v_mfma_f32_16x16x32_bf16 v[80:83], a[68:71], v[152:155], v[80:83]
ds_read_b128 a[156:159], v234 offset:9408
s_add_u32 m0, m0, 1024
v_cmp_ne_u32 vcc, 0, v176
v_mfma_f32_16x16x32_bf16 v[96:99], a[72:75], v[152:155], v[96:99]
buffer_load_dwordx4 v227, s[40:43], s64 offen lds
v_mfma_f32_16x16x32_bf16 v[112:115], a[76:79], v[152:155], v[112:115]
buffer_load_dwordx3 a[180:182], v235, s[56:59], s80 offen offset:0
s_add_u32 s80, s80, 1024
s_cmp_lg_u64 vcc, 0
v_mfma_f32_16x16x32_bf16 v[208:211], v[180:183], v[152:155], v[208:211]
ds_read_b128 a[160:163], v234 offset:13824
s_cbranch_scc1 SLOW17
SLOWR17:
v_mfma_f32_16x16x32_bf16 v[136:139], a[112:115], a[204:207], v[172:175]
v_exp_f32 v144, v128
v_mfma_f32_16x16x32_bf16 v[140:143], a[128:131], a[204:207], v[172:175]
v_exp_f32 v145, v129
v_mfma_f32_16x16x32_bf16 v[136:139], a[116:119], a[220:223], v[136:139]
ds_read_b128 a[164:167], v234 offset:13888
v_cvt_pk_bf16_f32 v152, v144, v145
v_mfma_f32_16x16x32_bf16 v[140:143], a[132:135], a[220:223], v[140:143]
v_exp_f32 v146, v130
v_mfma_f32_16x16x32_bf16 v[136:139], a[120:123], a[236:239], v[136:139]
v_exp_f32 v147, v131
v_mfma_f32_16x16x32_bf16 v[140:143], a[136:139], a[236:239], v[140:143]
ds_read_b128 a[168:171], v234 offset:13952
v_cvt_pk_bf16_f32 v153, v146, v147
v_mfma_f32_16x16x32_bf16 v[136:139], a[124:127], a[252:255], v[136:139]
v_exp_f32 v148, v132
v_mfma_f32_16x16x32_bf16 v[140:143], a[140:143], a[252:255], v[140:143]
v_exp_f32 v149, v133
v_mfma_f32_16x16x32_bf16 v[4:7], a[48:51], v[156:159], v[4:7]
ds_read_b128 a[172:175], v234 offset:14016
v_cvt_pk_bf16_f32 v154, v148, v149
v_mfma_f32_16x16x32_bf16 v[20:23], a[52:55], v[156:159], v[20:23]
v_exp_f32 v150, v134
v_mfma_f32_16x16x32_bf16 v[36:39], a[56:59], v[156:159], v[36:39]
v_exp_f32 v151, v135
v_mfma_f32_16x16x32_bf16 v[52:55], a[60:63], v[156:159], v[52:55]
v_or3_b32 v176, v152, v153, v154
v_cvt_pk_bf16_f32 v155, v150, v151
v_mfma_f32_16x16x32_bf16 v[68:71], a[64:67], v[156:159], v[68:71]
ds_read_b64_tr_b16 a[80:81], v233 offset:27648
v_bitop3_b32 v176, v176, v155, s87 bitop3:0xa8
v_mfma_f32_16x16x32_bf16 v[84:87], a[68:71], v[156:159], v[84:87]
ds_read_b64_tr_b16 a[82:83], v233 offset:32256
s_add_u32 m0, m0, 1024
v_cmp_ne_u32 vcc, 0, v176
v_mfma_f32_16x16x32_bf16 v[100:103], a[72:75], v[156:159], v[100:103]
buffer_load_dwordx4 v228, s[40:43], s64 offen lds
v_mfma_f32_16x16x32_bf16 v[116:119], a[76:79], v[156:159], v[116:119]
buffer_store_dwordx2 v[184:185], v235, s[48:51], s38 offen offset:0 sc1
s_cmp_lg_u64 vcc, 0
v_mfma_f32_16x16x32_bf16 v[212:215], v[180:183], v[156:159], v[212:215]
ds_read_b64_tr_b16 a[84:85], v233 offset:27680
ds_read_b64_tr_b16 a[86:87], v233 offset:32288
s_cbranch_scc1 SLOW18
SLOWR18:
s_waitcnt lgkmcnt(4)
v_mfma_f32_16x16x32_bf16 v[128:131], a[144:147], a[192:195], v[160:163]
v_exp_f32 v144, v136
v_mfma_f32_16x16x32_bf16 v[132:135], a[160:163], a[192:195], v[160:163]
v_exp_f32 v145, v137
v_mfma_f32_16x16x32_bf16 v[128:131], a[148:151], a[208:211], v[128:131]
ds_read_b64_tr_b16 a[88:89], v233 offset:27712
v_cvt_pk_bf16_f32 v156, v144, v145
v_mfma_f32_16x16x32_bf16 v[132:135], a[164:167], a[208:211], v[132:135]
v_exp_f32 v146, v138
v_mfma_f32_16x16x32_bf16 v[128:131], a[152:155], a[224:227], v[128:131]
v_exp_f32 v147, v139
v_mfma_f32_16x16x32_bf16 v[132:135], a[168:171], a[224:227], v[132:135]
ds_read_b64_tr_b16 a[90:91], v233 offset:32320
v_cvt_pk_bf16_f32 v157, v146, v147
v_mfma_f32_16x16x32_bf16 v[128:131], a[156:159], a[240:243], v[128:131]
v_exp_f32 v148, v140
v_mfma_f32_16x16x32_bf16 v[132:135], a[172:175], a[240:243], v[132:135]
v_exp_f32 v149, v141
v_mfma_f32_16x16x32_bf16 v[8:11], a[48:51], v[152:155], v[8:11]
ds_read_b64_tr_b16 a[92:93], v233 offset:27744
v_cvt_pk_bf16_f32 v158, v148, v149
v_mfma_f32_16x16x32_bf16 v[24:27], a[52:55], v[152:155], v[24:27]
v_exp_f32 v150, v142
v_mfma_f32_16x16x32_bf16 v[40:43], a[56:59], v[152:155], v[40:43]
v_exp_f32 v151, v143
v_mfma_f32_16x16x32_bf16 v[56:59], a[60:63], v[152:155], v[56:59]
v_or3_b32 v176, v156, v157, v158
v_cvt_pk_bf16_f32 v159, v150, v151
v_mfma_f32_16x16x32_bf16 v[72:75], a[64:67], v[152:155], v[72:75]
ds_read_b64_tr_b16 a[94:95], v233 offset:32352
v_bitop3_b32 v176, v176, v159, s87 bitop3:0xa8
v_mfma_f32_16x16x32_bf16 v[88:91], a[68:71], v[152:155], v[88:91]
ds_read_b64_tr_b16 a[96:97], v233 offset:27776
s_add_u32 m0, m0, 1024
v_cmp_ne_u32 vcc, 0, v176
v_mfma_f32_16x16x32_bf16 v[104:107], a[72:75], v[152:155], v[104:107]
buffer_load_dwordx4 v229, s[40:43], s64 offen lds
v_mfma_f32_16x16x32_bf16 v[120:123], a[76:79], v[152:155], v[120:123]
buffer_load_dwordx3 a[180:182], v235, s[56:59], s80 offen offset:0
s_add_u32 s80, s80, 1024
s_cmp_lg_u64 vcc, 0
v_mfma_f32_16x16x32_bf16 v[216:219], v[180:183], v[152:155], v[216:219]
ds_read_b64_tr_b16 a[98:99], v233 offset:32384
s_cbranch_scc1 SLOW19
SLOWR19:
v_mfma_f32_16x16x32_bf16 v[136:139], a[144:147], a[196:199], v[164:167]
v_exp_f32 v144, v128
v_mfma_f32_16x16x32_bf16 v[140:143], a[160:163], a[196:199], v[164:167]
v_exp_f32 v145, v129
s_waitcnt lgkmcnt(3)
v_mfma_f32_16x16x32_bf16 v[136:139], a[148:151], a[212:215], v[136:139]
ds_read_b64_tr_b16 a[100:101], v233 offset:27808
v_cvt_pk_bf16_f32 v152, v144, v145
v_mfma_f32_16x16x32_bf16 v[140:143], a[164:167], a[212:215], v[140:143]
v_exp_f32 v146, v130
v_mfma_f32_16x16x32_bf16 v[136:139], a[152:155], a[228:231], v[136:139]
v_exp_f32 v147, v131
v_mfma_f32_16x16x32_bf16 v[140:143], a[168:171], a[228:231], v[140:143]
ds_read_b64_tr_b16 a[102:103], v233 offset:32416
v_cvt_pk_bf16_f32 v153, v146, v147
v_mfma_f32_16x16x32_bf16 v[136:139], a[156:159], a[244:247], v[136:139]
v_exp_f32 v148, v132
v_mfma_f32_16x16x32_bf16 v[140:143], a[172:175], a[244:247], v[140:143]
v_exp_f32 v149, v133
v_mfma_f32_16x16x32_bf16 v[12:15], a[48:51], v[156:159], v[12:15]
ds_read_b64_tr_b16 a[104:105], v233 offset:27840
v_cvt_pk_bf16_f32 v154, v148, v149
v_mfma_f32_16x16x32_bf16 v[28:31], a[52:55], v[156:159], v[28:31]
v_exp_f32 v150, v134
v_mfma_f32_16x16x32_bf16 v[44:47], a[56:59], v[156:159], v[44:47]
v_exp_f32 v151, v135
v_mfma_f32_16x16x32_bf16 v[60:63], a[60:63], v[156:159], v[60:63]
v_or3_b32 v176, v152, v153, v154
v_cvt_pk_bf16_f32 v155, v150, v151
v_mfma_f32_16x16x32_bf16 v[76:79], a[64:67], v[156:159], v[76:79]
ds_read_b64_tr_b16 a[106:107], v233 offset:32448
v_bitop3_b32 v176, v176, v155, s87 bitop3:0xa8
v_mfma_f32_16x16x32_bf16 v[92:95], a[68:71], v[156:159], v[92:95]
ds_read_b64_tr_b16 a[108:109], v233 offset:27872
s_add_u32 m0, m0, 1024
v_cmp_ne_u32 vcc, 0, v176
v_mfma_f32_16x16x32_bf16 v[108:111], a[72:75], v[156:159], v[108:111]
buffer_load_dwordx4 v230, s[40:43], s64 offen lds
s_add_u32 s67, s67, 36864
s_cmp_eq_u32 s67, 147456
s_cselect_b32 s66, s76, 36864
s_cselect_b32 s67, 0, s67
v_mfma_f32_16x16x32_bf16 v[124:127], a[76:79], v[156:159], v[124:127]
buffer_store_dwordx2 v[186:187], v235, s[48:51], s38 offen offset:1024 sc1
s_cmp_lg_u64 vcc, 0
v_mfma_f32_16x16x32_bf16 v[220:223], v[180:183], v[156:159], v[220:223]
ds_read_b64_tr_b16 a[110:111], v233 offset:32480
s_cbranch_scc1 SLOW20
SLOWR20:
v_mfma_f32_16x16x32_bf16 v[128:131], a[144:147], a[200:203], v[168:171]
v_exp_f32 v144, v136
v_mfma_f32_16x16x32_bf16 v[132:135], a[160:163], a[200:203], v[168:171]
v_exp_f32 v145, v137
v_mfma_f32_16x16x32_bf16 v[128:131], a[148:151], a[216:219], v[128:131]
v_cvt_pk_bf16_f32 v156, v144, v145
v_mfma_f32_16x16x32_bf16 v[132:135], a[164:167], a[216:219], v[132:135]
v_exp_f32 v146, v138
v_mfma_f32_16x16x32_bf16 v[128:131], a[152:155], a[232:235], v[128:131]
v_exp_f32 v147, v139
v_mfma_f32_16x16x32_bf16 v[132:135], a[168:171], a[232:235], v[132:135]
s_waitcnt vmcnt(25) lgkmcnt(0)
s_barrier
v_cvt_pk_bf16_f32 v157, v146, v147
v_add_u32 v234, s66, v234
v_mfma_f32_16x16x32_bf16 v[128:131], a[156:159], a[248:251], v[128:131]
v_exp_f32 v148, v140
v_mfma_f32_16x16x32_bf16 v[132:135], a[172:175], a[248:251], v[132:135]
v_exp_f32 v149, v141
v_mfma_f32_16x16x32_bf16 v[0:3], a[80:83], v[152:155], v[0:3]
ds_read_b128 a[112:115], v234 offset:0
v_cvt_pk_bf16_f32 v158, v148, v149
v_mfma_f32_16x16x32_bf16 v[16:19], a[84:87], v[152:155], v[16:19]
v_exp_f32 v150, v142
v_mfma_f32_16x16x32_bf16 v[32:35], a[88:91], v[152:155], v[32:35]
v_exp_f32 v151, v143
v_mfma_f32_16x16x32_bf16 v[48:51], a[92:95], v[152:155], v[48:51]
v_or3_b32 v176, v156, v157, v158
v_cvt_pk_bf16_f32 v159, v150, v151
v_mfma_f32_16x16x32_bf16 v[64:67], a[96:99], v[152:155], v[64:67]
ds_read_b128 a[116:119], v234 offset:64
v_bitop3_b32 v176, v176, v159, s87 bitop3:0xa8
v_mfma_f32_16x16x32_bf16 v[80:83], a[100:103], v[152:155], v[80:83]
ds_read_b128 a[120:123], v234 offset:128
s_add_u32 m0, m0, 1024
v_cmp_ne_u32 vcc, 0, v176
v_mfma_f32_16x16x32_bf16 v[96:99], a[104:107], v[152:155], v[96:99]
buffer_load_dwordx4 v231, s[40:43], s64 offen lds
v_mfma_f32_16x16x32_bf16 v[112:115], a[108:111], v[152:155], v[112:115]
ds_read_b128 a[124:127], v234 offset:192
v_add_u32 v233, s66, v233
s_cmp_lg_u64 vcc, 0
v_mfma_f32_16x16x32_bf16 v[208:211], v[180:183], v[152:155], v[208:211]
ds_read_b128 a[128:131], v234 offset:4608
)"
R"(
s_cbranch_scc1 SLOW21
SLOWR21:
v_mfma_f32_16x16x32_bf16 v[136:139], a[144:147], a[204:207], v[172:175]
v_exp_f32 v144, v128
v_mfma_f32_16x16x32_bf16 v[140:143], a[160:163], a[204:207], v[172:175]
v_exp_f32 v145, v129
v_mfma_f32_16x16x32_bf16 v[136:139], a[148:151], a[220:223], v[136:139]
ds_read_b128 a[132:135], v234 offset:4672
v_cvt_pk_bf16_f32 v152, v144, v145
v_mfma_f32_16x16x32_bf16 v[140:143], a[164:167], a[220:223], v[140:143]
v_exp_f32 v146, v130
v_mfma_f32_16x16x32_bf16 v[136:139], a[152:155], a[236:239], v[136:139]
v_exp_f32 v147, v131
v_mfma_f32_16x16x32_bf16 v[140:143], a[168:171], a[236:239], v[140:143]
ds_read_b128 a[136:139], v234 offset:4736
v_cvt_pk_bf16_f32 v153, v146, v147
v_mfma_f32_16x16x32_bf16 v[136:139], a[156:159], a[252:255], v[136:139]
v_exp_f32 v148, v132
v_mfma_f32_16x16x32_bf16 v[140:143], a[172:175], a[252:255], v[140:143]
v_exp_f32 v149, v133
v_mfma_f32_16x16x32_bf16 v[4:7], a[80:83], v[156:159], v[4:7]
ds_read_b128 a[140:143], v234 offset:4800
v_cvt_pk_bf16_f32 v154, v148, v149
v_mfma_f32_16x16x32_bf16 v[20:23], a[84:87], v[156:159], v[20:23]
v_exp_f32 v150, v134
v_mfma_f32_16x16x32_bf16 v[36:39], a[88:91], v[156:159], v[36:39]
v_exp_f32 v151, v135
v_mfma_f32_16x16x32_bf16 v[52:55], a[92:95], v[156:159], v[52:55]
v_or3_b32 v176, v152, v153, v154
v_cvt_pk_bf16_f32 v155, v150, v151
v_mfma_f32_16x16x32_bf16 v[68:71], a[96:99], v[156:159], v[68:71]
ds_read_b64_tr_b16 a[48:49], v233 offset:18432
v_bitop3_b32 v176, v176, v155, s87 bitop3:0xa8
v_mfma_f32_16x16x32_bf16 v[84:87], a[100:103], v[156:159], v[84:87]
ds_read_b64_tr_b16 a[50:51], v233 offset:23040
s_add_u32 m0, m0, 1024
v_cmp_ne_u32 vcc, 0, v176
v_mfma_f32_16x16x32_bf16 v[100:103], a[104:107], v[156:159], v[100:103]
buffer_load_dwordx4 v232, s[40:43], s64 offen lds
s_add_u32 s64, s64, s65
s_add_u32 s69, s69, 36864
s_cmp_eq_u32 s69, 147456
s_cselect_b32 s69, 0, s69
v_mfma_f32_16x16x32_bf16 v[116:119], a[108:111], v[156:159], v[116:119]
buffer_store_dwordx2 v[188:189], v235, s[48:51], s38 offen offset:2048 sc1
s_cmp_lg_u64 vcc, 0
v_mfma_f32_16x16x32_bf16 v[212:215], v[180:183], v[156:159], v[212:215]
ds_read_b64_tr_b16 a[52:53], v233 offset:18464
s_cbranch_scc1 SLOW22
SLOWR22:
s_waitcnt lgkmcnt(3)
v_mfma_f32_16x16x32_bf16 v[128:131], a[112:115], a[192:195], v[160:163]
v_exp_f32 v144, v136
v_mfma_f32_16x16x32_bf16 v[132:135], a[128:131], a[192:195], v[160:163]
v_exp_f32 v145, v137
v_mfma_f32_16x16x32_bf16 v[128:131], a[116:119], a[208:211], v[128:131]
ds_read_b64_tr_b16 a[54:55], v233 offset:23072
v_cvt_pk_bf16_f32 v156, v144, v145
v_mfma_f32_16x16x32_bf16 v[132:135], a[132:135], a[208:211], v[132:135]
v_exp_f32 v146, v138
v_mfma_f32_16x16x32_bf16 v[128:131], a[120:123], a[224:227], v[128:131]
v_exp_f32 v147, v139
v_mfma_f32_16x16x32_bf16 v[132:135], a[136:139], a[224:227], v[132:135]
ds_read_b64_tr_b16 a[56:57], v233 offset:18496
v_cvt_pk_bf16_f32 v157, v146, v147
v_mfma_f32_16x16x32_bf16 v[128:131], a[124:127], a[240:243], v[128:131]
v_exp_f32 v148, v140
v_mfma_f32_16x16x32_bf16 v[132:135], a[140:143], a[240:243], v[132:135]
v_exp_f32 v149, v141
v_mfma_f32_16x16x32_bf16 v[8:11], a[80:83], v[152:155], v[8:11]
ds_read_b64_tr_b16 a[58:59], v233 offset:23104
v_cvt_pk_bf16_f32 v158, v148, v149
v_mfma_f32_16x16x32_bf16 v[24:27], a[84:87], v[152:155], v[24:27]
v_exp_f32 v150, v142
v_mfma_f32_16x16x32_bf16 v[40:43], a[88:91], v[152:155], v[40:43]
v_exp_f32 v151, v143
v_mfma_f32_16x16x32_bf16 v[56:59], a[92:95], v[152:155], v[56:59]
v_or3_b32 v176, v156, v157, v158
v_cvt_pk_bf16_f32 v159, v150, v151
v_mfma_f32_16x16x32_bf16 v[72:75], a[96:99], v[152:155], v[72:75]
ds_read_b64_tr_b16 a[60:61], v233 offset:18528
v_bitop3_b32 v176, v176, v159, s87 bitop3:0xa8
v_mfma_f32_16x16x32_bf16 v[88:91], a[100:103], v[152:155], v[88:91]
ds_read_b64_tr_b16 a[62:63], v233 offset:23136
s_add_u32 m0, s70, s69
v_cmp_ne_u32 vcc, 0, v176
v_mfma_f32_16x16x32_bf16 v[104:107], a[104:107], v[152:155], v[104:107]
buffer_load_dwordx4 v224, s[40:43], s64 offen lds
v_mfma_f32_16x16x32_bf16 v[120:123], a[108:111], v[152:155], v[120:123]
buffer_load_dwordx3 a[180:182], v235, s[56:59], s80 offen offset:0
s_add_u32 s80, s80, 1024
s_cmp_lg_u64 vcc, 0
v_mfma_f32_16x16x32_bf16 v[216:219], v[180:183], v[152:155], v[216:219]
ds_read_b64_tr_b16 a[64:65], v233 offset:18560
s_cbranch_scc1 SLOW23
SLOWR23:
v_mfma_f32_16x16x32_bf16 v[136:139], a[112:115], a[196:199], v[164:167]
v_exp_f32 v144, v128
v_mfma_f32_16x16x32_bf16 v[140:143], a[128:131], a[196:199], v[164:167]
v_exp_f32 v145, v129
s_waitcnt lgkmcnt(3)
v_mfma_f32_16x16x32_bf16 v[136:139], a[116:119], a[212:215], v[136:139]
ds_read_b64_tr_b16 a[66:67], v233 offset:23168
v_cvt_pk_bf16_f32 v152, v144, v145
v_mfma_f32_16x16x32_bf16 v[140:143], a[132:135], a[212:215], v[140:143]
v_exp_f32 v146, v130
v_mfma_f32_16x16x32_bf16 v[136:139], a[120:123], a[228:231], v[136:139]
v_exp_f32 v147, v131
v_mfma_f32_16x16x32_bf16 v[140:143], a[136:139], a[228:231], v[140:143]
ds_read_b64_tr_b16 a[68:69], v233 offset:18592
v_cvt_pk_bf16_f32 v153, v146, v147
v_mfma_f32_16x16x32_bf16 v[136:139], a[124:127], a[244:247], v[136:139]
v_exp_f32 v148, v132
v_mfma_f32_16x16x32_bf16 v[140:143], a[140:143], a[244:247], v[140:143]
v_exp_f32 v149, v133
v_mfma_f32_16x16x32_bf16 v[12:15], a[80:83], v[156:159], v[12:15]
ds_read_b64_tr_b16 a[70:71], v233 offset:23200
v_cvt_pk_bf16_f32 v154, v148, v149
v_mfma_f32_16x16x32_bf16 v[28:31], a[84:87], v[156:159], v[28:31]
v_exp_f32 v150, v134
v_mfma_f32_16x16x32_bf16 v[44:47], a[88:91], v[156:159], v[44:47]
v_exp_f32 v151, v135
v_mfma_f32_16x16x32_bf16 v[60:63], a[92:95], v[156:159], v[60:63]
v_or3_b32 v176, v152, v153, v154
v_cvt_pk_bf16_f32 v155, v150, v151
v_mfma_f32_16x16x32_bf16 v[76:79], a[96:99], v[156:159], v[76:79]
ds_read_b64_tr_b16 a[72:73], v233 offset:18624
v_bitop3_b32 v176, v176, v155, s87 bitop3:0xa8
v_mfma_f32_16x16x32_bf16 v[92:95], a[100:103], v[156:159], v[92:95]
ds_read_b64_tr_b16 a[74:75], v233 offset:23232
s_add_u32 m0, m0, 1024
v_cmp_ne_u32 vcc, 0, v176
v_mfma_f32_16x16x32_bf16 v[108:111], a[104:107], v[156:159], v[108:111]
buffer_load_dwordx4 v225, s[40:43], s64 offen lds
v_mfma_f32_16x16x32_bf16 v[124:127], a[108:111], v[156:159], v[124:127]
buffer_store_dwordx2 v[190:191], v235, s[48:51], s38 offen offset:3072 sc1
s_cmp_lg_u64 vcc, 0
v_mfma_f32_16x16x32_bf16 v[220:223], v[180:183], v[156:159], v[220:223]
ds_read_b64_tr_b16 a[76:77], v233 offset:18656
s_cbranch_scc1 SLOW24
SLOWR24:
v_mfma_f32_16x16x32_bf16 v[128:131], a[112:115], a[200:203], v[168:171]
v_exp_f32 v144, v136
v_mfma_f32_16x16x32_bf16 v[132:135], a[128:131], a[200:203], v[168:171]
v_exp_f32 v145, v137
v_mfma_f32_16x16x32_bf16 v[128:131], a[116:119], a[216:219], v[128:131]
ds_read_b64_tr_b16 a[78:79], v233 offset:23264
v_cvt_pk_bf16_f32 v156, v144, v145
v_mfma_f32_16x16x32_bf16 v[132:135], a[132:135], a[216:219], v[132:135]
v_exp_f32 v146, v138
v_mfma_f32_16x16x32_bf16 v[128:131], a[120:123], a[232:235], v[128:131]
v_exp_f32 v147, v139
v_mfma_f32_16x16x32_bf16 v[132:135], a[136:139], a[232:235], v[132:135]
ds_read_b128 a[144:147], v234 offset:9216
v_cvt_pk_bf16_f32 v157, v146, v147
v_mfma_f32_16x16x32_bf16 v[128:131], a[124:127], a[248:251], v[128:131]
v_exp_f32 v148, v140
v_mfma_f32_16x16x32_bf16 v[132:135], a[140:143], a[248:251], v[132:135]
v_exp_f32 v149, v141
s_waitcnt lgkmcnt(1)
v_mfma_f32_16x16x32_bf16 v[0:3], a[48:51], v[152:155], v[0:3]
ds_read_b128 a[148:151], v234 offset:9280
v_cvt_pk_bf16_f32 v158, v148, v149
v_mfma_f32_16x16x32_bf16 v[16:19], a[52:55], v[152:155], v[16:19]
v_exp_f32 v150, v142
v_mfma_f32_16x16x32_bf16 v[32:35], a[56:59], v[152:155], v[32:35]
v_exp_f32 v151, v143
v_mfma_f32_16x16x32_bf16 v[48:51], a[60:63], v[152:155], v[48:51]
v_or3_b32 v176, v156, v157, v158
v_cvt_pk_bf16_f32 v159, v150, v151
v_mfma_f32_16x16x32_bf16 v[64:67], a[64:67], v[152:155], v[64:67]
ds_read_b128 a[152:155], v234 offset:9344
v_bitop3_b32 v176, v176, v159, s87 bitop3:0xa8
v_mfma_f32_16x16x32_bf16 v[80:83], a[68:71], v[152:155], v[80:83]
ds_read_b128 a[156:159], v234 offset:9408
s_add_u32 m0, m0, 1024
v_cmp_ne_u32 vcc, 0, v176
v_mfma_f32_16x16x32_bf16 v[96:99], a[72:75], v[152:155], v[96:99]
buffer_load_dwordx4 v226, s[40:43], s64 offen lds
v_mfma_f32_16x16x32_bf16 v[112:115], a[76:79], v[152:155], v[112:115]
buffer_load_dwordx3 a[180:182], v235, s[56:59], s80 offen offset:0
s_add_u32 s80, s80, 1024
s_cmp_lg_u64 vcc, 0
v_mfma_f32_16x16x32_bf16 v[208:211], v[180:183], v[152:155], v[208:211]
ds_read_b128 a[160:163], v234 offset:13824
s_cbranch_scc1 SLOW25
SLOWR25:
v_mfma_f32_16x16x32_bf16 v[136:139], a[112:115], a[204:207], v[172:175]
v_exp_f32 v144, v128
v_mfma_f32_16x16x32_bf16 v[140:143], a[128:131], a[204:207], v[172:175]
v_exp_f32 v145, v129
v_mfma_f32_16x16x32_bf16 v[136:139], a[116:119], a[220:223], v[136:139]
ds_read_b128 a[164:167], v234 offset:13888
v_cvt_pk_bf16_f32 v152, v144, v145
v_mfma_f32_16x16x32_bf16 v[140:143], a[132:135], a[220:223], v[140:143]
v_exp_f32 v146, v130
v_mfma_f32_16x16x32_bf16 v[136:139], a[120:123], a[236:239], v[136:139]
v_exp_f32 v147, v131
v_mfma_f32_16x16x32_bf16 v[140:143], a[136:139], a[236:239], v[140:143]
ds_read_b128 a[168:171], v234 offset:13952
v_cvt_pk_bf16_f32 v153, v146, v147
v_mfma_f32_16x16x32_bf16 v[136:139], a[124:127], a[252:255], v[136:139]
v_exp_f32 v148, v132
v_mfma_f32_16x16x32_bf16 v[140:143], a[140:143], a[252:255], v[140:143]
v_exp_f32 v149, v133
v_mfma_f32_16x16x32_bf16 v[4:7], a[48:51], v[156:159], v[4:7]
ds_read_b128 a[172:175], v234 offset:14016
v_cvt_pk_bf16_f32 v154, v148, v149
v_mfma_f32_16x16x32_bf16 v[20:23], a[52:55], v[156:159], v[20:23]
v_exp_f32 v150, v134
v_mfma_f32_16x16x32_bf16 v[36:39], a[56:59], v[156:159], v[36:39]
v_exp_f32 v151, v135
v_mfma_f32_16x16x32_bf16 v[52:55], a[60:63], v[156:159], v[52:55]
v_or3_b32 v176, v152, v153, v154
v_cvt_pk_bf16_f32 v155, v150, v151
v_mfma_f32_16x16x32_bf16 v[68:71], a[64:67], v[156:159], v[68:71]
ds_read_b64_tr_b16 a[80:81], v233 offset:27648
v_bitop3_b32 v176, v176, v155, s87 bitop3:0xa8
v_mfma_f32_16x16x32_bf16 v[84:87], a[68:71], v[156:159], v[84:87]
ds_read_b64_tr_b16 a[82:83], v233 offset:32256
s_add_u32 m0, m0, 1024
v_cmp_ne_u32 vcc, 0, v176
v_mfma_f32_16x16x32_bf16 v[100:103], a[72:75], v[156:159], v[100:103]
buffer_load_dwordx4 v227, s[40:43], s64 offen lds
v_mfma_f32_16x16x32_bf16 v[116:119], a[76:79], v[156:159], v[116:119]
buffer_load_dwordx3 a[180:182], v235, s[56:59], s80 offen offset:0
s_add_u32 s80, s80, 1024
s_cmp_lg_u64 vcc, 0
v_mfma_f32_16x16x32_bf16 v[212:215], v[180:183], v[156:159], v[212:215]
ds_read_b64_tr_b16 a[84:85], v233 offset:27680
ds_read_b64_tr_b16 a[86:87], v233 offset:32288
s_cbranch_scc1 SLOW26
SLOWR26:
s_waitcnt lgkmcnt(4)
v_mfma_f32_16x16x32_bf16 v[128:131], a[144:147], a[192:195], v[160:163]
v_exp_f32 v144, v136
v_mfma_f32_16x16x32_bf16 v[132:135], a[160:163], a[192:195], v[160:163]
v_exp_f32 v145, v137
v_mfma_f32_16x16x32_bf16 v[128:131], a[148:151], a[208:211], v[128:131]
ds_read_b64_tr_b16 a[88:89], v233 offset:27712
v_cvt_pk_bf16_f32 v156, v144, v145
v_mfma_f32_16x16x32_bf16 v[132:135], a[164:167], a[208:211], v[132:135]
v_exp_f32 v146, v138
v_mfma_f32_16x16x32_bf16 v[128:131], a[152:155], a[224:227], v[128:131]
v_exp_f32 v147, v139
v_mfma_f32_16x16x32_bf16 v[132:135], a[168:171], a[224:227], v[132:135]
ds_read_b64_tr_b16 a[90:91], v233 offset:32320
v_cvt_pk_bf16_f32 v157, v146, v147
v_mfma_f32_16x16x32_bf16 v[128:131], a[156:159], a[240:243], v[128:131]
v_exp_f32 v148, v140
v_mfma_f32_16x16x32_bf16 v[132:135], a[172:175], a[240:243], v[132:135]
v_exp_f32 v149, v141
v_mfma_f32_16x16x32_bf16 v[8:11], a[48:51], v[152:155], v[8:11]
ds_read_b64_tr_b16 a[92:93], v233 offset:27744
v_cvt_pk_bf16_f32 v158, v148, v149
v_mfma_f32_16x16x32_bf16 v[24:27], a[52:55], v[152:155], v[24:27]
v_exp_f32 v150, v142
v_mfma_f32_16x16x32_bf16 v[40:43], a[56:59], v[152:155], v[40:43]
v_exp_f32 v151, v143
v_mfma_f32_16x16x32_bf16 v[56:59], a[60:63], v[152:155], v[56:59]
v_or3_b32 v176, v156, v157, v158
v_cvt_pk_bf16_f32 v159, v150, v151
v_mfma_f32_16x16x32_bf16 v[72:75], a[64:67], v[152:155], v[72:75]
ds_read_b64_tr_b16 a[94:95], v233 offset:32352
v_bitop3_b32 v176, v176, v159, s87 bitop3:0xa8
v_mfma_f32_16x16x32_bf16 v[88:91], a[68:71], v[152:155], v[88:91]
ds_read_b64_tr_b16 a[96:97], v233 offset:27776
s_add_u32 m0, m0, 1024
v_cmp_ne_u32 vcc, 0, v176
v_mfma_f32_16x16x32_bf16 v[104:107], a[72:75], v[152:155], v[104:107]
buffer_load_dwordx4 v228, s[40:43], s64 offen lds
v_mfma_f32_16x16x32_bf16 v[120:123], a[76:79], v[152:155], v[120:123]
buffer_load_dwordx3 a[180:182], v235, s[56:59], s80 offen offset:0
s_add_u32 s80, s80, 1024
s_cmp_lg_u64 vcc, 0
v_mfma_f32_16x16x32_bf16 v[216:219], v[180:183], v[152:155], v[216:219]
ds_read_b64_tr_b16 a[98:99], v233 offset:32384
s_cbranch_scc1 SLOW27
SLOWR27:
v_mfma_f32_16x16x32_bf16 v[136:139], a[144:147], a[196:199], v[164:167]
v_exp_f32 v144, v128
v_mfma_f32_16x16x32_bf16 v[140:143], a[160:163], a[196:199], v[164:167]
v_exp_f32 v145, v129
s_waitcnt lgkmcnt(3)
v_mfma_f32_16x16x32_bf16 v[136:139], a[148:151], a[212:215], v[136:139]
ds_read_b64_tr_b16 a[100:101], v233 offset:27808
v_cvt_pk_bf16_f32 v152, v144, v145
v_mfma_f32_16x16x32_bf16 v[140:143], a[164:167], a[212:215], v[140:143]
v_exp_f32 v146, v130
v_mfma_f32_16x16x32_bf16 v[136:139], a[152:155], a[228:231], v[136:139]
v_exp_f32 v147, v131
v_mfma_f32_16x16x32_bf16 v[140:143], a[168:171], a[228:231], v[140:143]
ds_read_b64_tr_b16 a[102:103], v233 offset:32416
v_cvt_pk_bf16_f32 v153, v146, v147
v_mfma_f32_16x16x32_bf16 v[136:139], a[156:159], a[244:247], v[136:139]
v_exp_f32 v148, v132
v_mfma_f32_16x16x32_bf16 v[140:143], a[172:175], a[244:247], v[140:143]
v_exp_f32 v149, v133
v_mfma_f32_16x16x32_bf16 v[12:15], a[48:51], v[156:159], v[12:15]
ds_read_b64_tr_b16 a[104:105], v233 offset:27840
v_cvt_pk_bf16_f32 v154, v148, v149
v_mfma_f32_16x16x32_bf16 v[28:31], a[52:55], v[156:159], v[28:31]
v_exp_f32 v150, v134
v_mfma_f32_16x16x32_bf16 v[44:47], a[56:59], v[156:159], v[44:47]
v_exp_f32 v151, v135
v_mfma_f32_16x16x32_bf16 v[60:63], a[60:63], v[156:159], v[60:63]
v_or3_b32 v176, v152, v153, v154
v_cvt_pk_bf16_f32 v155, v150, v151
v_mfma_f32_16x16x32_bf16 v[76:79], a[64:67], v[156:159], v[76:79]
ds_read_b64_tr_b16 a[106:107], v233 offset:32448
v_bitop3_b32 v176, v176, v155, s87 bitop3:0xa8
v_mfma_f32_16x16x32_bf16 v[92:95], a[68:71], v[156:159], v[92:95]
ds_read_b64_tr_b16 a[108:109], v233 offset:27872
s_add_u32 m0, m0, 1024
v_cmp_ne_u32 vcc, 0, v176
v_mfma_f32_16x16x32_bf16 v[108:111], a[72:75], v[156:159], v[108:111]
buffer_load_dwordx4 v229, s[40:43], s64 offen lds
s_add_u32 s67, s67, 36864
s_cmp_eq_u32 s67, 147456
s_cselect_b32 s66, s76, 36864
s_cselect_b32 s67, 0, s67
v_mfma_f32_16x16x32_bf16 v[124:127], a[76:79], v[156:159], v[124:127]
buffer_load_dwordx3 a[180:182], v235, s[56:59], s80 offen offset:0
s_add_u32 s80, s80, 1024
s_cmp_lg_u64 vcc, 0
v_mfma_f32_16x16x32_bf16 v[220:223], v[180:183], v[156:159], v[220:223]
ds_read_b64_tr_b16 a[110:111], v233 offset:32480
s_cbranch_scc1 SLOW28
SLOWR28:
v_mfma_f32_16x16x32_bf16 v[128:131], a[144:147], a[200:203], v[168:171]
v_exp_f32 v144, v136
v_mfma_f32_16x16x32_bf16 v[132:135], a[160:163], a[200:203], v[168:171]
v_exp_f32 v145, v137
v_mfma_f32_16x16x32_bf16 v[128:131], a[148:151], a[216:219], v[128:131]
v_cvt_pk_bf16_f32 v156, v144, v145
v_mfma_f32_16x16x32_bf16 v[132:135], a[164:167], a[216:219], v[132:135]
v_exp_f32 v146, v138
v_mfma_f32_16x16x32_bf16 v[128:131], a[152:155], a[232:235], v[128:131]
v_exp_f32 v147, v139
v_mfma_f32_16x16x32_bf16 v[132:135], a[168:171], a[232:235], v[132:135]
s_waitcnt vmcnt(28) lgkmcnt(0)
s_barrier
v_cvt_pk_bf16_f32 v157, v146, v147
v_add_u32 v234, s66, v234
v_mfma_f32_16x16x32_bf16 v[128:131], a[156:159], a[248:251], v[128:131]
v_exp_f32 v148, v140
v_mfma_f32_16x16x32_bf16 v[132:135], a[172:175], a[248:251], v[132:135]
v_exp_f32 v149, v141
v_mfma_f32_16x16x32_bf16 v[0:3], a[80:83], v[152:155], v[0:3]
ds_read_b128 a[112:115], v234 offset:0
v_cvt_pk_bf16_f32 v158, v148, v149
v_mfma_f32_16x16x32_bf16 v[16:19], a[84:87], v[152:155], v[16:19]
v_exp_f32 v150, v142
v_mfma_f32_16x16x32_bf16 v[32:35], a[88:91], v[152:155], v[32:35]
v_exp_f32 v151, v143
v_mfma_f32_16x16x32_bf16 v[48:51], a[92:95], v[152:155], v[48:51]
v_or3_b32 v176, v156, v157, v158
v_cvt_pk_bf16_f32 v159, v150, v151
v_mfma_f32_16x16x32_bf16 v[64:67], a[96:99], v[152:155], v[64:67]
ds_read_b128 a[116:119], v234 offset:64
v_bitop3_b32 v176, v176, v159, s87 bitop3:0xa8
v_mfma_f32_16x16x32_bf16 v[80:83], a[100:103], v[152:155], v[80:83]
ds_read_b128 a[120:123], v234 offset:128
s_add_u32 m0, m0, 1024
v_cmp_ne_u32 vcc, 0, v176
v_mfma_f32_16x16x32_bf16 v[96:99], a[104:107], v[152:155], v[96:99]
buffer_load_dwordx4 v230, s[40:43], s64 offen lds
v_mfma_f32_16x16x32_bf16 v[112:115], a[108:111], v[152:155], v[112:115]
ds_read_b128 a[124:127], v234 offset:192
v_add_u32 v233, s66, v233
s_cmp_lg_u64 vcc, 0
v_mfma_f32_16x16x32_bf16 v[208:211], v[180:183], v[152:155], v[208:211]
ds_read_b128 a[128:131], v234 offset:4608
s_cbranch_scc1 SLOW29
SLOWR29:
v_mfma_f32_16x16x32_bf16 v[136:139], a[144:147], a[204:207], v[172:175]
v_exp_f32 v144, v128
v_mfma_f32_16x16x32_bf16 v[140:143], a[160:163], a[204:207], v[172:175]
v_exp_f32 v145, v129
v_mfma_f32_16x16x32_bf16 v[136:139], a[148:151], a[220:223], v[136:139]
ds_read_b128 a[132:135], v234 offset:4672
v_cvt_pk_bf16_f32 v152, v144, v145
v_mfma_f32_16x16x32_bf16 v[140:143], a[164:167], a[220:223], v[140:143]
v_exp_f32 v146, v130
v_mfma_f32_16x16x32_bf16 v[136:139], a[152:155], a[236:239], v[136:139]
v_exp_f32 v147, v131
v_mfma_f32_16x16x32_bf16 v[140:143], a[168:171], a[236:239], v[140:143]
ds_read_b128 a[136:139], v234 offset:4736
v_cvt_pk_bf16_f32 v153, v146, v147
v_mfma_f32_16x16x32_bf16 v[136:139], a[156:159], a[252:255], v[136:139]
v_exp_f32 v148, v132
v_mfma_f32_16x16x32_bf16 v[140:143], a[172:175], a[252:255], v[140:143]
v_exp_f32 v149, v133
v_mfma_f32_16x16x32_bf16 v[4:7], a[80:83], v[156:159], v[4:7]
ds_read_b128 a[140:143], v234 offset:4800
v_cvt_pk_bf16_f32 v154, v148, v149
v_mfma_f32_16x16x32_bf16 v[20:23], a[84:87], v[156:159], v[20:23]
v_exp_f32 v150, v134
v_mfma_f32_16x16x32_bf16 v[36:39], a[88:91], v[156:159], v[36:39]
v_exp_f32 v151, v135
)"
R"(
v_mfma_f32_16x16x32_bf16 v[52:55], a[92:95], v[156:159], v[52:55]
v_or3_b32 v176, v152, v153, v154
v_cvt_pk_bf16_f32 v155, v150, v151
v_mfma_f32_16x16x32_bf16 v[68:71], a[96:99], v[156:159], v[68:71]
ds_read_b64_tr_b16 a[48:49], v233 offset:18432
v_bitop3_b32 v176, v176, v155, s87 bitop3:0xa8
v_mfma_f32_16x16x32_bf16 v[84:87], a[100:103], v[156:159], v[84:87]
ds_read_b64_tr_b16 a[50:51], v233 offset:23040
s_add_u32 m0, m0, 1024
v_cmp_ne_u32 vcc, 0, v176
v_mfma_f32_16x16x32_bf16 v[100:103], a[104:107], v[156:159], v[100:103]
buffer_load_dwordx4 v231, s[40:43], s64 offen lds
v_mfma_f32_16x16x32_bf16 v[116:119], a[108:111], v[156:159], v[116:119]
buffer_load_dwordx3 a[180:182], v235, s[56:59], s80 offen offset:0
s_add_u32 s80, s80, 1024
s_cmp_lg_u64 vcc, 0
v_mfma_f32_16x16x32_bf16 v[212:215], v[180:183], v[156:159], v[212:215]
ds_read_b64_tr_b16 a[52:53], v233 offset:18464
s_cbranch_scc1 SLOW30
SLOWR30:
s_waitcnt lgkmcnt(3)
v_mfma_f32_16x16x32_bf16 v[128:131], a[112:115], a[192:195], v[160:163]
v_exp_f32 v144, v136
v_mfma_f32_16x16x32_bf16 v[132:135], a[128:131], a[192:195], v[160:163]
v_exp_f32 v145, v137
v_mfma_f32_16x16x32_bf16 v[128:131], a[116:119], a[208:211], v[128:131]
ds_read_b64_tr_b16 a[54:55], v233 offset:23072
v_cvt_pk_bf16_f32 v156, v144, v145
v_mfma_f32_16x16x32_bf16 v[132:135], a[132:135], a[208:211], v[132:135]
v_exp_f32 v146, v138
v_mfma_f32_16x16x32_bf16 v[128:131], a[120:123], a[224:227], v[128:131]
v_exp_f32 v147, v139
v_mfma_f32_16x16x32_bf16 v[132:135], a[136:139], a[224:227], v[132:135]
ds_read_b64_tr_b16 a[56:57], v233 offset:18496
v_cvt_pk_bf16_f32 v157, v146, v147
v_mfma_f32_16x16x32_bf16 v[128:131], a[124:127], a[240:243], v[128:131]
v_exp_f32 v148, v140
v_mfma_f32_16x16x32_bf16 v[132:135], a[140:143], a[240:243], v[132:135]
v_exp_f32 v149, v141
v_mfma_f32_16x16x32_bf16 v[8:11], a[80:83], v[152:155], v[8:11]
ds_read_b64_tr_b16 a[58:59], v233 offset:23104
v_cvt_pk_bf16_f32 v158, v148, v149
v_mfma_f32_16x16x32_bf16 v[24:27], a[84:87], v[152:155], v[24:27]
v_exp_f32 v150, v142
v_mfma_f32_16x16x32_bf16 v[40:43], a[88:91], v[152:155], v[40:43]
v_exp_f32 v151, v143
v_mfma_f32_16x16x32_bf16 v[56:59], a[92:95], v[152:155], v[56:59]
v_or3_b32 v176, v156, v157, v158
v_cvt_pk_bf16_f32 v159, v150, v151
v_mfma_f32_16x16x32_bf16 v[72:75], a[96:99], v[152:155], v[72:75]
ds_read_b64_tr_b16 a[60:61], v233 offset:18528
v_bitop3_b32 v176, v176, v159, s87 bitop3:0xa8
v_mfma_f32_16x16x32_bf16 v[88:91], a[100:103], v[152:155], v[88:91]
ds_read_b64_tr_b16 a[62:63], v233 offset:23136
s_add_u32 m0, m0, 1024
v_cmp_ne_u32 vcc, 0, v176
v_mfma_f32_16x16x32_bf16 v[104:107], a[104:107], v[152:155], v[104:107]
buffer_load_dwordx4 v232, s[40:43], s64 offen lds
s_add_u32 s64, s64, s65
s_add_u32 s69, s69, 36864
s_cmp_eq_u32 s69, 147456
s_cselect_b32 s69, 0, s69
v_mfma_f32_16x16x32_bf16 v[120:123], a[108:111], v[152:155], v[120:123]
buffer_load_dwordx3 a[180:182], v235, s[56:59], s80 offen offset:0
s_add_u32 s80, s80, 1024
s_cmp_lg_u64 vcc, 0
v_mfma_f32_16x16x32_bf16 v[216:219], v[180:183], v[152:155], v[216:219]
ds_read_b64_tr_b16 a[64:65], v233 offset:18560
s_cbranch_scc1 SLOW31
SLOWR31:
v_mfma_f32_16x16x32_bf16 v[136:139], a[112:115], a[196:199], v[164:167]
v_exp_f32 v144, v128
v_mfma_f32_16x16x32_bf16 v[140:143], a[128:131], a[196:199], v[164:167]
v_exp_f32 v145, v129
s_waitcnt lgkmcnt(3)
v_mfma_f32_16x16x32_bf16 v[136:139], a[116:119], a[212:215], v[136:139]
ds_read_b64_tr_b16 a[66:67], v233 offset:23168
v_cvt_pk_bf16_f32 v152, v144, v145
v_mfma_f32_16x16x32_bf16 v[140:143], a[132:135], a[212:215], v[140:143]
v_exp_f32 v146, v130
v_mfma_f32_16x16x32_bf16 v[136:139], a[120:123], a[228:231], v[136:139]
v_exp_f32 v147, v131
v_mfma_f32_16x16x32_bf16 v[140:143], a[136:139], a[228:231], v[140:143]
ds_read_b64_tr_b16 a[68:69], v233 offset:18592
v_cvt_pk_bf16_f32 v153, v146, v147
v_mfma_f32_16x16x32_bf16 v[136:139], a[124:127], a[244:247], v[136:139]
v_exp_f32 v148, v132
v_mfma_f32_16x16x32_bf16 v[140:143], a[140:143], a[244:247], v[140:143]
v_exp_f32 v149, v133
v_mfma_f32_16x16x32_bf16 v[12:15], a[80:83], v[156:159], v[12:15]
ds_read_b64_tr_b16 a[70:71], v233 offset:23200
v_cvt_pk_bf16_f32 v154, v148, v149
v_mfma_f32_16x16x32_bf16 v[28:31], a[84:87], v[156:159], v[28:31]
v_exp_f32 v150, v134
v_mfma_f32_16x16x32_bf16 v[44:47], a[88:91], v[156:159], v[44:47]
v_exp_f32 v151, v135
v_mfma_f32_16x16x32_bf16 v[60:63], a[92:95], v[156:159], v[60:63]
v_or3_b32 v176, v152, v153, v154
v_cvt_pk_bf16_f32 v155, v150, v151
v_mfma_f32_16x16x32_bf16 v[76:79], a[96:99], v[156:159], v[76:79]
ds_read_b64_tr_b16 a[72:73], v233 offset:18624
v_bitop3_b32 v176, v176, v155, s87 bitop3:0xa8
v_mfma_f32_16x16x32_bf16 v[92:95], a[100:103], v[156:159], v[92:95]
ds_read_b64_tr_b16 a[74:75], v233 offset:23232
s_add_u32 m0, s70, s69
v_cmp_ne_u32 vcc, 0, v176
v_mfma_f32_16x16x32_bf16 v[108:111], a[104:107], v[156:159], v[108:111]
buffer_load_dwordx4 v224, s[40:43], s64 offen lds
v_mfma_f32_16x16x32_bf16 v[124:127], a[108:111], v[156:159], v[124:127]
buffer_load_dwordx3 a[180:182], v235, s[56:59], s80 offen offset:0
s_add_u32 s80, s80, 1024
s_cmp_lg_u64 vcc, 0
v_mfma_f32_16x16x32_bf16 v[220:223], v[180:183], v[156:159], v[220:223]
ds_read_b64_tr_b16 a[76:77], v233 offset:18656
s_cbranch_scc1 SLOW32
SLOWR32:
v_mfma_f32_16x16x32_bf16 v[128:131], a[112:115], a[200:203], v[168:171]
v_exp_f32 v144, v136
v_mfma_f32_16x16x32_bf16 v[132:135], a[128:131], a[200:203], v[168:171]
v_exp_f32 v145, v137
v_mfma_f32_16x16x32_bf16 v[128:131], a[116:119], a[216:219], v[128:131]
ds_read_b64_tr_b16 a[78:79], v233 offset:23264
v_cvt_pk_bf16_f32 v156, v144, v145
v_mfma_f32_16x16x32_bf16 v[132:135], a[132:135], a[216:219], v[132:135]
v_exp_f32 v146, v138
v_mfma_f32_16x16x32_bf16 v[128:131], a[120:123], a[232:235], v[128:131]
v_exp_f32 v147, v139
v_mfma_f32_16x16x32_bf16 v[132:135], a[136:139], a[232:235], v[132:135]
ds_read_b128 a[144:147], v234 offset:9216
v_cvt_pk_bf16_f32 v157, v146, v147
v_mfma_f32_16x16x32_bf16 v[128:131], a[124:127], a[248:251], v[128:131]
v_exp_f32 v148, v140
v_mfma_f32_16x16x32_bf16 v[132:135], a[140:143], a[248:251], v[132:135]
v_exp_f32 v149, v141
s_waitcnt lgkmcnt(1)
v_mfma_f32_16x16x32_bf16 v[0:3], a[48:51], v[152:155], v[0:3]
ds_read_b128 a[148:151], v234 offset:9280
v_cvt_pk_bf16_f32 v158, v148, v149
v_mfma_f32_16x16x32_bf16 v[16:19], a[52:55], v[152:155], v[16:19]
v_exp_f32 v150, v142
v_mfma_f32_16x16x32_bf16 v[32:35], a[56:59], v[152:155], v[32:35]
v_exp_f32 v151, v143
v_mfma_f32_16x16x32_bf16 v[48:51], a[60:63], v[152:155], v[48:51]
v_or3_b32 v176, v156, v157, v158
v_cvt_pk_bf16_f32 v159, v150, v151
v_mfma_f32_16x16x32_bf16 v[64:67], a[64:67], v[152:155], v[64:67]
ds_read_b128 a[152:155], v234 offset:9344
v_bitop3_b32 v176, v176, v159, s87 bitop3:0xa8
v_mfma_f32_16x16x32_bf16 v[80:83], a[68:71], v[152:155], v[80:83]
ds_read_b128 a[156:159], v234 offset:9408
s_add_u32 m0, m0, 1024
v_cmp_ne_u32 vcc, 0, v176
v_mfma_f32_16x16x32_bf16 v[96:99], a[72:75], v[152:155], v[96:99]
buffer_load_dwordx4 v225, s[40:43], s64 offen lds
v_mfma_f32_16x16x32_bf16 v[112:115], a[76:79], v[152:155], v[112:115]
buffer_load_dwordx3 a[180:182], v235, s[56:59], s80 offen offset:0
s_add_u32 s80, s80, 1024
s_cmp_lg_u64 vcc, 0
v_mfma_f32_16x16x32_bf16 v[208:211], v[180:183], v[152:155], v[208:211]
ds_read_b128 a[160:163], v234 offset:13824
s_cbranch_scc1 SLOW33
SLOWR33:
v_mfma_f32_16x16x32_bf16 v[136:139], a[112:115], a[204:207], v[172:175]
v_exp_f32 v144, v128
v_mfma_f32_16x16x32_bf16 v[140:143], a[128:131], a[204:207], v[172:175]
v_exp_f32 v145, v129
v_mfma_f32_16x16x32_bf16 v[136:139], a[116:119], a[220:223], v[136:139]
ds_read_b128 a[164:167], v234 offset:13888
v_cvt_pk_bf16_f32 v152, v144, v145
v_mfma_f32_16x16x32_bf16 v[140:143], a[132:135], a[220:223], v[140:143]
v_exp_f32 v146, v130
v_mfma_f32_16x16x32_bf16 v[136:139], a[120:123], a[236:239], v[136:139]
v_exp_f32 v147, v131
v_mfma_f32_16x16x32_bf16 v[140:143], a[136:139], a[236:239], v[140:143]
ds_read_b128 a[168:171], v234 offset:13952
v_cvt_pk_bf16_f32 v153, v146, v147
v_mfma_f32_16x16x32_bf16 v[136:139], a[124:127], a[252:255], v[136:139]
v_exp_f32 v148, v132
v_mfma_f32_16x16x32_bf16 v[140:143], a[140:143], a[252:255], v[140:143]
v_exp_f32 v149, v133
v_mfma_f32_16x16x32_bf16 v[4:7], a[48:51], v[156:159], v[4:7]
ds_read_b128 a[172:175], v234 offset:14016
v_cvt_pk_bf16_f32 v154, v148, v149
v_mfma_f32_16x16x32_bf16 v[20:23], a[52:55], v[156:159], v[20:23]
v_exp_f32 v150, v134
v_mfma_f32_16x16x32_bf16 v[36:39], a[56:59], v[156:159], v[36:39]
v_exp_f32 v151, v135
v_mfma_f32_16x16x32_bf16 v[52:55], a[60:63], v[156:159], v[52:55]
v_or3_b32 v176, v152, v153, v154
v_cvt_pk_bf16_f32 v155, v150, v151
v_mfma_f32_16x16x32_bf16 v[68:71], a[64:67], v[156:159], v[68:71]
ds_read_b64_tr_b16 a[80:81], v233 offset:27648
v_bitop3_b32 v176, v176, v155, s87 bitop3:0xa8
v_mfma_f32_16x16x32_bf16 v[84:87], a[68:71], v[156:159], v[84:87]
ds_read_b64_tr_b16 a[82:83], v233 offset:32256
s_add_u32 m0, m0, 1024
v_cmp_ne_u32 vcc, 0, v176
v_mfma_f32_16x16x32_bf16 v[100:103], a[72:75], v[156:159], v[100:103]
buffer_load_dwordx4 v226, s[40:43], s64 offen lds
s_add_u32 m0, m0, 1024
v_mfma_f32_16x16x32_bf16 v[116:119], a[76:79], v[156:159], v[116:119]
buffer_load_dwordx4 v227, s[40:43], s64 offen lds
s_waitcnt vmcnt(19)
s_add_u32 s77, s39, 1024
v_mov_b32 v238, s82
v_mov_b32 v239, s83
s_mov_b64 exec, 1
buffer_store_dwordx2 v[238:239], off, s[52:55], s77 offset:0 sc1
s_mov_b64 exec, -1
s_nop 1
s_cmp_lg_u64 vcc, 0
v_mfma_f32_16x16x32_bf16 v[212:215], v[180:183], v[156:159], v[212:215]
ds_read_b64_tr_b16 a[84:85], v233 offset:27680
ds_read_b64_tr_b16 a[86:87], v233 offset:32288
s_cbranch_scc1 SLOW34
SLOWR34:
s_waitcnt lgkmcnt(4)
v_mfma_f32_16x16x32_bf16 v[128:131], a[144:147], a[192:195], v[160:163]
v_exp_f32 v144, v136
v_mfma_f32_16x16x32_bf16 v[132:135], a[160:163], a[192:195], v[160:163]
v_exp_f32 v145, v137
v_mfma_f32_16x16x32_bf16 v[128:131], a[148:151], a[208:211], v[128:131]
ds_read_b64_tr_b16 a[88:89], v233 offset:27712
v_cvt_pk_bf16_f32 v156, v144, v145
v_mfma_f32_16x16x32_bf16 v[132:135], a[164:167], a[208:211], v[132:135]
v_exp_f32 v146, v138
v_mfma_f32_16x16x32_bf16 v[128:131], a[152:155], a[224:227], v[128:131]
v_exp_f32 v147, v139
v_mfma_f32_16x16x32_bf16 v[132:135], a[168:171], a[224:227], v[132:135]
ds_read_b64_tr_b16 a[90:91], v233 offset:32320
v_cvt_pk_bf16_f32 v157, v146, v147
v_mfma_f32_16x16x32_bf16 v[128:131], a[156:159], a[240:243], v[128:131]
v_exp_f32 v148, v140
v_mfma_f32_16x16x32_bf16 v[132:135], a[172:175], a[240:243], v[132:135]
v_exp_f32 v149, v141
v_mfma_f32_16x16x32_bf16 v[8:11], a[48:51], v[152:155], v[8:11]
ds_read_b64_tr_b16 a[92:93], v233 offset:27744
v_cvt_pk_bf16_f32 v158, v148, v149
v_mfma_f32_16x16x32_bf16 v[24:27], a[52:55], v[152:155], v[24:27]
v_exp_f32 v150, v142
v_mfma_f32_16x16x32_bf16 v[40:43], a[56:59], v[152:155], v[40:43]
v_exp_f32 v151, v143
v_mfma_f32_16x16x32_bf16 v[56:59], a[60:63], v[152:155], v[56:59]
v_or3_b32 v176, v156, v157, v158
v_cvt_pk_bf16_f32 v159, v150, v151
v_mfma_f32_16x16x32_bf16 v[72:75], a[64:67], v[152:155], v[72:75]
ds_read_b64_tr_b16 a[94:95], v233 offset:32352
v_bitop3_b32 v176, v176, v159, s87 bitop3:0xa8
v_mfma_f32_16x16x32_bf16 v[88:91], a[68:71], v[152:155], v[88:91]
ds_read_b64_tr_b16 a[96:97], v233 offset:27776
s_add_u32 m0, m0, 1024
v_cmp_ne_u32 vcc, 0, v176
v_mfma_f32_16x16x32_bf16 v[104:107], a[72:75], v[152:155], v[104:107]
buffer_load_dwordx4 v228, s[40:43], s64 offen lds
s_add_u32 m0, m0, 1024
v_mfma_f32_16x16x32_bf16 v[120:123], a[76:79], v[152:155], v[120:123]
buffer_load_dwordx4 v229, s[40:43], s64 offen lds
s_cmp_lg_u64 vcc, 0
v_mfma_f32_16x16x32_bf16 v[216:219], v[180:183], v[152:155], v[216:219]
ds_read_b64_tr_b16 a[98:99], v233 offset:32384
s_cbranch_scc1 SLOW35
SLOWR35:
v_mfma_f32_16x16x32_bf16 v[136:139], a[144:147], a[196:199], v[164:167]
v_exp_f32 v144, v128
v_mfma_f32_16x16x32_bf16 v[140:143], a[160:163], a[196:199], v[164:167]
v_exp_f32 v145, v129
s_waitcnt lgkmcnt(3)
v_mfma_f32_16x16x32_bf16 v[136:139], a[148:151], a[212:215], v[136:139]
ds_read_b64_tr_b16 a[100:101], v233 offset:27808
v_cvt_pk_bf16_f32 v152, v144, v145
v_mfma_f32_16x16x32_bf16 v[140:143], a[164:167], a[212:215], v[140:143]
v_exp_f32 v146, v130
v_mfma_f32_16x16x32_bf16 v[136:139], a[152:155], a[228:231], v[136:139]
v_exp_f32 v147, v131
v_mfma_f32_16x16x32_bf16 v[140:143], a[168:171], a[228:231], v[140:143]
ds_read_b64_tr_b16 a[102:103], v233 offset:32416
v_cvt_pk_bf16_f32 v153, v146, v147
v_mfma_f32_16x16x32_bf16 v[136:139], a[156:159], a[244:247], v[136:139]
v_exp_f32 v148, v132
v_mfma_f32_16x16x32_bf16 v[140:143], a[172:175], a[244:247], v[140:143]
v_exp_f32 v149, v133
v_mfma_f32_16x16x32_bf16 v[12:15], a[48:51], v[156:159], v[12:15]
ds_read_b64_tr_b16 a[104:105], v233 offset:27840
v_cvt_pk_bf16_f32 v154, v148, v149
v_mfma_f32_16x16x32_bf16 v[28:31], a[52:55], v[156:159], v[28:31]
v_exp_f32 v150, v134
v_mfma_f32_16x16x32_bf16 v[44:47], a[56:59], v[156:159], v[44:47]
v_exp_f32 v151, v135
v_mfma_f32_16x16x32_bf16 v[60:63], a[60:63], v[156:159], v[60:63]
v_or3_b32 v176, v152, v153, v154
v_cvt_pk_bf16_f32 v155, v150, v151
v_mfma_f32_16x16x32_bf16 v[76:79], a[64:67], v[156:159], v[76:79]
ds_read_b64_tr_b16 a[106:107], v233 offset:32448
v_bitop3_b32 v176, v176, v155, s87 bitop3:0xa8
v_mfma_f32_16x16x32_bf16 v[92:95], a[68:71], v[156:159], v[92:95]
ds_read_b64_tr_b16 a[108:109], v233 offset:27872
s_add_u32 m0, m0, 1024
v_cmp_ne_u32 vcc, 0, v176
v_mfma_f32_16x16x32_bf16 v[108:111], a[72:75], v[156:159], v[108:111]
buffer_load_dwordx4 v230, s[40:43], s64 offen lds
s_add_u32 m0, m0, 1024
s_add_u32 s67, s67, 36864
s_cmp_eq_u32 s67, 147456
s_cselect_b32 s66, s76, 36864
s_cselect_b32 s67, 0, s67
v_mfma_f32_16x16x32_bf16 v[124:127], a[76:79], v[156:159], v[124:127]
buffer_load_dwordx4 v231, s[40:43], s64 offen lds
s_cmp_lg_u64 vcc, 0
v_mfma_f32_16x16x32_bf16 v[220:223], v[180:183], v[156:159], v[220:223]
ds_read_b64_tr_b16 a[110:111], v233 offset:32480
s_cbranch_scc1 SLOW36
SLOWR36:
v_mfma_f32_16x16x32_bf16 v[128:131], a[144:147], a[200:203], v[168:171]
v_exp_f32 v144, v136
v_mfma_f32_16x16x32_bf16 v[132:135], a[160:163], a[200:203], v[168:171]
v_exp_f32 v145, v137
v_mfma_f32_16x16x32_bf16 v[128:131], a[148:151], a[216:219], v[128:131]
v_cvt_pk_bf16_f32 v156, v144, v145
v_mfma_f32_16x16x32_bf16 v[132:135], a[164:167], a[216:219], v[132:135]
v_exp_f32 v146, v138
v_mfma_f32_16x16x32_bf16 v[128:131], a[152:155], a[232:235], v[128:131]
v_exp_f32 v147, v139
v_mfma_f32_16x16x32_bf16 v[132:135], a[168:171], a[232:235], v[132:135]
s_waitcnt vmcnt(29) lgkmcnt(0)
s_barrier
v_cvt_pk_bf16_f32 v157, v146, v147
v_add_u32 v234, s66, v234
v_mfma_f32_16x16x32_bf16 v[128:131], a[156:159], a[248:251], v[128:131]
v_exp_f32 v148, v140
v_mfma_f32_16x16x32_bf16 v[132:135], a[172:175], a[248:251], v[132:135]
v_exp_f32 v149, v141
v_mfma_f32_16x16x32_bf16 v[0:3], a[80:83], v[152:155], v[0:3]
ds_read_b128 a[112:115], v234 offset:0
v_cvt_pk_bf16_f32 v158, v148, v149
v_mfma_f32_16x16x32_bf16 v[16:19], a[84:87], v[152:155], v[16:19]
v_exp_f32 v150, v142
v_mfma_f32_16x16x32_bf16 v[32:35], a[88:91], v[152:155], v[32:35]
v_exp_f32 v151, v143
v_mfma_f32_16x16x32_bf16 v[48:51], a[92:95], v[152:155], v[48:51]
v_or3_b32 v176, v156, v157, v158
v_cvt_pk_bf16_f32 v159, v150, v151
v_mfma_f32_16x16x32_bf16 v[64:67], a[96:99], v[152:155], v[64:67]
ds_read_b128 a[116:119], v234 offset:64
v_bitop3_b32 v176, v176, v159, s87 bitop3:0xa8
v_mfma_f32_16x16x32_bf16 v[80:83], a[100:103], v[152:155], v[80:83]
ds_read_b128 a[120:123], v234 offset:128
s_add_u32 m0, m0, 1024
v_cmp_ne_u32 vcc, 0, v176
v_mfma_f32_16x16x32_bf16 v[96:99], a[104:107], v[152:155], v[96:99]
buffer_load_dwordx4 v232, s[40:43], s64 offen lds
s_add_u32 s64, s64, s65
s_add_u32 s69, s69, 36864
s_cmp_eq_u32 s69, 147456
s_cselect_b32 s69, 0, s69
s_mov_b64 s[40:41], s[30:31]
s_and_b32 s41, s41, 65535
s_mul_i32 s42, s93, 36864
s_sub_u32 s42, s42, 864
s_lshr_b32 s77, %[wid], 1
s_lshl_b32 s77, s77, 2
s_and_b32 s78, %[wid], 1
s_add_u32 s77, s77, s78
s_cmp_lt_u32 s34, 4
s_cselect_b32 s77, s77, %[wid]
s_cselect_b32 s78, 8, 4
s_mul_i32 s64, s77, 36864
s_mul_i32 s65, s78, 36864
v_mfma_f32_16x16x32_bf16 v[112:115], a[108:111], v[152:155], v[112:115]
ds_read_b128 a[124:127], v234 offset:192
v_add_u32 v233, s66, v233
s_cmp_lg_u64 vcc, 0
v_mfma_f32_16x16x32_bf16 v[208:211], v[180:183], v[152:155], v[208:211]
ds_read_b128 a[128:131], v234 offset:4608
s_cbranch_scc1 SLOW37
SLOWR37:
v_mfma_f32_16x16x32_bf16 v[136:139], a[144:147], a[204:207], v[172:175]
v_exp_f32 v144, v128
v_mfma_f32_16x16x32_bf16 v[140:143], a[160:163], a[204:207], v[172:175]
v_exp_f32 v145, v129
v_mfma_f32_16x16x32_bf16 v[136:139], a[148:151], a[220:223], v[136:139]
ds_read_b128 a[132:135], v234 offset:4672
v_cvt_pk_bf16_f32 v152, v144, v145
v_mfma_f32_16x16x32_bf16 v[140:143], a[164:167], a[220:223], v[140:143]
v_exp_f32 v146, v130
v_mfma_f32_16x16x32_bf16 v[136:139], a[152:155], a[236:239], v[136:139]
v_exp_f32 v147, v131
v_mfma_f32_16x16x32_bf16 v[140:143], a[168:171], a[236:239], v[140:143]
ds_read_b128 a[136:139], v234 offset:4736
v_cvt_pk_bf16_f32 v153, v146, v147
v_mfma_f32_16x16x32_bf16 v[136:139], a[156:159], a[252:255], v[136:139]
v_exp_f32 v148, v132
v_mfma_f32_16x16x32_bf16 v[140:143], a[172:175], a[252:255], v[140:143]
v_exp_f32 v149, v133
v_mfma_f32_16x16x32_bf16 v[4:7], a[80:83], v[156:159], v[4:7]
ds_read_b128 a[140:143], v234 offset:4800
v_cvt_pk_bf16_f32 v154, v148, v149
v_mfma_f32_16x16x32_bf16 v[20:23], a[84:87], v[156:159], v[20:23]
v_exp_f32 v150, v134
v_mfma_f32_16x16x32_bf16 v[36:39], a[88:91], v[156:159], v[36:39]
v_exp_f32 v151, v135
)"
R"(
v_mfma_f32_16x16x32_bf16 v[52:55], a[92:95], v[156:159], v[52:55]
v_or3_b32 v176, v152, v153, v154
v_cvt_pk_bf16_f32 v155, v150, v151
v_mfma_f32_16x16x32_bf16 v[68:71], a[96:99], v[156:159], v[68:71]
ds_read_b64_tr_b16 a[48:49], v233 offset:18432
v_bitop3_b32 v176, v176, v155, s87 bitop3:0xa8
v_mfma_f32_16x16x32_bf16 v[84:87], a[100:103], v[156:159], v[84:87]
ds_read_b64_tr_b16 a[50:51], v233 offset:23040
s_add_u32 m0, s70, s69
v_cmp_ne_u32 vcc, 0, v176
v_mfma_f32_16x16x32_bf16 v[100:103], a[104:107], v[156:159], v[100:103]
buffer_load_dwordx4 v240, s[40:43], s64 offen lds
s_add_u32 m0, m0, 1024
v_mfma_f32_16x16x32_bf16 v[116:119], a[108:111], v[156:159], v[116:119]
buffer_load_dwordx4 v241, s[40:43], s64 offen lds
s_cmp_lg_u64 vcc, 0
v_mfma_f32_16x16x32_bf16 v[212:215], v[180:183], v[156:159], v[212:215]
ds_read_b64_tr_b16 a[52:53], v233 offset:18464
s_cbranch_scc1 SLOW38
SLOWR38:
s_waitcnt lgkmcnt(3)
v_mfma_f32_16x16x32_bf16 v[128:131], a[112:115], a[192:195], v[160:163]
v_exp_f32 v144, v136
v_mfma_f32_16x16x32_bf16 v[132:135], a[128:131], a[192:195], v[160:163]
v_exp_f32 v145, v137
v_mfma_f32_16x16x32_bf16 v[128:131], a[116:119], a[208:211], v[128:131]
ds_read_b64_tr_b16 a[54:55], v233 offset:23072
v_cvt_pk_bf16_f32 v156, v144, v145
v_mfma_f32_16x16x32_bf16 v[132:135], a[132:135], a[208:211], v[132:135]
v_exp_f32 v146, v138
v_mfma_f32_16x16x32_bf16 v[128:131], a[120:123], a[224:227], v[128:131]
v_exp_f32 v147, v139
v_mfma_f32_16x16x32_bf16 v[132:135], a[136:139], a[224:227], v[132:135]
ds_read_b64_tr_b16 a[56:57], v233 offset:18496
v_cvt_pk_bf16_f32 v157, v146, v147
v_mfma_f32_16x16x32_bf16 v[128:131], a[124:127], a[240:243], v[128:131]
v_exp_f32 v148, v140
v_mfma_f32_16x16x32_bf16 v[132:135], a[140:143], a[240:243], v[132:135]
v_exp_f32 v149, v141
v_mfma_f32_16x16x32_bf16 v[8:11], a[80:83], v[152:155], v[8:11]
ds_read_b64_tr_b16 a[58:59], v233 offset:23104
v_cvt_pk_bf16_f32 v158, v148, v149
v_mfma_f32_16x16x32_bf16 v[24:27], a[84:87], v[152:155], v[24:27]
v_exp_f32 v150, v142
v_mfma_f32_16x16x32_bf16 v[40:43], a[88:91], v[152:155], v[40:43]
v_exp_f32 v151, v143
v_mfma_f32_16x16x32_bf16 v[56:59], a[92:95], v[152:155], v[56:59]
v_or3_b32 v176, v156, v157, v158
v_cvt_pk_bf16_f32 v159, v150, v151
v_mfma_f32_16x16x32_bf16 v[72:75], a[96:99], v[152:155], v[72:75]
ds_read_b64_tr_b16 a[60:61], v233 offset:18528
v_bitop3_b32 v176, v176, v159, s87 bitop3:0xa8
v_mfma_f32_16x16x32_bf16 v[88:91], a[100:103], v[152:155], v[88:91]
ds_read_b64_tr_b16 a[62:63], v233 offset:23136
s_add_u32 m0, m0, 1024
v_cmp_ne_u32 vcc, 0, v176
v_mfma_f32_16x16x32_bf16 v[104:107], a[104:107], v[152:155], v[104:107]
buffer_load_dwordx4 v242, s[40:43], s64 offen lds
s_add_u32 m0, m0, 1024
v_mfma_f32_16x16x32_bf16 v[120:123], a[108:111], v[152:155], v[120:123]
buffer_load_dwordx4 v243, s[40:43], s64 offen lds
s_cmp_lg_u64 vcc, 0
v_mfma_f32_16x16x32_bf16 v[216:219], v[180:183], v[152:155], v[216:219]
ds_read_b64_tr_b16 a[64:65], v233 offset:18560
s_cbranch_scc1 SLOW39
SLOWR39:
v_mfma_f32_16x16x32_bf16 v[136:139], a[112:115], a[196:199], v[164:167]
v_exp_f32 v144, v128
v_mfma_f32_16x16x32_bf16 v[140:143], a[128:131], a[196:199], v[164:167]
v_exp_f32 v145, v129
s_waitcnt lgkmcnt(3)
v_mfma_f32_16x16x32_bf16 v[136:139], a[116:119], a[212:215], v[136:139]
ds_read_b64_tr_b16 a[66:67], v233 offset:23168
v_cvt_pk_bf16_f32 v152, v144, v145
v_mfma_f32_16x16x32_bf16 v[140:143], a[132:135], a[212:215], v[140:143]
v_exp_f32 v146, v130
v_mfma_f32_16x16x32_bf16 v[136:139], a[120:123], a[228:231], v[136:139]
v_exp_f32 v147, v131
v_mfma_f32_16x16x32_bf16 v[140:143], a[136:139], a[228:231], v[140:143]
ds_read_b64_tr_b16 a[68:69], v233 offset:18592
v_cvt_pk_bf16_f32 v153, v146, v147
v_mfma_f32_16x16x32_bf16 v[136:139], a[124:127], a[244:247], v[136:139]
v_exp_f32 v148, v132
v_mfma_f32_16x16x32_bf16 v[140:143], a[140:143], a[244:247], v[140:143]
v_exp_f32 v149, v133
v_mfma_f32_16x16x32_bf16 v[12:15], a[80:83], v[156:159], v[12:15]
ds_read_b64_tr_b16 a[70:71], v233 offset:23200
v_cvt_pk_bf16_f32 v154, v148, v149
v_mfma_f32_16x16x32_bf16 v[28:31], a[84:87], v[156:159], v[28:31]
v_exp_f32 v150, v134
v_mfma_f32_16x16x32_bf16 v[44:47], a[88:91], v[156:159], v[44:47]
v_exp_f32 v151, v135
v_mfma_f32_16x16x32_bf16 v[60:63], a[92:95], v[156:159], v[60:63]
v_or3_b32 v176, v152, v153, v154
v_cvt_pk_bf16_f32 v155, v150, v151
v_mfma_f32_16x16x32_bf16 v[76:79], a[96:99], v[156:159], v[76:79]
ds_read_b64_tr_b16 a[72:73], v233 offset:18624
v_bitop3_b32 v176, v176, v155, s87 bitop3:0xa8
v_mfma_f32_16x16x32_bf16 v[92:95], a[100:103], v[156:159], v[92:95]
ds_read_b64_tr_b16 a[74:75], v233 offset:23232
s_add_u32 m0, m0, 1024
v_cmp_ne_u32 vcc, 0, v176
v_mfma_f32_16x16x32_bf16 v[108:111], a[104:107], v[156:159], v[108:111]
buffer_load_dwordx4 v244, s[40:43], s64 offen lds
s_add_u32 m0, m0, 1024
v_mfma_f32_16x16x32_bf16 v[124:127], a[108:111], v[156:159], v[124:127]
buffer_load_dwordx4 v245, s[40:43], s64 offen lds
s_cmp_lg_u64 vcc, 0
v_mfma_f32_16x16x32_bf16 v[220:223], v[180:183], v[156:159], v[220:223]
ds_read_b64_tr_b16 a[76:77], v233 offset:18656
s_cbranch_scc1 SLOW40
SLOWR40:
v_mfma_f32_16x16x32_bf16 v[128:131], a[112:115], a[200:203], v[168:171]
v_exp_f32 v144, v136
v_mfma_f32_16x16x32_bf16 v[132:135], a[128:131], a[200:203], v[168:171]
v_exp_f32 v145, v137
v_mfma_f32_16x16x32_bf16 v[128:131], a[116:119], a[216:219], v[128:131]
ds_read_b64_tr_b16 a[78:79], v233 offset:23264
v_cvt_pk_bf16_f32 v156, v144, v145
v_mfma_f32_16x16x32_bf16 v[132:135], a[132:135], a[216:219], v[132:135]
v_exp_f32 v146, v138
v_mfma_f32_16x16x32_bf16 v[128:131], a[120:123], a[232:235], v[128:131]
v_exp_f32 v147, v139
v_mfma_f32_16x16x32_bf16 v[132:135], a[136:139], a[232:235], v[132:135]
ds_read_b128 a[144:147], v234 offset:9216
v_cvt_pk_bf16_f32 v157, v146, v147
v_mfma_f32_16x16x32_bf16 v[128:131], a[124:127], a[248:251], v[128:131]
v_exp_f32 v148, v140
v_mfma_f32_16x16x32_bf16 v[132:135], a[140:143], a[248:251], v[132:135]
v_exp_f32 v149, v141
s_waitcnt lgkmcnt(1)
v_mfma_f32_16x16x32_bf16 v[0:3], a[48:51], v[152:155], v[0:3]
ds_read_b128 a[148:151], v234 offset:9280
v_cvt_pk_bf16_f32 v158, v148, v149
v_mfma_f32_16x16x32_bf16 v[16:19], a[52:55], v[152:155], v[16:19]
v_exp_f32 v150, v142
v_mfma_f32_16x16x32_bf16 v[32:35], a[56:59], v[152:155], v[32:35]
v_exp_f32 v151, v143
v_mfma_f32_16x16x32_bf16 v[48:51], a[60:63], v[152:155], v[48:51]
v_or3_b32 v176, v156, v157, v158
v_cvt_pk_bf16_f32 v159, v150, v151
v_mfma_f32_16x16x32_bf16 v[64:67], a[64:67], v[152:155], v[64:67]
ds_read_b128 a[152:155], v234 offset:9344
v_bitop3_b32 v176, v176, v159, s87 bitop3:0xa8
v_mfma_f32_16x16x32_bf16 v[80:83], a[68:71], v[152:155], v[80:83]
ds_read_b128 a[156:159], v234 offset:9408
s_add_u32 m0, m0, 1024
v_cmp_ne_u32 vcc, 0, v176
v_mfma_f32_16x16x32_bf16 v[96:99], a[72:75], v[152:155], v[96:99]
buffer_load_dwordx4 v246, s[40:43], s64 offen lds
s_add_u32 m0, m0, 1024
v_mfma_f32_16x16x32_bf16 v[112:115], a[76:79], v[152:155], v[112:115]
buffer_load_dwordx4 v247, s[40:43], s64 offen lds
s_cmp_lg_u64 vcc, 0
v_mfma_f32_16x16x32_bf16 v[208:211], v[180:183], v[152:155], v[208:211]
ds_read_b128 a[160:163], v234 offset:13824
s_cbranch_scc1 SLOW41
SLOWR41:
v_mfma_f32_16x16x32_bf16 v[136:139], a[112:115], a[204:207], v[172:175]
v_exp_f32 v144, v128
v_mfma_f32_16x16x32_bf16 v[140:143], a[128:131], a[204:207], v[172:175]
v_exp_f32 v145, v129
v_mfma_f32_16x16x32_bf16 v[136:139], a[116:119], a[220:223], v[136:139]
ds_read_b128 a[164:167], v234 offset:13888
v_cvt_pk_bf16_f32 v152, v144, v145
v_mfma_f32_16x16x32_bf16 v[140:143], a[132:135], a[220:223], v[140:143]
v_exp_f32 v146, v130
v_mfma_f32_16x16x32_bf16 v[136:139], a[120:123], a[236:239], v[136:139]
v_exp_f32 v147, v131
v_mfma_f32_16x16x32_bf16 v[140:143], a[136:139], a[236:239], v[140:143]
ds_read_b128 a[168:171], v234 offset:13952
v_cvt_pk_bf16_f32 v153, v146, v147
v_mfma_f32_16x16x32_bf16 v[136:139], a[124:127], a[252:255], v[136:139]
v_exp_f32 v148, v132
v_mfma_f32_16x16x32_bf16 v[140:143], a[140:143], a[252:255], v[140:143]
v_exp_f32 v149, v133
v_mfma_f32_16x16x32_bf16 v[4:7], a[48:51], v[156:159], v[4:7]
ds_read_b128 a[172:175], v234 offset:14016
v_cvt_pk_bf16_f32 v154, v148, v149
v_mfma_f32_16x16x32_bf16 v[20:23], a[52:55], v[156:159], v[20:23]
v_exp_f32 v150, v134
v_mfma_f32_16x16x32_bf16 v[36:39], a[56:59], v[156:159], v[36:39]
v_exp_f32 v151, v135
v_mfma_f32_16x16x32_bf16 v[52:55], a[60:63], v[156:159], v[52:55]
v_or3_b32 v176, v152, v153, v154
v_cvt_pk_bf16_f32 v155, v150, v151
v_mfma_f32_16x16x32_bf16 v[68:71], a[64:67], v[156:159], v[68:71]
ds_read_b64_tr_b16 a[80:81], v233 offset:27648
v_bitop3_b32 v176, v176, v155, s87 bitop3:0xa8
v_mfma_f32_16x16x32_bf16 v[84:87], a[68:71], v[156:159], v[84:87]
ds_read_b64_tr_b16 a[82:83], v233 offset:32256
s_add_u32 m0, m0, 1024
v_cmp_ne_u32 vcc, 0, v176
v_mfma_f32_16x16x32_bf16 v[100:103], a[72:75], v[156:159], v[100:103]
buffer_load_dwordx4 v248, s[40:43], s64 offen lds
s_add_u32 s64, s64, s65
s_add_u32 s69, s69, 36864
s_cmp_eq_u32 s69, 147456
s_cselect_b32 s69, 0, s69
v_mfma_f32_16x16x32_bf16 v[116:119], a[76:79], v[156:159], v[116:119]
s_cmp_lg_u64 vcc, 0
v_mfma_f32_16x16x32_bf16 v[212:215], v[180:183], v[156:159], v[212:215]
ds_read_b64_tr_b16 a[84:85], v233 offset:27680
ds_read_b64_tr_b16 a[86:87], v233 offset:32288
s_cbranch_scc1 SLOW42
SLOWR42:
s_waitcnt lgkmcnt(4)
v_mfma_f32_16x16x32_bf16 v[128:131], a[144:147], a[192:195], v[160:163]
v_exp_f32 v144, v136
v_mfma_f32_16x16x32_bf16 v[132:135], a[160:163], a[192:195], v[160:163]
v_exp_f32 v145, v137
v_mfma_f32_16x16x32_bf16 v[128:131], a[148:151], a[208:211], v[128:131]
ds_read_b64_tr_b16 a[88:89], v233 offset:27712
v_cvt_pk_bf16_f32 v156, v144, v145
v_mfma_f32_16x16x32_bf16 v[132:135], a[164:167], a[208:211], v[132:135]
v_exp_f32 v146, v138
v_mfma_f32_16x16x32_bf16 v[128:131], a[152:155], a[224:227], v[128:131]
v_exp_f32 v147, v139
v_mfma_f32_16x16x32_bf16 v[132:135], a[168:171], a[224:227], v[132:135]
ds_read_b64_tr_b16 a[90:91], v233 offset:32320
v_cvt_pk_bf16_f32 v157, v146, v147
v_mfma_f32_16x16x32_bf16 v[128:131], a[156:159], a[240:243], v[128:131]
v_exp_f32 v148, v140
v_mfma_f32_16x16x32_bf16 v[132:135], a[172:175], a[240:243], v[132:135]
v_exp_f32 v149, v141
v_mfma_f32_16x16x32_bf16 v[8:11], a[48:51], v[152:155], v[8:11]
ds_read_b64_tr_b16 a[92:93], v233 offset:27744
v_cvt_pk_bf16_f32 v158, v148, v149
v_mfma_f32_16x16x32_bf16 v[24:27], a[52:55], v[152:155], v[24:27]
v_exp_f32 v150, v142
v_mfma_f32_16x16x32_bf16 v[40:43], a[56:59], v[152:155], v[40:43]
v_exp_f32 v151, v143
v_mfma_f32_16x16x32_bf16 v[56:59], a[60:63], v[152:155], v[56:59]
v_or3_b32 v176, v156, v157, v158
v_cvt_pk_bf16_f32 v159, v150, v151
v_mfma_f32_16x16x32_bf16 v[72:75], a[64:67], v[152:155], v[72:75]
ds_read_b64_tr_b16 a[94:95], v233 offset:32352
v_bitop3_b32 v176, v176, v159, s87 bitop3:0xa8
v_mfma_f32_16x16x32_bf16 v[88:91], a[68:71], v[152:155], v[88:91]
ds_read_b64_tr_b16 a[96:97], v233 offset:27776
v_cmp_ne_u32 vcc, 0, v176
v_mfma_f32_16x16x32_bf16 v[104:107], a[72:75], v[152:155], v[104:107]
v_mfma_f32_16x16x32_bf16 v[120:123], a[76:79], v[152:155], v[120:123]
s_cmp_lg_u64 vcc, 0
v_mfma_f32_16x16x32_bf16 v[216:219], v[180:183], v[152:155], v[216:219]
ds_read_b64_tr_b16 a[98:99], v233 offset:32384
s_cbranch_scc1 SLOW43
SLOWR43:
v_mfma_f32_16x16x32_bf16 v[136:139], a[144:147], a[196:199], v[164:167]
v_exp_f32 v144, v128
v_mfma_f32_16x16x32_bf16 v[140:143], a[160:163], a[196:199], v[164:167]
v_exp_f32 v145, v129
s_waitcnt lgkmcnt(3)
v_mfma_f32_16x16x32_bf16 v[136:139], a[148:151], a[212:215], v[136:139]
ds_read_b64_tr_b16 a[100:101], v233 offset:27808
v_cvt_pk_bf16_f32 v152, v144, v145
v_mfma_f32_16x16x32_bf16 v[140:143], a[164:167], a[212:215], v[140:143]
v_exp_f32 v146, v130
v_mfma_f32_16x16x32_bf16 v[136:139], a[152:155], a[228:231], v[136:139]
v_exp_f32 v147, v131
v_mfma_f32_16x16x32_bf16 v[140:143], a[168:171], a[228:231], v[140:143]
ds_read_b64_tr_b16 a[102:103], v233 offset:32416
v_cvt_pk_bf16_f32 v153, v146, v147
v_mfma_f32_16x16x32_bf16 v[136:139], a[156:159], a[244:247], v[136:139]
v_exp_f32 v148, v132
v_mfma_f32_16x16x32_bf16 v[140:143], a[172:175], a[244:247], v[140:143]
v_exp_f32 v149, v133
v_mfma_f32_16x16x32_bf16 v[12:15], a[48:51], v[156:159], v[12:15]
ds_read_b64_tr_b16 a[104:105], v233 offset:27840
v_cvt_pk_bf16_f32 v154, v148, v149
v_mfma_f32_16x16x32_bf16 v[28:31], a[52:55], v[156:159], v[28:31]
v_exp_f32 v150, v134
v_mfma_f32_16x16x32_bf16 v[44:47], a[56:59], v[156:159], v[44:47]
v_exp_f32 v151, v135
v_mfma_f32_16x16x32_bf16 v[60:63], a[60:63], v[156:159], v[60:63]
v_or3_b32 v176, v152, v153, v154
v_cvt_pk_bf16_f32 v155, v150, v151
v_mfma_f32_16x16x32_bf16 v[76:79], a[64:67], v[156:159], v[76:79]
ds_read_b64_tr_b16 a[106:107], v233 offset:32448
v_bitop3_b32 v176, v176, v155, s87 bitop3:0xa8
v_mfma_f32_16x16x32_bf16 v[92:95], a[68:71], v[156:159], v[92:95]
ds_read_b64_tr_b16 a[108:109], v233 offset:27872
v_cmp_ne_u32 vcc, 0, v176
v_mfma_f32_16x16x32_bf16 v[108:111], a[72:75], v[156:159], v[108:111]
s_add_u32 s67, s67, 36864
s_cmp_eq_u32 s67, 147456
s_cselect_b32 s66, s76, 36864
s_cselect_b32 s67, 0, s67
v_mfma_f32_16x16x32_bf16 v[124:127], a[76:79], v[156:159], v[124:127]
s_cmp_lg_u64 vcc, 0
v_mfma_f32_16x16x32_bf16 v[220:223], v[180:183], v[156:159], v[220:223]
ds_read_b64_tr_b16 a[110:111], v233 offset:32480
s_cbranch_scc1 SLOW44
SLOWR44:
v_mfma_f32_16x16x32_bf16 v[128:131], a[144:147], a[200:203], v[168:171]
v_exp_f32 v144, v136
v_mfma_f32_16x16x32_bf16 v[132:135], a[160:163], a[200:203], v[168:171]
v_exp_f32 v145, v137
v_mfma_f32_16x16x32_bf16 v[128:131], a[148:151], a[216:219], v[128:131]
v_cvt_pk_bf16_f32 v156, v144, v145
v_mfma_f32_16x16x32_bf16 v[132:135], a[164:167], a[216:219], v[132:135]
v_exp_f32 v146, v138
v_mfma_f32_16x16x32_bf16 v[128:131], a[152:155], a[232:235], v[128:131]
v_exp_f32 v147, v139
v_mfma_f32_16x16x32_bf16 v[132:135], a[168:171], a[232:235], v[132:135]
s_waitcnt vmcnt(22) lgkmcnt(0)
s_barrier
v_cvt_pk_bf16_f32 v157, v146, v147
v_add_u32 v234, s66, v234
v_mfma_f32_16x16x32_bf16 v[128:131], a[156:159], a[248:251], v[128:131]
v_exp_f32 v148, v140
v_mfma_f32_16x16x32_bf16 v[132:135], a[172:175], a[248:251], v[132:135]
v_exp_f32 v149, v141
v_mfma_f32_16x16x32_bf16 v[0:3], a[80:83], v[152:155], v[0:3]
ds_read_b128 a[112:115], v234 offset:0
v_cvt_pk_bf16_f32 v158, v148, v149
v_mfma_f32_16x16x32_bf16 v[16:19], a[84:87], v[152:155], v[16:19]
v_exp_f32 v150, v142
v_mfma_f32_16x16x32_bf16 v[32:35], a[88:91], v[152:155], v[32:35]
v_exp_f32 v151, v143
v_mfma_f32_16x16x32_bf16 v[48:51], a[92:95], v[152:155], v[48:51]
v_or3_b32 v176, v156, v157, v158
v_cvt_pk_bf16_f32 v159, v150, v151
v_mfma_f32_16x16x32_bf16 v[64:67], a[96:99], v[152:155], v[64:67]
ds_read_b128 a[116:119], v234 offset:64
v_bitop3_b32 v176, v176, v159, s87 bitop3:0xa8
v_mfma_f32_16x16x32_bf16 v[80:83], a[100:103], v[152:155], v[80:83]
ds_read_b128 a[120:123], v234 offset:128
s_add_u32 m0, s70, s69
v_cmp_ne_u32 vcc, 0, v176
v_mfma_f32_16x16x32_bf16 v[96:99], a[104:107], v[152:155], v[96:99]
buffer_load_dwordx4 v240, s[40:43], s64 offen lds
v_mfma_f32_16x16x32_bf16 v[112:115], a[108:111], v[152:155], v[112:115]
ds_read_b128 a[124:127], v234 offset:192
v_add_u32 v233, s66, v233
s_cmp_lg_u64 vcc, 0
v_mfma_f32_16x16x32_bf16 v[208:211], v[180:183], v[152:155], v[208:211]
ds_read_b128 a[128:131], v234 offset:4608
s_cbranch_scc1 SLOW45
SLOWR45:
v_mfma_f32_16x16x32_bf16 v[136:139], a[144:147], a[204:207], v[172:175]
v_exp_f32 v144, v128
v_mfma_f32_16x16x32_bf16 v[140:143], a[160:163], a[204:207], v[172:175]
v_exp_f32 v145, v129
v_mfma_f32_16x16x32_bf16 v[136:139], a[148:151], a[220:223], v[136:139]
ds_read_b128 a[132:135], v234 offset:4672
v_cvt_pk_bf16_f32 v152, v144, v145
v_mfma_f32_16x16x32_bf16 v[140:143], a[164:167], a[220:223], v[140:143]
v_exp_f32 v146, v130
v_mfma_f32_16x16x32_bf16 v[136:139], a[152:155], a[236:239], v[136:139]
v_exp_f32 v147, v131
v_mfma_f32_16x16x32_bf16 v[140:143], a[168:171], a[236:239], v[140:143]
ds_read_b128 a[136:139], v234 offset:4736
v_cvt_pk_bf16_f32 v153, v146, v147
v_mfma_f32_16x16x32_bf16 v[136:139], a[156:159], a[252:255], v[136:139]
v_exp_f32 v148, v132
v_mfma_f32_16x16x32_bf16 v[140:143], a[172:175], a[252:255], v[140:143]
v_exp_f32 v149, v133
v_mfma_f32_16x16x32_bf16 v[4:7], a[80:83], v[156:159], v[4:7]
ds_read_b128 a[140:143], v234 offset:4800
v_cvt_pk_bf16_f32 v154, v148, v149
v_mfma_f32_16x16x32_bf16 v[20:23], a[84:87], v[156:159], v[20:23]
v_exp_f32 v150, v134
v_mfma_f32_16x16x32_bf16 v[36:39], a[88:91], v[156:159], v[36:39]
v_exp_f32 v151, v135
v_mfma_f32_16x16x32_bf16 v[52:55], a[92:95], v[156:159], v[52:55]
v_or3_b32 v176, v152, v153, v154
v_cvt_pk_bf16_f32 v155, v150, v151
v_mfma_f32_16x16x32_bf16 v[68:71], a[96:99], v[156:159], v[68:71]
ds_read_b64_tr_b16 a[48:49], v233 offset:18432
v_bitop3_b32 v176, v176, v155, s87 bitop3:0xa8
v_mfma_f32_16x16x32_bf16 v[84:87], a[100:103], v[156:159], v[84:87]
ds_read_b64_tr_b16 a[50:51], v233 offset:23040
s_add_u32 m0, m0, 1024
v_cmp_ne_u32 vcc, 0, v176
v_mfma_f32_16x16x32_bf16 v[100:103], a[104:107], v[156:159], v[100:103]
buffer_load_dwordx4 v241, s[40:43], s64 offen lds
v_mfma_f32_16x16x32_bf16 v[116:119], a[108:111], v[156:159], v[116:119]
buffer_load_dwordx2 v[250:251], v252, s[52:55], s92 offen offset:0 sc1
s_cmp_lg_u64 vcc, 0
v_mfma_f32_16x16x32_bf16 v[212:215], v[180:183], v[156:159], v[212:215]
ds_read_b64_tr_b16 a[52:53], v233 offset:18464
s_cbranch_scc1 SLOW46
SLOWR46:
s_waitcnt lgkmcnt(3)
v_mfma_f32_16x16x32_bf16 v[128:131], a[112:115], a[192:195], v[160:163]
v_exp_f32 v144, v136
v_mfma_f32_16x16x32_bf16 v[132:135], a[128:131], a[192:195], v[160:163]
v_exp_f32 v145, v137
v_mfma_f32_16x16x32_bf16 v[128:131], a[116:119], a[208:211], v[128:131]
ds_read_b64_tr_b16 a[54:55], v233 offset:23072
v_cvt_pk_bf16_f32 v156, v144, v145
v_mfma_f32_16x16x32_bf16 v[132:135], a[132:135], a[208:211], v[132:135]
v_exp_f32 v146, v138
v_mfma_f32_16x16x32_bf16 v[128:131], a[120:123], a[224:227], v[128:131]
v_exp_f32 v147, v139
v_mfma_f32_16x16x32_bf16 v[132:135], a[136:139], a[224:227], v[132:135]
ds_read_b64_tr_b16 a[56:57], v233 offset:18496
v_cvt_pk_bf16_f32 v157, v146, v147
v_mfma_f32_16x16x32_bf16 v[128:131], a[124:127], a[240:243], v[128:131]
)"
R"(
v_exp_f32 v148, v140
v_mfma_f32_16x16x32_bf16 v[132:135], a[140:143], a[240:243], v[132:135]
v_exp_f32 v149, v141
v_mfma_f32_16x16x32_bf16 v[8:11], a[80:83], v[152:155], v[8:11]
ds_read_b64_tr_b16 a[58:59], v233 offset:23104
v_cvt_pk_bf16_f32 v158, v148, v149
v_mfma_f32_16x16x32_bf16 v[24:27], a[84:87], v[152:155], v[24:27]
v_exp_f32 v150, v142
v_mfma_f32_16x16x32_bf16 v[40:43], a[88:91], v[152:155], v[40:43]
v_exp_f32 v151, v143
v_mfma_f32_16x16x32_bf16 v[56:59], a[92:95], v[152:155], v[56:59]
v_or3_b32 v176, v156, v157, v158
v_cvt_pk_bf16_f32 v159, v150, v151
v_mfma_f32_16x16x32_bf16 v[72:75], a[96:99], v[152:155], v[72:75]
ds_read_b64_tr_b16 a[60:61], v233 offset:18528
v_bitop3_b32 v176, v176, v159, s87 bitop3:0xa8
v_mfma_f32_16x16x32_bf16 v[88:91], a[100:103], v[152:155], v[88:91]
ds_read_b64_tr_b16 a[62:63], v233 offset:23136
s_add_u32 m0, m0, 1024
v_cmp_ne_u32 vcc, 0, v176
v_mfma_f32_16x16x32_bf16 v[104:107], a[104:107], v[152:155], v[104:107]
buffer_load_dwordx4 v242, s[40:43], s64 offen lds
s_add_u32 m0, m0, 1024
v_mfma_f32_16x16x32_bf16 v[120:123], a[108:111], v[152:155], v[120:123]
buffer_load_dwordx4 v243, s[40:43], s64 offen lds
s_cmp_lg_u64 vcc, 0
v_mfma_f32_16x16x32_bf16 v[216:219], v[180:183], v[152:155], v[216:219]
ds_read_b64_tr_b16 a[64:65], v233 offset:18560
s_cbranch_scc1 SLOW47
SLOWR47:
v_mfma_f32_16x16x32_bf16 v[136:139], a[112:115], a[196:199], v[164:167]
v_exp_f32 v144, v128
v_mfma_f32_16x16x32_bf16 v[140:143], a[128:131], a[196:199], v[164:167]
v_exp_f32 v145, v129
s_waitcnt lgkmcnt(3)
v_mfma_f32_16x16x32_bf16 v[136:139], a[116:119], a[212:215], v[136:139]
ds_read_b64_tr_b16 a[66:67], v233 offset:23168
v_cvt_pk_bf16_f32 v152, v144, v145
v_mfma_f32_16x16x32_bf16 v[140:143], a[132:135], a[212:215], v[140:143]
v_exp_f32 v146, v130
v_mfma_f32_16x16x32_bf16 v[136:139], a[120:123], a[228:231], v[136:139]
v_exp_f32 v147, v131
v_mfma_f32_16x16x32_bf16 v[140:143], a[136:139], a[228:231], v[140:143]
ds_read_b64_tr_b16 a[68:69], v233 offset:18592
v_cvt_pk_bf16_f32 v153, v146, v147
v_mfma_f32_16x16x32_bf16 v[136:139], a[124:127], a[244:247], v[136:139]
v_exp_f32 v148, v132
v_mfma_f32_16x16x32_bf16 v[140:143], a[140:143], a[244:247], v[140:143]
v_exp_f32 v149, v133
v_mfma_f32_16x16x32_bf16 v[12:15], a[80:83], v[156:159], v[12:15]
ds_read_b64_tr_b16 a[70:71], v233 offset:23200
v_cvt_pk_bf16_f32 v154, v148, v149
v_mfma_f32_16x16x32_bf16 v[28:31], a[84:87], v[156:159], v[28:31]
v_exp_f32 v150, v134
v_mfma_f32_16x16x32_bf16 v[44:47], a[88:91], v[156:159], v[44:47]
v_exp_f32 v151, v135
v_mfma_f32_16x16x32_bf16 v[60:63], a[92:95], v[156:159], v[60:63]
v_or3_b32 v176, v152, v153, v154
v_cvt_pk_bf16_f32 v155, v150, v151
v_mfma_f32_16x16x32_bf16 v[76:79], a[96:99], v[156:159], v[76:79]
ds_read_b64_tr_b16 a[72:73], v233 offset:18624
v_bitop3_b32 v176, v176, v155, s87 bitop3:0xa8
v_mfma_f32_16x16x32_bf16 v[92:95], a[100:103], v[156:159], v[92:95]
ds_read_b64_tr_b16 a[74:75], v233 offset:23232
s_add_u32 m0, m0, 1024
v_cmp_ne_u32 vcc, 0, v176
v_mfma_f32_16x16x32_bf16 v[108:111], a[104:107], v[156:159], v[108:111]
buffer_load_dwordx4 v244, s[40:43], s64 offen lds
s_add_u32 m0, m0, 1024
v_mfma_f32_16x16x32_bf16 v[124:127], a[108:111], v[156:159], v[124:127]
buffer_load_dwordx4 v245, s[40:43], s64 offen lds
s_cmp_lg_u64 vcc, 0
v_mfma_f32_16x16x32_bf16 v[220:223], v[180:183], v[156:159], v[220:223]
ds_read_b64_tr_b16 a[76:77], v233 offset:18656
s_cbranch_scc1 SLOW48
SLOWR48:
v_mfma_f32_16x16x32_bf16 v[128:131], a[112:115], a[200:203], v[168:171]
v_exp_f32 v144, v136
v_mfma_f32_16x16x32_bf16 v[132:135], a[128:131], a[200:203], v[168:171]
v_exp_f32 v145, v137
v_mfma_f32_16x16x32_bf16 v[128:131], a[116:119], a[216:219], v[128:131]
ds_read_b64_tr_b16 a[78:79], v233 offset:23264
v_cvt_pk_bf16_f32 v156, v144, v145
v_mfma_f32_16x16x32_bf16 v[132:135], a[132:135], a[216:219], v[132:135]
v_exp_f32 v146, v138
v_mfma_f32_16x16x32_bf16 v[128:131], a[120:123], a[232:235], v[128:131]
v_exp_f32 v147, v139
v_mfma_f32_16x16x32_bf16 v[132:135], a[136:139], a[232:235], v[132:135]
ds_read_b128 a[144:147], v234 offset:9216
v_cvt_pk_bf16_f32 v157, v146, v147
v_mfma_f32_16x16x32_bf16 v[128:131], a[124:127], a[248:251], v[128:131]
v_exp_f32 v148, v140
s_cmp_lt_u32 s34, 4
s_mul_i32 s88, s34, s93
s_cselect_b32 s78, 0, 16
s_cselect_b32 s46, 0, 147456
s_sub_u32 s88, s88, s78
s_lshl_b32 s88, s88, 12
s_add_u32 s44, s48, s88
s_addc_u32 s45, s49, 0
s_mov_b32 s71, 0
v_mfma_f32_16x16x32_bf16 v[132:135], a[140:143], a[248:251], v[132:135]
v_exp_f32 v149, v141
s_waitcnt lgkmcnt(1)
v_mfma_f32_16x16x32_bf16 v[0:3], a[48:51], v[152:155], v[0:3]
ds_read_b128 a[148:151], v234 offset:9280
v_cvt_pk_bf16_f32 v158, v148, v149
v_mfma_f32_16x16x32_bf16 v[16:19], a[52:55], v[152:155], v[16:19]
v_exp_f32 v150, v142
v_mfma_f32_16x16x32_bf16 v[32:35], a[56:59], v[152:155], v[32:35]
v_exp_f32 v151, v143
v_mfma_f32_16x16x32_bf16 v[48:51], a[60:63], v[152:155], v[48:51]
v_or3_b32 v176, v156, v157, v158
v_cvt_pk_bf16_f32 v159, v150, v151
v_mfma_f32_16x16x32_bf16 v[64:67], a[64:67], v[152:155], v[64:67]
ds_read_b128 a[152:155], v234 offset:9344
v_bitop3_b32 v176, v176, v159, s87 bitop3:0xa8
v_mfma_f32_16x16x32_bf16 v[80:83], a[68:71], v[152:155], v[80:83]
ds_read_b128 a[156:159], v234 offset:9408
s_add_u32 m0, m0, 1024
v_cmp_ne_u32 vcc, 0, v176
v_mfma_f32_16x16x32_bf16 v[96:99], a[72:75], v[152:155], v[96:99]
buffer_load_dwordx4 v246, s[40:43], s64 offen lds
s_add_u32 m0, m0, 1024
v_mfma_f32_16x16x32_bf16 v[112:115], a[76:79], v[152:155], v[112:115]
buffer_load_dwordx4 v247, s[40:43], s64 offen lds
s_cmp_lg_u64 vcc, 0
v_mfma_f32_16x16x32_bf16 v[208:211], v[180:183], v[152:155], v[208:211]
ds_read_b128 a[160:163], v234 offset:13824
s_cbranch_scc1 SLOW49
SLOWR49:
v_mfma_f32_16x16x32_bf16 v[136:139], a[112:115], a[204:207], v[172:175]
v_exp_f32 v144, v128
v_mfma_f32_16x16x32_bf16 v[140:143], a[128:131], a[204:207], v[172:175]
v_exp_f32 v145, v129
v_mfma_f32_16x16x32_bf16 v[136:139], a[116:119], a[220:223], v[136:139]
ds_read_b128 a[164:167], v234 offset:13888
v_cvt_pk_bf16_f32 v152, v144, v145
v_mfma_f32_16x16x32_bf16 v[140:143], a[132:135], a[220:223], v[140:143]
v_exp_f32 v146, v130
v_mfma_f32_16x16x32_bf16 v[136:139], a[120:123], a[236:239], v[136:139]
v_exp_f32 v147, v131
v_mfma_f32_16x16x32_bf16 v[140:143], a[136:139], a[236:239], v[140:143]
ds_read_b128 a[168:171], v234 offset:13952
v_cvt_pk_bf16_f32 v153, v146, v147
s_waitcnt vmcnt(6)
v_cmp_ne_u64 vcc, s[82:83], v[250:251]
v_mfma_f32_16x16x32_bf16 v[136:139], a[124:127], a[252:255], v[136:139]
v_exp_f32 v148, v132
s_cmp_lg_u64 vcc, 0
v_mfma_f32_16x16x32_bf16 v[140:143], a[140:143], a[252:255], v[140:143]
v_exp_f32 v149, v133
s_cbranch_scc1 CK_H
CK_H_R:
v_mfma_f32_16x16x32_bf16 v[4:7], a[48:51], v[156:159], v[4:7]
ds_read_b128 a[172:175], v234 offset:14016
v_cvt_pk_bf16_f32 v154, v148, v149
v_mfma_f32_16x16x32_bf16 v[20:23], a[52:55], v[156:159], v[20:23]
v_exp_f32 v150, v134
v_mfma_f32_16x16x32_bf16 v[36:39], a[56:59], v[156:159], v[36:39]
v_exp_f32 v151, v135
v_mfma_f32_16x16x32_bf16 v[52:55], a[60:63], v[156:159], v[52:55]
v_or3_b32 v176, v152, v153, v154
v_cvt_pk_bf16_f32 v155, v150, v151
v_mfma_f32_16x16x32_bf16 v[68:71], a[64:67], v[156:159], v[68:71]
ds_read_b64_tr_b16 a[80:81], v233 offset:27648
v_bitop3_b32 v176, v176, v155, s87 bitop3:0xa8
v_mfma_f32_16x16x32_bf16 v[84:87], a[68:71], v[156:159], v[84:87]
ds_read_b64_tr_b16 a[82:83], v233 offset:32256
s_add_u32 m0, m0, 1024
v_cmp_ne_u32 vcc, 0, v176
v_mfma_f32_16x16x32_bf16 v[100:103], a[72:75], v[156:159], v[100:103]
buffer_load_dwordx4 v248, s[40:43], s64 offen lds
s_add_u32 s64, s64, s65
s_add_u32 s69, s69, 36864
s_cmp_eq_u32 s69, 147456
s_cselect_b32 s69, 0, s69
v_mfma_f32_16x16x32_bf16 v[116:119], a[76:79], v[156:159], v[116:119]
s_cmp_lg_u64 vcc, 0
v_mfma_f32_16x16x32_bf16 v[212:215], v[180:183], v[156:159], v[212:215]
ds_read_b64_tr_b16 a[84:85], v233 offset:27680
ds_read_b64_tr_b16 a[86:87], v233 offset:32288
s_cbranch_scc1 SLOW50
SLOWR50:
s_waitcnt lgkmcnt(4)
v_mfma_f32_16x16x32_bf16 v[128:131], a[144:147], a[192:195], v[160:163]
v_exp_f32 v144, v136
v_mfma_f32_16x16x32_bf16 v[132:135], a[160:163], a[192:195], v[160:163]
v_exp_f32 v145, v137
v_mfma_f32_16x16x32_bf16 v[128:131], a[148:151], a[208:211], v[128:131]
ds_read_b64_tr_b16 a[88:89], v233 offset:27712
v_cvt_pk_bf16_f32 v156, v144, v145
v_mfma_f32_16x16x32_bf16 v[132:135], a[164:167], a[208:211], v[132:135]
v_exp_f32 v146, v138
v_mfma_f32_16x16x32_bf16 v[128:131], a[152:155], a[224:227], v[128:131]
v_exp_f32 v147, v139
v_mfma_f32_16x16x32_bf16 v[132:135], a[168:171], a[224:227], v[132:135]
ds_read_b64_tr_b16 a[90:91], v233 offset:32320
v_cvt_pk_bf16_f32 v157, v146, v147
v_mfma_f32_16x16x32_bf16 v[128:131], a[156:159], a[240:243], v[128:131]
v_exp_f32 v148, v140
v_mfma_f32_16x16x32_bf16 v[132:135], a[172:175], a[240:243], v[132:135]
v_exp_f32 v149, v141
v_mfma_f32_16x16x32_bf16 v[8:11], a[48:51], v[152:155], v[8:11]
ds_read_b64_tr_b16 a[92:93], v233 offset:27744
v_cvt_pk_bf16_f32 v158, v148, v149
v_mfma_f32_16x16x32_bf16 v[24:27], a[52:55], v[152:155], v[24:27]
v_exp_f32 v150, v142
v_mfma_f32_16x16x32_bf16 v[40:43], a[56:59], v[152:155], v[40:43]
v_exp_f32 v151, v143
v_mfma_f32_16x16x32_bf16 v[56:59], a[60:63], v[152:155], v[56:59]
v_or3_b32 v176, v156, v157, v158
v_cvt_pk_bf16_f32 v159, v150, v151
v_mfma_f32_16x16x32_bf16 v[72:75], a[64:67], v[152:155], v[72:75]
ds_read_b64_tr_b16 a[94:95], v233 offset:32352
v_bitop3_b32 v176, v176, v159, s87 bitop3:0xa8
v_mfma_f32_16x16x32_bf16 v[88:91], a[68:71], v[152:155], v[88:91]
ds_read_b64_tr_b16 a[96:97], v233 offset:27776
v_cmp_ne_u32 vcc, 0, v176
v_mfma_f32_16x16x32_bf16 v[104:107], a[72:75], v[152:155], v[104:107]
v_mfma_f32_16x16x32_bf16 v[120:123], a[76:79], v[152:155], v[120:123]
buffer_load_dwordx4 a[0:3], v235, s[44:47], s71 offen offset:0 sc1
s_cmp_lg_u64 vcc, 0
v_mfma_f32_16x16x32_bf16 v[216:219], v[180:183], v[152:155], v[216:219]
ds_read_b64_tr_b16 a[98:99], v233 offset:32384
s_cbranch_scc1 SLOW51
SLOWR51:
v_mfma_f32_16x16x32_bf16 v[136:139], a[144:147], a[196:199], v[164:167]
v_exp_f32 v144, v128
v_mfma_f32_16x16x32_bf16 v[140:143], a[160:163], a[196:199], v[164:167]
v_exp_f32 v145, v129
s_waitcnt lgkmcnt(3)
v_mfma_f32_16x16x32_bf16 v[136:139], a[148:151], a[212:215], v[136:139]
ds_read_b64_tr_b16 a[100:101], v233 offset:27808
v_cvt_pk_bf16_f32 v152, v144, v145
v_mfma_f32_16x16x32_bf16 v[140:143], a[164:167], a[212:215], v[140:143]
v_exp_f32 v146, v130
v_mfma_f32_16x16x32_bf16 v[136:139], a[152:155], a[228:231], v[136:139]
v_exp_f32 v147, v131
v_mfma_f32_16x16x32_bf16 v[140:143], a[168:171], a[228:231], v[140:143]
ds_read_b64_tr_b16 a[102:103], v233 offset:32416
v_cvt_pk_bf16_f32 v153, v146, v147
v_mfma_f32_16x16x32_bf16 v[136:139], a[156:159], a[244:247], v[136:139]
v_exp_f32 v148, v132
v_mfma_f32_16x16x32_bf16 v[140:143], a[172:175], a[244:247], v[140:143]
v_exp_f32 v149, v133
v_mfma_f32_16x16x32_bf16 v[12:15], a[48:51], v[156:159], v[12:15]
ds_read_b64_tr_b16 a[104:105], v233 offset:27840
v_cvt_pk_bf16_f32 v154, v148, v149
v_mfma_f32_16x16x32_bf16 v[28:31], a[52:55], v[156:159], v[28:31]
v_exp_f32 v150, v134
v_mfma_f32_16x16x32_bf16 v[44:47], a[56:59], v[156:159], v[44:47]
v_exp_f32 v151, v135
v_mfma_f32_16x16x32_bf16 v[60:63], a[60:63], v[156:159], v[60:63]
v_or3_b32 v176, v152, v153, v154
v_cvt_pk_bf16_f32 v155, v150, v151
v_mfma_f32_16x16x32_bf16 v[76:79], a[64:67], v[156:159], v[76:79]
ds_read_b64_tr_b16 a[106:107], v233 offset:32448
v_bitop3_b32 v176, v176, v155, s87 bitop3:0xa8
v_mfma_f32_16x16x32_bf16 v[92:95], a[68:71], v[156:159], v[92:95]
ds_read_b64_tr_b16 a[108:109], v233 offset:27872
v_cmp_ne_u32 vcc, 0, v176
v_mfma_f32_16x16x32_bf16 v[108:111], a[72:75], v[156:159], v[108:111]
s_add_u32 s67, s67, 36864
s_cmp_eq_u32 s67, 147456
s_cselect_b32 s66, s76, 36864
s_cselect_b32 s67, 0, s67
v_mfma_f32_16x16x32_bf16 v[124:127], a[76:79], v[156:159], v[124:127]
buffer_load_dwordx4 a[4:7], v235, s[44:47], s71 offen offset:1024 sc1
s_cmp_lg_u64 vcc, 0
v_mfma_f32_16x16x32_bf16 v[220:223], v[180:183], v[156:159], v[220:223]
ds_read_b64_tr_b16 a[110:111], v233 offset:32480
s_cbranch_scc1 SLOW52
SLOWR52:
v_mfma_f32_16x16x32_bf16 v[128:131], a[144:147], a[200:203], v[168:171]
v_exp_f32 v144, v136
v_mfma_f32_16x16x32_bf16 v[132:135], a[160:163], a[200:203], v[168:171]
v_exp_f32 v145, v137
v_mfma_f32_16x16x32_bf16 v[128:131], a[148:151], a[216:219], v[128:131]
v_cvt_pk_bf16_f32 v156, v144, v145
v_mfma_f32_16x16x32_bf16 v[132:135], a[164:167], a[216:219], v[132:135]
v_exp_f32 v146, v138
v_mfma_f32_16x16x32_bf16 v[128:131], a[152:155], a[232:235], v[128:131]
v_exp_f32 v147, v139
v_mfma_f32_16x16x32_bf16 v[132:135], a[168:171], a[232:235], v[132:135]
s_waitcnt vmcnt(21) lgkmcnt(0)
s_barrier
v_cvt_pk_bf16_f32 v157, v146, v147
v_add_u32 v234, s66, v234
v_mfma_f32_16x16x32_bf16 v[128:131], a[156:159], a[248:251], v[128:131]
v_exp_f32 v148, v140
v_mfma_f32_16x16x32_bf16 v[132:135], a[172:175], a[248:251], v[132:135]
v_exp_f32 v149, v141
v_mfma_f32_16x16x32_bf16 v[0:3], a[80:83], v[152:155], v[0:3]
ds_read_b128 a[112:115], v234 offset:0
v_cvt_pk_bf16_f32 v158, v148, v149
v_mfma_f32_16x16x32_bf16 v[16:19], a[84:87], v[152:155], v[16:19]
v_exp_f32 v150, v142
v_mfma_f32_16x16x32_bf16 v[32:35], a[88:91], v[152:155], v[32:35]
v_exp_f32 v151, v143
v_mfma_f32_16x16x32_bf16 v[48:51], a[92:95], v[152:155], v[48:51]
v_or3_b32 v176, v156, v157, v158
v_cvt_pk_bf16_f32 v159, v150, v151
v_mfma_f32_16x16x32_bf16 v[64:67], a[96:99], v[152:155], v[64:67]
ds_read_b128 a[116:119], v234 offset:64
v_bitop3_b32 v176, v176, v159, s87 bitop3:0xa8
v_mfma_f32_16x16x32_bf16 v[80:83], a[100:103], v[152:155], v[80:83]
ds_read_b128 a[120:123], v234 offset:128
s_add_u32 m0, s70, s69
v_cmp_ne_u32 vcc, 0, v176
v_mfma_f32_16x16x32_bf16 v[96:99], a[104:107], v[152:155], v[96:99]
buffer_load_dwordx4 v240, s[40:43], s64 offen lds
v_mfma_f32_16x16x32_bf16 v[112:115], a[108:111], v[152:155], v[112:115]
ds_read_b128 a[124:127], v234 offset:192
v_add_u32 v233, s66, v233
s_cmp_lg_u64 vcc, 0
v_mfma_f32_16x16x32_bf16 v[208:211], v[180:183], v[152:155], v[208:211]
ds_read_b128 a[128:131], v234 offset:4608
s_cbranch_scc1 SLOW53
SLOWR53:
v_mfma_f32_16x16x32_bf16 v[136:139], a[144:147], a[204:207], v[172:175]
v_exp_f32 v144, v128
v_mfma_f32_16x16x32_bf16 v[140:143], a[160:163], a[204:207], v[172:175]
v_exp_f32 v145, v129
v_mfma_f32_16x16x32_bf16 v[136:139], a[148:151], a[220:223], v[136:139]
ds_read_b128 a[132:135], v234 offset:4672
v_cvt_pk_bf16_f32 v152, v144, v145
v_mfma_f32_16x16x32_bf16 v[140:143], a[164:167], a[220:223], v[140:143]
v_exp_f32 v146, v130
v_mfma_f32_16x16x32_bf16 v[136:139], a[152:155], a[236:239], v[136:139]
v_exp_f32 v147, v131
v_mfma_f32_16x16x32_bf16 v[140:143], a[168:171], a[236:239], v[140:143]
ds_read_b128 a[136:139], v234 offset:4736
v_cvt_pk_bf16_f32 v153, v146, v147
v_mfma_f32_16x16x32_bf16 v[136:139], a[156:159], a[252:255], v[136:139]
v_exp_f32 v148, v132
v_mfma_f32_16x16x32_bf16 v[140:143], a[172:175], a[252:255], v[140:143]
v_exp_f32 v149, v133
v_mfma_f32_16x16x32_bf16 v[4:7], a[80:83], v[156:159], v[4:7]
ds_read_b128 a[140:143], v234 offset:4800
v_cvt_pk_bf16_f32 v154, v148, v149
v_mfma_f32_16x16x32_bf16 v[20:23], a[84:87], v[156:159], v[20:23]
v_exp_f32 v150, v134
v_mfma_f32_16x16x32_bf16 v[36:39], a[88:91], v[156:159], v[36:39]
v_exp_f32 v151, v135
v_mfma_f32_16x16x32_bf16 v[52:55], a[92:95], v[156:159], v[52:55]
v_or3_b32 v176, v152, v153, v154
v_cvt_pk_bf16_f32 v155, v150, v151
v_mfma_f32_16x16x32_bf16 v[68:71], a[96:99], v[156:159], v[68:71]
ds_read_b64_tr_b16 a[48:49], v233 offset:18432
v_bitop3_b32 v176, v176, v155, s87 bitop3:0xa8
v_mfma_f32_16x16x32_bf16 v[84:87], a[100:103], v[156:159], v[84:87]
ds_read_b64_tr_b16 a[50:51], v233 offset:23040
s_add_u32 m0, m0, 1024
v_cmp_ne_u32 vcc, 0, v176
v_mfma_f32_16x16x32_bf16 v[100:103], a[104:107], v[156:159], v[100:103]
buffer_load_dwordx4 v241, s[40:43], s64 offen lds
v_mfma_f32_16x16x32_bf16 v[116:119], a[108:111], v[156:159], v[116:119]
buffer_load_dwordx4 a[8:11], v235, s[44:47], s71 offen offset:2048 sc1
s_cmp_lg_u64 vcc, 0
v_mfma_f32_16x16x32_bf16 v[212:215], v[180:183], v[156:159], v[212:215]
ds_read_b64_tr_b16 a[52:53], v233 offset:18464
s_cbranch_scc1 SLOW54
SLOWR54:
s_waitcnt lgkmcnt(3)
v_mfma_f32_16x16x32_bf16 v[128:131], a[112:115], a[192:195], v[160:163]
v_exp_f32 v144, v136
v_mfma_f32_16x16x32_bf16 v[132:135], a[128:131], a[192:195], v[160:163]
v_exp_f32 v145, v137
v_mfma_f32_16x16x32_bf16 v[128:131], a[116:119], a[208:211], v[128:131]
ds_read_b64_tr_b16 a[54:55], v233 offset:23072
v_cvt_pk_bf16_f32 v156, v144, v145
v_mfma_f32_16x16x32_bf16 v[132:135], a[132:135], a[208:211], v[132:135]
v_exp_f32 v146, v138
v_mfma_f32_16x16x32_bf16 v[128:131], a[120:123], a[224:227], v[128:131]
v_exp_f32 v147, v139
v_mfma_f32_16x16x32_bf16 v[132:135], a[136:139], a[224:227], v[132:135]
ds_read_b64_tr_b16 a[56:57], v233 offset:18496
v_cvt_pk_bf16_f32 v157, v146, v147
v_mfma_f32_16x16x32_bf16 v[128:131], a[124:127], a[240:243], v[128:131]
v_exp_f32 v148, v140
v_mfma_f32_16x16x32_bf16 v[132:135], a[140:143], a[240:243], v[132:135]
v_exp_f32 v149, v141
v_mfma_f32_16x16x32_bf16 v[8:11], a[80:83], v[152:155], v[8:11]
ds_read_b64_tr_b16 a[58:59], v233 offset:23104
v_cvt_pk_bf16_f32 v158, v148, v149
v_mfma_f32_16x16x32_bf16 v[24:27], a[84:87], v[152:155], v[24:27]
v_exp_f32 v150, v142
v_mfma_f32_16x16x32_bf16 v[40:43], a[88:91], v[152:155], v[40:43]
v_exp_f32 v151, v143
v_mfma_f32_16x16x32_bf16 v[56:59], a[92:95], v[152:155], v[56:59]
v_or3_b32 v176, v156, v157, v158
v_cvt_pk_bf16_f32 v159, v150, v151
v_mfma_f32_16x16x32_bf16 v[72:75], a[96:99], v[152:155], v[72:75]
ds_read_b64_tr_b16 a[60:61], v233 offset:18528
v_bitop3_b32 v176, v176, v159, s87 bitop3:0xa8
v_mfma_f32_16x16x32_bf16 v[88:91], a[100:103], v[152:155], v[88:91]
ds_read_b64_tr_b16 a[62:63], v233 offset:23136
s_add_u32 m0, m0, 1024
v_cmp_ne_u32 vcc, 0, v176
)"
R"(
v_mfma_f32_16x16x32_bf16 v[104:107], a[104:107], v[152:155], v[104:107]
buffer_load_dwordx4 v242, s[40:43], s64 offen lds
s_add_u32 m0, m0, 1024
v_mfma_f32_16x16x32_bf16 v[120:123], a[108:111], v[152:155], v[120:123]
buffer_load_dwordx4 v243, s[40:43], s64 offen lds
s_cmp_lg_u64 vcc, 0
v_mfma_f32_16x16x32_bf16 v[216:219], v[180:183], v[152:155], v[216:219]
ds_read_b64_tr_b16 a[64:65], v233 offset:18560
s_cbranch_scc1 SLOW55
SLOWR55:
v_mfma_f32_16x16x32_bf16 v[136:139], a[112:115], a[196:199], v[164:167]
v_exp_f32 v144, v128
v_mfma_f32_16x16x32_bf16 v[140:143], a[128:131], a[196:199], v[164:167]
v_exp_f32 v145, v129
s_waitcnt lgkmcnt(3)
v_mfma_f32_16x16x32_bf16 v[136:139], a[116:119], a[212:215], v[136:139]
ds_read_b64_tr_b16 a[66:67], v233 offset:23168
v_cvt_pk_bf16_f32 v152, v144, v145
v_mfma_f32_16x16x32_bf16 v[140:143], a[132:135], a[212:215], v[140:143]
v_exp_f32 v146, v130
v_mfma_f32_16x16x32_bf16 v[136:139], a[120:123], a[228:231], v[136:139]
v_exp_f32 v147, v131
v_mfma_f32_16x16x32_bf16 v[140:143], a[136:139], a[228:231], v[140:143]
ds_read_b64_tr_b16 a[68:69], v233 offset:18592
v_cvt_pk_bf16_f32 v153, v146, v147
v_mfma_f32_16x16x32_bf16 v[136:139], a[124:127], a[244:247], v[136:139]
v_exp_f32 v148, v132
v_mfma_f32_16x16x32_bf16 v[140:143], a[140:143], a[244:247], v[140:143]
v_exp_f32 v149, v133
v_mfma_f32_16x16x32_bf16 v[12:15], a[80:83], v[156:159], v[12:15]
ds_read_b64_tr_b16 a[70:71], v233 offset:23200
v_cvt_pk_bf16_f32 v154, v148, v149
v_mfma_f32_16x16x32_bf16 v[28:31], a[84:87], v[156:159], v[28:31]
v_exp_f32 v150, v134
v_mfma_f32_16x16x32_bf16 v[44:47], a[88:91], v[156:159], v[44:47]
v_exp_f32 v151, v135
v_mfma_f32_16x16x32_bf16 v[60:63], a[92:95], v[156:159], v[60:63]
v_or3_b32 v176, v152, v153, v154
v_cvt_pk_bf16_f32 v155, v150, v151
v_mfma_f32_16x16x32_bf16 v[76:79], a[96:99], v[156:159], v[76:79]
ds_read_b64_tr_b16 a[72:73], v233 offset:18624
v_bitop3_b32 v176, v176, v155, s87 bitop3:0xa8
v_mfma_f32_16x16x32_bf16 v[92:95], a[100:103], v[156:159], v[92:95]
ds_read_b64_tr_b16 a[74:75], v233 offset:23232
s_add_u32 m0, m0, 1024
v_cmp_ne_u32 vcc, 0, v176
v_mfma_f32_16x16x32_bf16 v[108:111], a[104:107], v[156:159], v[108:111]
buffer_load_dwordx4 v244, s[40:43], s64 offen lds
v_mfma_f32_16x16x32_bf16 v[124:127], a[108:111], v[156:159], v[124:127]
buffer_load_dwordx4 a[12:15], v235, s[44:47], s71 offen offset:3072 sc1
s_add_u32 s71, s71, 4096
s_cmp_lg_u64 vcc, 0
v_mfma_f32_16x16x32_bf16 v[220:223], v[180:183], v[156:159], v[220:223]
ds_read_b64_tr_b16 a[76:77], v233 offset:18656
s_cbranch_scc1 SLOW56
SLOWR56:
v_mfma_f32_16x16x32_bf16 v[128:131], a[112:115], a[200:203], v[168:171]
v_exp_f32 v144, v136
v_mfma_f32_16x16x32_bf16 v[132:135], a[128:131], a[200:203], v[168:171]
v_exp_f32 v145, v137
v_mfma_f32_16x16x32_bf16 v[128:131], a[116:119], a[216:219], v[128:131]
ds_read_b64_tr_b16 a[78:79], v233 offset:23264
v_cvt_pk_bf16_f32 v156, v144, v145
v_mfma_f32_16x16x32_bf16 v[132:135], a[132:135], a[216:219], v[132:135]
v_exp_f32 v146, v138
v_mfma_f32_16x16x32_bf16 v[128:131], a[120:123], a[232:235], v[128:131]
v_exp_f32 v147, v139
v_mfma_f32_16x16x32_bf16 v[132:135], a[136:139], a[232:235], v[132:135]
ds_read_b128 a[144:147], v234 offset:9216
v_cvt_pk_bf16_f32 v157, v146, v147
v_mfma_f32_16x16x32_bf16 v[128:131], a[124:127], a[248:251], v[128:131]
v_exp_f32 v148, v140
v_mfma_f32_16x16x32_bf16 v[132:135], a[140:143], a[248:251], v[132:135]
v_exp_f32 v149, v141
s_waitcnt lgkmcnt(1)
v_mfma_f32_16x16x32_bf16 v[0:3], a[48:51], v[152:155], v[0:3]
ds_read_b128 a[148:151], v234 offset:9280
v_cvt_pk_bf16_f32 v158, v148, v149
v_mfma_f32_16x16x32_bf16 v[16:19], a[52:55], v[152:155], v[16:19]
v_exp_f32 v150, v142
v_mfma_f32_16x16x32_bf16 v[32:35], a[56:59], v[152:155], v[32:35]
v_exp_f32 v151, v143
v_mfma_f32_16x16x32_bf16 v[48:51], a[60:63], v[152:155], v[48:51]
v_or3_b32 v176, v156, v157, v158
v_cvt_pk_bf16_f32 v159, v150, v151
v_mfma_f32_16x16x32_bf16 v[64:67], a[64:67], v[152:155], v[64:67]
ds_read_b128 a[152:155], v234 offset:9344
v_bitop3_b32 v176, v176, v159, s87 bitop3:0xa8
v_mfma_f32_16x16x32_bf16 v[80:83], a[68:71], v[152:155], v[80:83]
ds_read_b128 a[156:159], v234 offset:9408
s_add_u32 m0, m0, 1024
v_cmp_ne_u32 vcc, 0, v176
v_mfma_f32_16x16x32_bf16 v[96:99], a[72:75], v[152:155], v[96:99]
buffer_load_dwordx4 v245, s[40:43], s64 offen lds
v_mfma_f32_16x16x32_bf16 v[112:115], a[76:79], v[152:155], v[112:115]
buffer_load_dwordx4 a[16:19], v235, s[44:47], s71 offen offset:0 sc1
s_cmp_lg_u64 vcc, 0
v_mfma_f32_16x16x32_bf16 v[208:211], v[180:183], v[152:155], v[208:211]
ds_read_b128 a[160:163], v234 offset:13824
s_cbranch_scc1 SLOW57
SLOWR57:
v_mfma_f32_16x16x32_bf16 v[136:139], a[112:115], a[204:207], v[172:175]
v_exp_f32 v144, v128
v_mfma_f32_16x16x32_bf16 v[140:143], a[128:131], a[204:207], v[172:175]
v_exp_f32 v145, v129
v_mfma_f32_16x16x32_bf16 v[136:139], a[116:119], a[220:223], v[136:139]
ds_read_b128 a[164:167], v234 offset:13888
v_cvt_pk_bf16_f32 v152, v144, v145
v_mfma_f32_16x16x32_bf16 v[140:143], a[132:135], a[220:223], v[140:143]
v_exp_f32 v146, v130
v_mfma_f32_16x16x32_bf16 v[136:139], a[120:123], a[236:239], v[136:139]
v_exp_f32 v147, v131
v_mfma_f32_16x16x32_bf16 v[140:143], a[136:139], a[236:239], v[140:143]
ds_read_b128 a[168:171], v234 offset:13952
v_cvt_pk_bf16_f32 v153, v146, v147
v_mfma_f32_16x16x32_bf16 v[136:139], a[124:127], a[252:255], v[136:139]
v_exp_f32 v148, v132
v_mfma_f32_16x16x32_bf16 v[140:143], a[140:143], a[252:255], v[140:143]
v_exp_f32 v149, v133
v_mfma_f32_16x16x32_bf16 v[4:7], a[48:51], v[156:159], v[4:7]
ds_read_b128 a[172:175], v234 offset:14016
v_cvt_pk_bf16_f32 v154, v148, v149
v_mfma_f32_16x16x32_bf16 v[20:23], a[52:55], v[156:159], v[20:23]
v_exp_f32 v150, v134
v_mfma_f32_16x16x32_bf16 v[36:39], a[56:59], v[156:159], v[36:39]
v_exp_f32 v151, v135
v_mfma_f32_16x16x32_bf16 v[52:55], a[60:63], v[156:159], v[52:55]
v_or3_b32 v176, v152, v153, v154
v_cvt_pk_bf16_f32 v155, v150, v151
v_mfma_f32_16x16x32_bf16 v[68:71], a[64:67], v[156:159], v[68:71]
ds_read_b64_tr_b16 a[80:81], v233 offset:27648
v_bitop3_b32 v176, v176, v155, s87 bitop3:0xa8
v_mfma_f32_16x16x32_bf16 v[84:87], a[68:71], v[156:159], v[84:87]
ds_read_b64_tr_b16 a[82:83], v233 offset:32256
s_add_u32 m0, m0, 1024
v_cmp_ne_u32 vcc, 0, v176
v_mfma_f32_16x16x32_bf16 v[100:103], a[72:75], v[156:159], v[100:103]
buffer_load_dwordx4 v246, s[40:43], s64 offen lds
v_mfma_f32_16x16x32_bf16 v[116:119], a[76:79], v[156:159], v[116:119]
buffer_load_dwordx4 a[20:23], v235, s[44:47], s71 offen offset:1024 sc1
s_cmp_lg_u64 vcc, 0
v_mfma_f32_16x16x32_bf16 v[212:215], v[180:183], v[156:159], v[212:215]
ds_read_b64_tr_b16 a[84:85], v233 offset:27680
ds_read_b64_tr_b16 a[86:87], v233 offset:32288
s_cbranch_scc1 SLOW58
SLOWR58:
s_waitcnt lgkmcnt(4)
v_mfma_f32_16x16x32_bf16 v[128:131], a[144:147], a[192:195], v[160:163]
v_exp_f32 v144, v136
v_mfma_f32_16x16x32_bf16 v[132:135], a[160:163], a[192:195], v[160:163]
v_exp_f32 v145, v137
v_mfma_f32_16x16x32_bf16 v[128:131], a[148:151], a[208:211], v[128:131]
ds_read_b64_tr_b16 a[88:89], v233 offset:27712
v_cvt_pk_bf16_f32 v156, v144, v145
v_mfma_f32_16x16x32_bf16 v[132:135], a[164:167], a[208:211], v[132:135]
v_exp_f32 v146, v138
v_mfma_f32_16x16x32_bf16 v[128:131], a[152:155], a[224:227], v[128:131]
v_exp_f32 v147, v139
v_mfma_f32_16x16x32_bf16 v[132:135], a[168:171], a[224:227], v[132:135]
ds_read_b64_tr_b16 a[90:91], v233 offset:32320
v_cvt_pk_bf16_f32 v157, v146, v147
v_mfma_f32_16x16x32_bf16 v[128:131], a[156:159], a[240:243], v[128:131]
v_exp_f32 v148, v140
v_mfma_f32_16x16x32_bf16 v[132:135], a[172:175], a[240:243], v[132:135]
v_exp_f32 v149, v141
v_mfma_f32_16x16x32_bf16 v[8:11], a[48:51], v[152:155], v[8:11]
ds_read_b64_tr_b16 a[92:93], v233 offset:27744
v_cvt_pk_bf16_f32 v158, v148, v149
v_mfma_f32_16x16x32_bf16 v[24:27], a[52:55], v[152:155], v[24:27]
v_exp_f32 v150, v142
v_mfma_f32_16x16x32_bf16 v[40:43], a[56:59], v[152:155], v[40:43]
v_exp_f32 v151, v143
v_mfma_f32_16x16x32_bf16 v[56:59], a[60:63], v[152:155], v[56:59]
v_or3_b32 v176, v156, v157, v158
v_cvt_pk_bf16_f32 v159, v150, v151
v_mfma_f32_16x16x32_bf16 v[72:75], a[64:67], v[152:155], v[72:75]
ds_read_b64_tr_b16 a[94:95], v233 offset:32352
v_bitop3_b32 v176, v176, v159, s87 bitop3:0xa8
v_mfma_f32_16x16x32_bf16 v[88:91], a[68:71], v[152:155], v[88:91]
ds_read_b64_tr_b16 a[96:97], v233 offset:27776
s_add_u32 m0, m0, 1024
v_cmp_ne_u32 vcc, 0, v176
v_mfma_f32_16x16x32_bf16 v[104:107], a[72:75], v[152:155], v[104:107]
buffer_load_dwordx4 v247, s[40:43], s64 offen lds
v_mfma_f32_16x16x32_bf16 v[120:123], a[76:79], v[152:155], v[120:123]
buffer_load_dwordx4 a[24:27], v235, s[44:47], s71 offen offset:2048 sc1
s_cmp_lg_u64 vcc, 0
v_mfma_f32_16x16x32_bf16 v[216:219], v[180:183], v[152:155], v[216:219]
ds_read_b64_tr_b16 a[98:99], v233 offset:32384
s_cbranch_scc1 SLOW59
SLOWR59:
v_mfma_f32_16x16x32_bf16 v[136:139], a[144:147], a[196:199], v[164:167]
v_exp_f32 v144, v128
v_mfma_f32_16x16x32_bf16 v[140:143], a[160:163], a[196:199], v[164:167]
v_exp_f32 v145, v129
s_waitcnt lgkmcnt(3)
v_mfma_f32_16x16x32_bf16 v[136:139], a[148:151], a[212:215], v[136:139]
ds_read_b64_tr_b16 a[100:101], v233 offset:27808
v_cvt_pk_bf16_f32 v152, v144, v145
v_mfma_f32_16x16x32_bf16 v[140:143], a[164:167], a[212:215], v[140:143]
v_exp_f32 v146, v130
v_mfma_f32_16x16x32_bf16 v[136:139], a[152:155], a[228:231], v[136:139]
v_exp_f32 v147, v131
v_mfma_f32_16x16x32_bf16 v[140:143], a[168:171], a[228:231], v[140:143]
ds_read_b64_tr_b16 a[102:103], v233 offset:32416
v_cvt_pk_bf16_f32 v153, v146, v147
v_mfma_f32_16x16x32_bf16 v[136:139], a[156:159], a[244:247], v[136:139]
v_exp_f32 v148, v132
v_mfma_f32_16x16x32_bf16 v[140:143], a[172:175], a[244:247], v[140:143]
v_exp_f32 v149, v133
v_mfma_f32_16x16x32_bf16 v[12:15], a[48:51], v[156:159], v[12:15]
ds_read_b64_tr_b16 a[104:105], v233 offset:27840
v_cvt_pk_bf16_f32 v154, v148, v149
v_mfma_f32_16x16x32_bf16 v[28:31], a[52:55], v[156:159], v[28:31]
v_exp_f32 v150, v134
v_mfma_f32_16x16x32_bf16 v[44:47], a[56:59], v[156:159], v[44:47]
v_exp_f32 v151, v135
v_mfma_f32_16x16x32_bf16 v[60:63], a[60:63], v[156:159], v[60:63]
v_or3_b32 v176, v152, v153, v154
v_cvt_pk_bf16_f32 v155, v150, v151
v_mfma_f32_16x16x32_bf16 v[76:79], a[64:67], v[156:159], v[76:79]
ds_read_b64_tr_b16 a[106:107], v233 offset:32448
v_bitop3_b32 v176, v176, v155, s87 bitop3:0xa8
v_mfma_f32_16x16x32_bf16 v[92:95], a[68:71], v[156:159], v[92:95]
ds_read_b64_tr_b16 a[108:109], v233 offset:27872
s_add_u32 m0, m0, 1024
v_cmp_ne_u32 vcc, 0, v176
v_mfma_f32_16x16x32_bf16 v[108:111], a[72:75], v[156:159], v[108:111]
buffer_load_dwordx4 v248, s[40:43], s64 offen lds
s_add_u32 s64, s64, s65
s_add_u32 s69, s69, 36864
s_cmp_eq_u32 s69, 147456
s_cselect_b32 s69, 0, s69
s_add_u32 s67, s67, 36864
s_cmp_eq_u32 s67, 147456
s_cselect_b32 s66, s76, 36864
s_cselect_b32 s67, 0, s67
v_mfma_f32_16x16x32_bf16 v[124:127], a[76:79], v[156:159], v[124:127]
buffer_load_dwordx4 a[28:31], v235, s[44:47], s71 offen offset:3072 sc1
s_add_u32 s71, s71, 4096
s_cmp_lg_u64 vcc, 0
v_mfma_f32_16x16x32_bf16 v[220:223], v[180:183], v[156:159], v[220:223]
ds_read_b64_tr_b16 a[110:111], v233 offset:32480
s_cbranch_scc1 SLOW60
SLOWR60:
v_mfma_f32_16x16x32_bf16 v[128:131], a[144:147], a[200:203], v[168:171]
v_exp_f32 v144, v136
v_mfma_f32_16x16x32_bf16 v[132:135], a[160:163], a[200:203], v[168:171]
v_exp_f32 v145, v137
v_mfma_f32_16x16x32_bf16 v[128:131], a[148:151], a[216:219], v[128:131]
v_cvt_pk_bf16_f32 v156, v144, v145
v_mfma_f32_16x16x32_bf16 v[132:135], a[164:167], a[216:219], v[132:135]
v_exp_f32 v146, v138
v_mfma_f32_16x16x32_bf16 v[128:131], a[152:155], a[232:235], v[128:131]
v_exp_f32 v147, v139
v_mfma_f32_16x16x32_bf16 v[132:135], a[168:171], a[232:235], v[132:135]
s_waitcnt vmcnt(27) lgkmcnt(0)
s_barrier
v_cvt_pk_bf16_f32 v157, v146, v147
v_add_u32 v234, s66, v234
v_mfma_f32_16x16x32_bf16 v[128:131], a[156:159], a[248:251], v[128:131]
v_exp_f32 v148, v140
v_mfma_f32_16x16x32_bf16 v[132:135], a[172:175], a[248:251], v[132:135]
s_add_u32 m0, s70, s69
v_exp_f32 v149, v141
v_mfma_f32_16x16x32_bf16 v[0:3], a[80:83], v[152:155], v[0:3]
buffer_load_dwordx4 v240, s[40:43], s64 offen lds
v_cvt_pk_bf16_f32 v158, v148, v149
v_mfma_f32_16x16x32_bf16 v[16:19], a[84:87], v[152:155], v[16:19]
v_exp_f32 v150, v142
v_mfma_f32_16x16x32_bf16 v[32:35], a[88:91], v[152:155], v[32:35]
v_exp_f32 v151, v143
v_mfma_f32_16x16x32_bf16 v[48:51], a[92:95], v[152:155], v[48:51]
s_add_u32 m0, m0, 1024
v_or3_b32 v176, v156, v157, v158
v_cvt_pk_bf16_f32 v159, v150, v151
v_mfma_f32_16x16x32_bf16 v[64:67], a[96:99], v[152:155], v[64:67]
buffer_load_dwordx4 v241, s[40:43], s64 offen lds
s_add_u32 m0, m0, 1024
v_bitop3_b32 v176, v176, v159, s87 bitop3:0xa8
v_mfma_f32_16x16x32_bf16 v[80:83], a[100:103], v[152:155], v[80:83]
buffer_load_dwordx4 v242, s[40:43], s64 offen lds
s_add_u32 m0, m0, 1024
v_cmp_ne_u32 vcc, 0, v176
v_mfma_f32_16x16x32_bf16 v[96:99], a[104:107], v[152:155], v[96:99]
buffer_load_dwordx4 v243, s[40:43], s64 offen lds
v_mfma_f32_16x16x32_bf16 v[112:115], a[108:111], v[152:155], v[112:115]
s_add_u32 m0, m0, 1024
v_add_u32 v233, s66, v233
s_cmp_lg_u64 vcc, 0
v_mfma_f32_16x16x32_bf16 v[208:211], v[180:183], v[152:155], v[208:211]
buffer_load_dwordx4 v244, s[40:43], s64 offen lds
s_cbranch_scc1 SLOW61
SLOWR61:
v_mfma_f32_16x16x32_bf16 v[136:139], a[144:147], a[204:207], v[172:175]
v_exp_f32 v144, v128
v_mfma_f32_16x16x32_bf16 v[140:143], a[160:163], a[204:207], v[172:175]
s_add_u32 m0, m0, 1024
v_exp_f32 v145, v129
v_mfma_f32_16x16x32_bf16 v[136:139], a[148:151], a[220:223], v[136:139]
buffer_load_dwordx4 v245, s[40:43], s64 offen lds
v_cvt_pk_bf16_f32 v152, v144, v145
v_mfma_f32_16x16x32_bf16 v[140:143], a[164:167], a[220:223], v[140:143]
v_exp_f32 v146, v130
v_mfma_f32_16x16x32_bf16 v[136:139], a[152:155], a[236:239], v[136:139]
s_add_u32 m0, m0, 1024
v_exp_f32 v147, v131
v_mfma_f32_16x16x32_bf16 v[140:143], a[168:171], a[236:239], v[140:143]
buffer_load_dwordx4 v246, s[40:43], s64 offen lds
v_cvt_pk_bf16_f32 v153, v146, v147
v_mfma_f32_16x16x32_bf16 v[136:139], a[156:159], a[252:255], v[136:139]
v_exp_f32 v148, v132
v_mfma_f32_16x16x32_bf16 v[140:143], a[172:175], a[252:255], v[140:143]
s_add_u32 m0, m0, 1024
v_exp_f32 v149, v133
v_mfma_f32_16x16x32_bf16 v[4:7], a[80:83], v[156:159], v[4:7]
buffer_load_dwordx4 v247, s[40:43], s64 offen lds
v_cvt_pk_bf16_f32 v154, v148, v149
v_mfma_f32_16x16x32_bf16 v[20:23], a[84:87], v[156:159], v[20:23]
v_exp_f32 v150, v134
v_mfma_f32_16x16x32_bf16 v[36:39], a[88:91], v[156:159], v[36:39]
v_exp_f32 v151, v135
v_mfma_f32_16x16x32_bf16 v[52:55], a[92:95], v[156:159], v[52:55]
s_add_u32 m0, m0, 1024
v_or3_b32 v176, v152, v153, v154
v_cvt_pk_bf16_f32 v155, v150, v151
v_mfma_f32_16x16x32_bf16 v[68:71], a[96:99], v[156:159], v[68:71]
buffer_load_dwordx4 v248, s[40:43], s64 offen lds
s_add_u32 s64, s64, s65
s_add_u32 s69, s69, 36864
s_cmp_eq_u32 s69, 147456
s_cselect_b32 s69, 0, s69
v_bitop3_b32 v176, v176, v155, s87 bitop3:0xa8
v_mfma_f32_16x16x32_bf16 v[84:87], a[100:103], v[156:159], v[84:87]
buffer_load_dwordx4 a[32:35], v235, s[44:47], s71 offen offset:0 sc1
v_cmp_ne_u32 vcc, 0, v176
v_mfma_f32_16x16x32_bf16 v[100:103], a[104:107], v[156:159], v[100:103]
buffer_load_dwordx4 a[36:39], v235, s[44:47], s71 offen offset:1024 sc1
buffer_load_dwordx4 a[40:43], v235, s[44:47], s71 offen offset:2048 sc1
v_mfma_f32_16x16x32_bf16 v[116:119], a[108:111], v[156:159], v[116:119]
buffer_load_dwordx4 a[44:47], v235, s[44:47], s71 offen offset:3072 sc1
s_add_u32 s71, s71, 4096
s_cmp_lg_u64 vcc, 0
v_mfma_f32_16x16x32_bf16 v[212:215], v[180:183], v[156:159], v[212:215]
s_cbranch_scc1 SLOW62
SLOWR62:
v_mfma_f32_16x16x32_bf16 v[8:11], a[80:83], v[152:155], v[8:11]
v_exp_f32 v144, v136
v_exp_f32 v145, v137
v_mfma_f32_16x16x32_bf16 v[24:27], a[84:87], v[152:155], v[24:27]
v_cvt_pk_bf16_f32 v156, v144, v145
v_exp_f32 v146, v138
v_mfma_f32_16x16x32_bf16 v[40:43], a[88:91], v[152:155], v[40:43]
v_exp_f32 v147, v139
v_mfma_f32_16x16x32_bf16 v[56:59], a[92:95], v[152:155], v[56:59]
v_cvt_pk_bf16_f32 v157, v146, v147
v_exp_f32 v148, v140
v_mfma_f32_16x16x32_bf16 v[72:75], a[96:99], v[152:155], v[72:75]
v_exp_f32 v149, v141
s_nop 0
v_cvt_pk_bf16_f32 v158, v148, v149
v_mfma_f32_16x16x32_bf16 v[88:91], a[100:103], v[152:155], v[88:91]
v_exp_f32 v150, v142
v_mfma_f32_16x16x32_bf16 v[104:107], a[104:107], v[152:155], v[104:107]
v_exp_f32 v151, v143
v_or3_b32 v176, v156, v157, v158
v_mfma_f32_16x16x32_bf16 v[120:123], a[108:111], v[152:155], v[120:123]
v_cvt_pk_bf16_f32 v159, v150, v151
v_bitop3_b32 v176, v176, v159, s87 bitop3:0xa8
v_mfma_f32_16x16x32_bf16 v[216:219], v[180:183], v[152:155], v[216:219]
v_cmp_ne_u32 vcc, 0, v176
s_cmp_lg_u64 vcc, 0
s_cbranch_scc1 SLOW63
SLOWR63:
v_mfma_f32_16x16x32_bf16 v[12:15], a[80:83], v[156:159], v[12:15]
v_mfma_f32_16x16x32_bf16 v[28:31], a[84:87], v[156:159], v[28:31]
v_mfma_f32_16x16x32_bf16 v[44:47], a[88:91], v[156:159], v[44:47]
v_mfma_f32_16x16x32_bf16 v[60:63], a[92:95], v[156:159], v[60:63]
v_mfma_f32_16x16x32_bf16 v[76:79], a[96:99], v[156:159], v[76:79]
v_mfma_f32_16x16x32_bf16 v[92:95], a[100:103], v[156:159], v[92:95]
v_mfma_f32_16x16x32_bf16 v[108:111], a[104:107], v[156:159], v[108:111]
v_mfma_f32_16x16x32_bf16 v[124:127], a[108:111], v[156:159], v[124:127]
v_mfma_f32_16x16x32_bf16 v[220:223], v[180:183], v[156:159], v[220:223]
EP2:
s_lshr_b32 s72, s6, 3
s_lshl_b32 s72, s72, 5
s_and_b32 s78, s6, 7
s_lshl_b32 s78, s78, 1
s_add_u32 s72, s72, s78
s_lshl_b32 s72, s72, 12
v_rcp_f32 v128, v208
v_rcp_f32 v130, v212
v_rcp_f32 v132, v216
v_rcp_f32 v134, v220
v_mul_f32 v128, 0x40faf233, v128
v_mul_f32 v130, 0x40faf233, v130
v_mul_f32 v132, 0x40faf233, v132
v_mul_f32 v134, 0x40faf233, v134
v_pk_mul_f32 v[0:1], v[0:1], v[128:129] op_sel_hi:[1,0]
v_pk_mul_f32 v[2:3], v[2:3], v[128:129] op_sel_hi:[1,0]
)"
R"(
v_pk_mul_f32 v[16:17], v[16:17], v[128:129] op_sel_hi:[1,0]
v_pk_mul_f32 v[18:19], v[18:19], v[128:129] op_sel_hi:[1,0]
v_cvt_pk_bf16_f32 v144, v0, v1
v_cvt_pk_bf16_f32 v145, v2, v3
v_cvt_pk_bf16_f32 v146, v16, v17
v_cvt_pk_bf16_f32 v147, v18, v19
buffer_store_dwordx4 v[144:147], v235, s[48:51], s72 offen offset:0 sc1
v_pk_mul_f32 v[4:5], v[4:5], v[130:131] op_sel_hi:[1,0]
v_pk_mul_f32 v[6:7], v[6:7], v[130:131] op_sel_hi:[1,0]
v_pk_mul_f32 v[20:21], v[20:21], v[130:131] op_sel_hi:[1,0]
v_pk_mul_f32 v[22:23], v[22:23], v[130:131] op_sel_hi:[1,0]
v_cvt_pk_bf16_f32 v148, v4, v5
v_cvt_pk_bf16_f32 v149, v6, v7
v_cvt_pk_bf16_f32 v150, v20, v21
v_cvt_pk_bf16_f32 v151, v22, v23
buffer_store_dwordx4 v[148:151], v235, s[48:51], s72 offen offset:1024 sc1
v_pk_mul_f32 v[8:9], v[8:9], v[132:133] op_sel_hi:[1,0]
v_pk_mul_f32 v[10:11], v[10:11], v[132:133] op_sel_hi:[1,0]
v_pk_mul_f32 v[24:25], v[24:25], v[132:133] op_sel_hi:[1,0]
v_pk_mul_f32 v[26:27], v[26:27], v[132:133] op_sel_hi:[1,0]
v_cvt_pk_bf16_f32 v152, v8, v9
v_cvt_pk_bf16_f32 v153, v10, v11
v_cvt_pk_bf16_f32 v154, v24, v25
v_cvt_pk_bf16_f32 v155, v26, v27
buffer_store_dwordx4 v[152:155], v235, s[48:51], s72 offen offset:2048 sc1
v_pk_mul_f32 v[12:13], v[12:13], v[134:135] op_sel_hi:[1,0]
v_pk_mul_f32 v[14:15], v[14:15], v[134:135] op_sel_hi:[1,0]
v_pk_mul_f32 v[28:29], v[28:29], v[134:135] op_sel_hi:[1,0]
v_pk_mul_f32 v[30:31], v[30:31], v[134:135] op_sel_hi:[1,0]
v_cvt_pk_bf16_f32 v156, v12, v13
v_cvt_pk_bf16_f32 v157, v14, v15
v_cvt_pk_bf16_f32 v158, v28, v29
v_cvt_pk_bf16_f32 v159, v30, v31
buffer_store_dwordx4 v[156:159], v235, s[48:51], s72 offen offset:3072 sc1
s_add_u32 s72, s72, 4096
v_pk_mul_f32 v[32:33], v[32:33], v[128:129] op_sel_hi:[1,0]
v_pk_mul_f32 v[34:35], v[34:35], v[128:129] op_sel_hi:[1,0]
v_pk_mul_f32 v[48:49], v[48:49], v[128:129] op_sel_hi:[1,0]
v_pk_mul_f32 v[50:51], v[50:51], v[128:129] op_sel_hi:[1,0]
v_cvt_pk_bf16_f32 v160, v32, v33
v_cvt_pk_bf16_f32 v161, v34, v35
v_cvt_pk_bf16_f32 v162, v48, v49
v_cvt_pk_bf16_f32 v163, v50, v51
buffer_store_dwordx4 v[160:163], v235, s[48:51], s72 offen offset:0 sc1
v_pk_mul_f32 v[36:37], v[36:37], v[130:131] op_sel_hi:[1,0]
v_pk_mul_f32 v[38:39], v[38:39], v[130:131] op_sel_hi:[1,0]
v_pk_mul_f32 v[52:53], v[52:53], v[130:131] op_sel_hi:[1,0]
v_pk_mul_f32 v[54:55], v[54:55], v[130:131] op_sel_hi:[1,0]
v_cvt_pk_bf16_f32 v164, v36, v37
v_cvt_pk_bf16_f32 v165, v38, v39
v_cvt_pk_bf16_f32 v166, v52, v53
v_cvt_pk_bf16_f32 v167, v54, v55
buffer_store_dwordx4 v[164:167], v235, s[48:51], s72 offen offset:1024 sc1
v_pk_mul_f32 v[40:41], v[40:41], v[132:133] op_sel_hi:[1,0]
v_pk_mul_f32 v[42:43], v[42:43], v[132:133] op_sel_hi:[1,0]
v_pk_mul_f32 v[56:57], v[56:57], v[132:133] op_sel_hi:[1,0]
v_pk_mul_f32 v[58:59], v[58:59], v[132:133] op_sel_hi:[1,0]
v_cvt_pk_bf16_f32 v168, v40, v41
v_cvt_pk_bf16_f32 v169, v42, v43
v_cvt_pk_bf16_f32 v170, v56, v57
v_cvt_pk_bf16_f32 v171, v58, v59
buffer_store_dwordx4 v[168:171], v235, s[48:51], s72 offen offset:2048 sc1
v_pk_mul_f32 v[44:45], v[44:45], v[134:135] op_sel_hi:[1,0]
v_pk_mul_f32 v[46:47], v[46:47], v[134:135] op_sel_hi:[1,0]
v_pk_mul_f32 v[60:61], v[60:61], v[134:135] op_sel_hi:[1,0]
v_pk_mul_f32 v[62:63], v[62:63], v[134:135] op_sel_hi:[1,0]
v_cvt_pk_bf16_f32 v172, v44, v45
v_cvt_pk_bf16_f32 v173, v46, v47
v_cvt_pk_bf16_f32 v174, v60, v61
v_cvt_pk_bf16_f32 v175, v62, v63
buffer_store_dwordx4 v[172:175], v235, s[48:51], s72 offen offset:3072 sc1
s_add_u32 s72, s72, 61440
v_pk_mul_f32 v[64:65], v[64:65], v[128:129] op_sel_hi:[1,0]
v_pk_mul_f32 v[66:67], v[66:67], v[128:129] op_sel_hi:[1,0]
v_pk_mul_f32 v[80:81], v[80:81], v[128:129] op_sel_hi:[1,0]
v_pk_mul_f32 v[82:83], v[82:83], v[128:129] op_sel_hi:[1,0]
v_cvt_pk_bf16_f32 v176, v64, v65
v_cvt_pk_bf16_f32 v177, v66, v67
v_cvt_pk_bf16_f32 v178, v80, v81
v_cvt_pk_bf16_f32 v179, v82, v83
v_pk_mul_f32 v[68:69], v[68:69], v[130:131] op_sel_hi:[1,0]
v_pk_mul_f32 v[70:71], v[70:71], v[130:131] op_sel_hi:[1,0]
v_pk_mul_f32 v[84:85], v[84:85], v[130:131] op_sel_hi:[1,0]
v_pk_mul_f32 v[86:87], v[86:87], v[130:131] op_sel_hi:[1,0]
v_cvt_pk_bf16_f32 v180, v68, v69
v_cvt_pk_bf16_f32 v181, v70, v71
v_cvt_pk_bf16_f32 v182, v84, v85
v_cvt_pk_bf16_f32 v183, v86, v87
v_pk_mul_f32 v[72:73], v[72:73], v[132:133] op_sel_hi:[1,0]
v_pk_mul_f32 v[74:75], v[74:75], v[132:133] op_sel_hi:[1,0]
v_pk_mul_f32 v[88:89], v[88:89], v[132:133] op_sel_hi:[1,0]
v_pk_mul_f32 v[90:91], v[90:91], v[132:133] op_sel_hi:[1,0]
v_cvt_pk_bf16_f32 v184, v72, v73
v_cvt_pk_bf16_f32 v185, v74, v75
v_cvt_pk_bf16_f32 v186, v88, v89
v_cvt_pk_bf16_f32 v187, v90, v91
v_pk_mul_f32 v[76:77], v[76:77], v[134:135] op_sel_hi:[1,0]
v_pk_mul_f32 v[78:79], v[78:79], v[134:135] op_sel_hi:[1,0]
v_pk_mul_f32 v[92:93], v[92:93], v[134:135] op_sel_hi:[1,0]
v_pk_mul_f32 v[94:95], v[94:95], v[134:135] op_sel_hi:[1,0]
v_cvt_pk_bf16_f32 v188, v76, v77
v_cvt_pk_bf16_f32 v189, v78, v79
v_cvt_pk_bf16_f32 v190, v92, v93
v_cvt_pk_bf16_f32 v191, v94, v95
v_pk_mul_f32 v[96:97], v[96:97], v[128:129] op_sel_hi:[1,0]
v_pk_mul_f32 v[98:99], v[98:99], v[128:129] op_sel_hi:[1,0]
v_pk_mul_f32 v[112:113], v[112:113], v[128:129] op_sel_hi:[1,0]
v_pk_mul_f32 v[114:115], v[114:115], v[128:129] op_sel_hi:[1,0]
v_cvt_pk_bf16_f32 v192, v96, v97
v_cvt_pk_bf16_f32 v193, v98, v99
v_cvt_pk_bf16_f32 v194, v112, v113
v_cvt_pk_bf16_f32 v195, v114, v115
v_pk_mul_f32 v[100:101], v[100:101], v[130:131] op_sel_hi:[1,0]
v_pk_mul_f32 v[102:103], v[102:103], v[130:131] op_sel_hi:[1,0]
v_pk_mul_f32 v[116:117], v[116:117], v[130:131] op_sel_hi:[1,0]
v_pk_mul_f32 v[118:119], v[118:119], v[130:131] op_sel_hi:[1,0]
v_cvt_pk_bf16_f32 v196, v100, v101
v_cvt_pk_bf16_f32 v197, v102, v103
v_cvt_pk_bf16_f32 v198, v116, v117
v_cvt_pk_bf16_f32 v199, v118, v119
v_pk_mul_f32 v[104:105], v[104:105], v[132:133] op_sel_hi:[1,0]
v_pk_mul_f32 v[106:107], v[106:107], v[132:133] op_sel_hi:[1,0]
v_pk_mul_f32 v[120:121], v[120:121], v[132:133] op_sel_hi:[1,0]
v_pk_mul_f32 v[122:123], v[122:123], v[132:133] op_sel_hi:[1,0]
v_cvt_pk_bf16_f32 v200, v104, v105
v_cvt_pk_bf16_f32 v201, v106, v107
v_cvt_pk_bf16_f32 v202, v120, v121
v_cvt_pk_bf16_f32 v203, v122, v123
v_pk_mul_f32 v[108:109], v[108:109], v[134:135] op_sel_hi:[1,0]
v_pk_mul_f32 v[110:111], v[110:111], v[134:135] op_sel_hi:[1,0]
v_pk_mul_f32 v[124:125], v[124:125], v[134:135] op_sel_hi:[1,0]
v_pk_mul_f32 v[126:127], v[126:127], v[134:135] op_sel_hi:[1,0]
v_cvt_pk_bf16_f32 v204, v108, v109
v_cvt_pk_bf16_f32 v205, v110, v111
v_cvt_pk_bf16_f32 v206, v124, v125
v_cvt_pk_bf16_f32 v207, v126, v127
ds_read_b64_tr_b16 a[128:129], v233 offset:0
ds_read_b64_tr_b16 a[130:131], v233 offset:4608
ds_read_b64_tr_b16 a[132:133], v233 offset:32
ds_read_b64_tr_b16 a[134:135], v233 offset:4640
ds_read_b64_tr_b16 a[136:137], v233 offset:64
ds_read_b64_tr_b16 a[138:139], v233 offset:4672
ds_read_b64_tr_b16 a[140:141], v233 offset:96
ds_read_b64_tr_b16 a[142:143], v233 offset:4704
ds_read_b64_tr_b16 a[144:145], v233 offset:128
ds_read_b64_tr_b16 a[146:147], v233 offset:4736
ds_read_b64_tr_b16 a[148:149], v233 offset:160
ds_read_b64_tr_b16 a[150:151], v233 offset:4768
s_cmp_ge_u32 s34, 4
s_cbranch_scc1 FROLE
s_waitcnt vmcnt(0)
s_add_u32 s77, s39, 2048
v_mov_b32 v238, s82
v_mov_b32 v239, s83
s_mov_b64 exec, 1
buffer_store_dwordx2 v[238:239], off, s[52:55], s77 offset:0 sc1
s_mov_b64 exec, -1
s_nop 1
s_add_u32 s92, s92, 1024
s_mov_b32 s46, 131072
s_mov_b32 s71, 0
s_mov_b32 s86, 0
P_O0:
buffer_load_dwordx2 v[250:251], v252, s[52:55], s92 offen offset:0 sc1
s_waitcnt vmcnt(0)
v_cmp_ne_u64 vcc, s[82:83], v[250:251]
s_cbranch_vccz P_O0_OK
s_sleep 1
s_add_u32 s86, s86, 1
s_cmp_lt_u32 s86, 400000
s_cbranch_scc1 P_O0
P_O0_OK:
buffer_load_dwordx4 a[64:67], v235, s[44:47], s71 offen offset:0 sc1
buffer_load_dwordx4 a[68:71], v235, s[44:47], s71 offen offset:1024 sc1
buffer_load_dwordx4 a[72:75], v235, s[44:47], s71 offen offset:2048 sc1
buffer_load_dwordx4 a[76:79], v235, s[44:47], s71 offen offset:3072 sc1
s_add_u32 s71, s71, 4096
buffer_load_dwordx4 a[80:83], v235, s[44:47], s71 offen offset:0 sc1
buffer_load_dwordx4 a[84:87], v235, s[44:47], s71 offen offset:1024 sc1
buffer_load_dwordx4 a[88:91], v235, s[44:47], s71 offen offset:2048 sc1
buffer_load_dwordx4 a[92:95], v235, s[44:47], s71 offen offset:3072 sc1
s_add_u32 s71, s71, 4096
s_add_u32 s92, s92, 1024
s_mov_b32 s79, 1105920
s_waitcnt vmcnt(4) lgkmcnt(10)
v_mfma_f32_16x16x32_bf16 v[0:3], a[128:131], a[64:67], 0
ds_read_b64_tr_b16 a[152:153], v233 offset:192
v_mfma_f32_16x16x32_bf16 v[4:7], a[128:131], a[68:71], 0
ds_read_b64_tr_b16 a[154:155], v233 offset:4800
v_mfma_f32_16x16x32_bf16 v[8:11], a[128:131], a[72:75], 0
v_mfma_f32_16x16x32_bf16 v[12:15], a[128:131], a[76:79], 0
buffer_load_dwordx4 a[96:99], v235, s[44:47], s71 offen offset:0 sc1
s_waitcnt lgkmcnt(10)
v_mfma_f32_16x16x32_bf16 v[16:19], a[132:135], a[64:67], 0
ds_read_b64_tr_b16 a[156:157], v233 offset:224
v_mfma_f32_16x16x32_bf16 v[20:23], a[132:135], a[68:71], 0
ds_read_b64_tr_b16 a[158:159], v233 offset:4832
v_mfma_f32_16x16x32_bf16 v[24:27], a[132:135], a[72:75], 0
v_mfma_f32_16x16x32_bf16 v[28:31], a[132:135], a[76:79], 0
s_waitcnt lgkmcnt(10)
v_mfma_f32_16x16x32_bf16 v[32:35], a[136:139], a[64:67], 0
ds_read_b64_tr_b16 a[160:161], v233 offset:256
v_mfma_f32_16x16x32_bf16 v[36:39], a[136:139], a[68:71], 0
ds_read_b64_tr_b16 a[162:163], v233 offset:4864
v_mfma_f32_16x16x32_bf16 v[40:43], a[136:139], a[72:75], 0
buffer_store_dwordx4 v[176:179], v235, s[48:51], s72 offen offset:0 sc1
v_mfma_f32_16x16x32_bf16 v[44:47], a[136:139], a[76:79], 0
buffer_load_dwordx4 a[100:103], v235, s[44:47], s71 offen offset:1024 sc1
s_waitcnt lgkmcnt(10)
v_mfma_f32_16x16x32_bf16 v[48:51], a[140:143], a[64:67], 0
ds_read_b64_tr_b16 a[128:129], v233 offset:9216
v_mfma_f32_16x16x32_bf16 v[52:55], a[140:143], a[68:71], 0
ds_read_b64_tr_b16 a[130:131], v233 offset:13824
v_mfma_f32_16x16x32_bf16 v[56:59], a[140:143], a[72:75], 0
v_mfma_f32_16x16x32_bf16 v[60:63], a[140:143], a[76:79], 0
s_waitcnt lgkmcnt(10)
v_mfma_f32_16x16x32_bf16 v[64:67], a[144:147], a[64:67], 0
ds_read_b64_tr_b16 a[132:133], v233 offset:9248
v_mfma_f32_16x16x32_bf16 v[68:71], a[144:147], a[68:71], 0
ds_read_b64_tr_b16 a[134:135], v233 offset:13856
v_mfma_f32_16x16x32_bf16 v[72:75], a[144:147], a[72:75], 0
v_mfma_f32_16x16x32_bf16 v[76:79], a[144:147], a[76:79], 0
buffer_load_dwordx4 a[104:107], v235, s[44:47], s71 offen offset:2048 sc1
s_waitcnt lgkmcnt(10)
v_mfma_f32_16x16x32_bf16 v[80:83], a[148:151], a[64:67], 0
ds_read_b64_tr_b16 a[136:137], v233 offset:9280
v_mfma_f32_16x16x32_bf16 v[84:87], a[148:151], a[68:71], 0
ds_read_b64_tr_b16 a[138:139], v233 offset:13888
v_mfma_f32_16x16x32_bf16 v[88:91], a[148:151], a[72:75], 0
v_mfma_f32_16x16x32_bf16 v[92:95], a[148:151], a[76:79], 0
s_waitcnt lgkmcnt(10)
v_mfma_f32_16x16x32_bf16 v[96:99], a[152:155], a[64:67], 0
ds_read_b64_tr_b16 a[140:141], v233 offset:9312
v_mfma_f32_16x16x32_bf16 v[100:103], a[152:155], a[68:71], 0
ds_read_b64_tr_b16 a[142:143], v233 offset:13920
v_mfma_f32_16x16x32_bf16 v[104:107], a[152:155], a[72:75], 0
buffer_store_dwordx4 v[180:183], v235, s[48:51], s72 offen offset:1024 sc1
v_mfma_f32_16x16x32_bf16 v[108:111], a[152:155], a[76:79], 0
buffer_load_dwordx4 a[108:111], v235, s[44:47], s71 offen offset:3072 sc1
s_add_u32 s71, s71, 4096
s_waitcnt lgkmcnt(10)
v_mfma_f32_16x16x32_bf16 v[112:115], a[156:159], a[64:67], 0
ds_read_b64_tr_b16 a[144:145], v233 offset:9344
v_mfma_f32_16x16x32_bf16 v[116:119], a[156:159], a[68:71], 0
ds_read_b64_tr_b16 a[146:147], v233 offset:13952
v_mfma_f32_16x16x32_bf16 v[120:123], a[156:159], a[72:75], 0
v_mfma_f32_16x16x32_bf16 v[124:127], a[156:159], a[76:79], 0
s_waitcnt lgkmcnt(10)
v_mfma_f32_16x16x32_bf16 v[128:131], a[160:163], a[64:67], 0
ds_read_b64_tr_b16 a[148:149], v233 offset:9376
v_mfma_f32_16x16x32_bf16 v[132:135], a[160:163], a[68:71], 0
ds_read_b64_tr_b16 a[150:151], v233 offset:13984
v_mfma_f32_16x16x32_bf16 v[136:139], a[160:163], a[72:75], 0
v_mfma_f32_16x16x32_bf16 v[140:143], a[160:163], a[76:79], 0
s_waitcnt vmcnt(6) lgkmcnt(10)
v_mfma_f32_16x16x32_bf16 v[0:3], a[128:131], a[80:83], v[0:3]
ds_read_b64_tr_b16 a[152:153], v233 offset:9408
v_mfma_f32_16x16x32_bf16 v[4:7], a[128:131], a[84:87], v[4:7]
ds_read_b64_tr_b16 a[154:155], v233 offset:14016
v_mfma_f32_16x16x32_bf16 v[8:11], a[128:131], a[88:91], v[8:11]
v_mfma_f32_16x16x32_bf16 v[12:15], a[128:131], a[92:95], v[12:15]
buffer_load_dwordx4 a[112:115], v235, s[44:47], s71 offen offset:0 sc1
s_waitcnt lgkmcnt(10)
v_mfma_f32_16x16x32_bf16 v[16:19], a[132:135], a[80:83], v[16:19]
ds_read_b64_tr_b16 a[156:157], v233 offset:9440
v_mfma_f32_16x16x32_bf16 v[20:23], a[132:135], a[84:87], v[20:23]
ds_read_b64_tr_b16 a[158:159], v233 offset:14048
v_mfma_f32_16x16x32_bf16 v[24:27], a[132:135], a[88:91], v[24:27]
v_mfma_f32_16x16x32_bf16 v[28:31], a[132:135], a[92:95], v[28:31]
buffer_load_dwordx4 a[116:119], v235, s[44:47], s71 offen offset:1024 sc1
s_waitcnt lgkmcnt(10)
v_mfma_f32_16x16x32_bf16 v[32:35], a[136:139], a[80:83], v[32:35]
ds_read_b64_tr_b16 a[160:161], v233 offset:9472
v_mfma_f32_16x16x32_bf16 v[36:39], a[136:139], a[84:87], v[36:39]
ds_read_b64_tr_b16 a[162:163], v233 offset:14080
v_mfma_f32_16x16x32_bf16 v[40:43], a[136:139], a[88:91], v[40:43]
buffer_store_dwordx4 v[184:187], v235, s[48:51], s72 offen offset:2048 sc1
v_mfma_f32_16x16x32_bf16 v[44:47], a[136:139], a[92:95], v[44:47]
buffer_load_dwordx4 a[120:123], v235, s[44:47], s71 offen offset:2048 sc1
s_waitcnt lgkmcnt(10)
v_mfma_f32_16x16x32_bf16 v[48:51], a[140:143], a[80:83], v[48:51]
ds_read_b64_tr_b16 a[128:129], v233 offset:18432
v_mfma_f32_16x16x32_bf16 v[52:55], a[140:143], a[84:87], v[52:55]
ds_read_b64_tr_b16 a[130:131], v233 offset:23040
v_mfma_f32_16x16x32_bf16 v[56:59], a[140:143], a[88:91], v[56:59]
v_mfma_f32_16x16x32_bf16 v[60:63], a[140:143], a[92:95], v[60:63]
buffer_load_dwordx4 a[124:127], v235, s[44:47], s71 offen offset:3072 sc1
s_add_u32 s71, s71, 4096
s_waitcnt lgkmcnt(10)
v_mfma_f32_16x16x32_bf16 v[64:67], a[144:147], a[80:83], v[64:67]
ds_read_b64_tr_b16 a[132:133], v233 offset:18464
v_mfma_f32_16x16x32_bf16 v[68:71], a[144:147], a[84:87], v[68:71]
ds_read_b64_tr_b16 a[134:135], v233 offset:23072
v_mfma_f32_16x16x32_bf16 v[72:75], a[144:147], a[88:91], v[72:75]
v_mfma_f32_16x16x32_bf16 v[76:79], a[144:147], a[92:95], v[76:79]
buffer_load_dwordx4 a[0:3], v235, s[44:47], s71 offen offset:0 sc1
s_waitcnt lgkmcnt(10)
v_mfma_f32_16x16x32_bf16 v[80:83], a[148:151], a[80:83], v[80:83]
ds_read_b64_tr_b16 a[136:137], v233 offset:18496
v_mfma_f32_16x16x32_bf16 v[84:87], a[148:151], a[84:87], v[84:87]
ds_read_b64_tr_b16 a[138:139], v233 offset:23104
v_mfma_f32_16x16x32_bf16 v[88:91], a[148:151], a[88:91], v[88:91]
v_mfma_f32_16x16x32_bf16 v[92:95], a[148:151], a[92:95], v[92:95]
buffer_load_dwordx4 a[4:7], v235, s[44:47], s71 offen offset:1024 sc1
s_waitcnt lgkmcnt(10)
v_mfma_f32_16x16x32_bf16 v[96:99], a[152:155], a[80:83], v[96:99]
ds_read_b64_tr_b16 a[140:141], v233 offset:18528
v_mfma_f32_16x16x32_bf16 v[100:103], a[152:155], a[84:87], v[100:103]
ds_read_b64_tr_b16 a[142:143], v233 offset:23136
v_mfma_f32_16x16x32_bf16 v[104:107], a[152:155], a[88:91], v[104:107]
buffer_store_dwordx4 v[188:191], v235, s[48:51], s72 offen offset:3072 sc1
s_add_u32 s72, s72, 4096
v_mfma_f32_16x16x32_bf16 v[108:111], a[152:155], a[92:95], v[108:111]
buffer_load_dwordx4 a[8:11], v235, s[44:47], s71 offen offset:2048 sc1
s_waitcnt lgkmcnt(10)
v_mfma_f32_16x16x32_bf16 v[112:115], a[156:159], a[80:83], v[112:115]
ds_read_b64_tr_b16 a[144:145], v233 offset:18560
v_mfma_f32_16x16x32_bf16 v[116:119], a[156:159], a[84:87], v[116:119]
ds_read_b64_tr_b16 a[146:147], v233 offset:23168
v_mfma_f32_16x16x32_bf16 v[120:123], a[156:159], a[88:91], v[120:123]
v_mfma_f32_16x16x32_bf16 v[124:127], a[156:159], a[92:95], v[124:127]
buffer_load_dwordx4 a[12:15], v235, s[44:47], s71 offen offset:3072 sc1
s_add_u32 s71, s71, 4096
s_waitcnt lgkmcnt(10)
v_mfma_f32_16x16x32_bf16 v[128:131], a[160:163], a[80:83], v[128:131]
ds_read_b64_tr_b16 a[148:149], v233 offset:18592
v_mfma_f32_16x16x32_bf16 v[132:135], a[160:163], a[84:87], v[132:135]
ds_read_b64_tr_b16 a[150:151], v233 offset:23200
v_mfma_f32_16x16x32_bf16 v[136:139], a[160:163], a[88:91], v[136:139]
v_mfma_f32_16x16x32_bf16 v[140:143], a[160:163], a[92:95], v[140:143]
s_waitcnt vmcnt(10) lgkmcnt(10)
v_mfma_f32_16x16x32_bf16 v[0:3], a[128:131], a[96:99], v[0:3]
ds_read_b64_tr_b16 a[152:153], v233 offset:18624
v_mfma_f32_16x16x32_bf16 v[4:7], a[128:131], a[100:103], v[4:7]
ds_read_b64_tr_b16 a[154:155], v233 offset:23232
v_mfma_f32_16x16x32_bf16 v[8:11], a[128:131], a[104:107], v[8:11]
v_mfma_f32_16x16x32_bf16 v[12:15], a[128:131], a[108:111], v[12:15]
buffer_load_dwordx4 a[16:19], v235, s[44:47], s71 offen offset:0 sc1
s_waitcnt lgkmcnt(10)
v_mfma_f32_16x16x32_bf16 v[16:19], a[132:135], a[96:99], v[16:19]
ds_read_b64_tr_b16 a[156:157], v233 offset:18656
v_mfma_f32_16x16x32_bf16 v[20:23], a[132:135], a[100:103], v[20:23]
ds_read_b64_tr_b16 a[158:159], v233 offset:23264
v_mfma_f32_16x16x32_bf16 v[24:27], a[132:135], a[104:107], v[24:27]
v_mfma_f32_16x16x32_bf16 v[28:31], a[132:135], a[108:111], v[28:31]
s_waitcnt lgkmcnt(10)
v_mfma_f32_16x16x32_bf16 v[32:35], a[136:139], a[96:99], v[32:35]
ds_read_b64_tr_b16 a[160:161], v233 offset:18688
v_mfma_f32_16x16x32_bf16 v[36:39], a[136:139], a[100:103], v[36:39]
ds_read_b64_tr_b16 a[162:163], v233 offset:23296
v_mfma_f32_16x16x32_bf16 v[40:43], a[136:139], a[104:107], v[40:43]
buffer_store_dwordx4 v[192:195], v235, s[48:51], s72 offen offset:0 sc1
v_mfma_f32_16x16x32_bf16 v[44:47], a[136:139], a[108:111], v[44:47]
buffer_load_dwordx4 a[20:23], v235, s[44:47], s71 offen offset:1024 sc1
s_waitcnt lgkmcnt(10)
v_mfma_f32_16x16x32_bf16 v[48:51], a[140:143], a[96:99], v[48:51]
ds_read_b64_tr_b16 a[128:129], v233 offset:27648
v_mfma_f32_16x16x32_bf16 v[52:55], a[140:143], a[100:103], v[52:55]
ds_read_b64_tr_b16 a[130:131], v233 offset:32256
v_mfma_f32_16x16x32_bf16 v[56:59], a[140:143], a[104:107], v[56:59]
v_mfma_f32_16x16x32_bf16 v[60:63], a[140:143], a[108:111], v[60:63]
s_waitcnt lgkmcnt(10)
v_mfma_f32_16x16x32_bf16 v[64:67], a[144:147], a[96:99], v[64:67]
ds_read_b64_tr_b16 a[132:133], v233 offset:27680
v_mfma_f32_16x16x32_bf16 v[68:71], a[144:147], a[100:103], v[68:71]
ds_read_b64_tr_b16 a[134:135], v233 offset:32288
v_mfma_f32_16x16x32_bf16 v[72:75], a[144:147], a[104:107], v[72:75]
v_mfma_f32_16x16x32_bf16 v[76:79], a[144:147], a[108:111], v[76:79]
buffer_load_dwordx4 a[24:27], v235, s[44:47], s71 offen offset:2048 sc1
s_waitcnt lgkmcnt(10)
v_mfma_f32_16x16x32_bf16 v[80:83], a[148:151], a[96:99], v[80:83]
ds_read_b64_tr_b16 a[136:137], v233 offset:27712
v_mfma_f32_16x16x32_bf16 v[84:87], a[148:151], a[100:103], v[84:87]
ds_read_b64_tr_b16 a[138:139], v233 offset:32320
v_mfma_f32_16x16x32_bf16 v[88:91], a[148:151], a[104:107], v[88:91]
v_mfma_f32_16x16x32_bf16 v[92:95], a[148:151], a[108:111], v[92:95]
s_waitcnt lgkmcnt(10)
v_mfma_f32_16x16x32_bf16 v[96:99], a[152:155], a[96:99], v[96:99]
ds_read_b64_tr_b16 a[140:141], v233 offset:27744
v_mfma_f32_16x16x32_bf16 v[100:103], a[152:155], a[100:103], v[100:103]
ds_read_b64_tr_b16 a[142:143], v233 offset:32352
v_mfma_f32_16x16x32_bf16 v[104:107], a[152:155], a[104:107], v[104:107]
buffer_store_dwordx4 v[196:199], v235, s[48:51], s72 offen offset:1024 sc1
v_mfma_f32_16x16x32_bf16 v[108:111], a[152:155], a[108:111], v[108:111]
buffer_load_dwordx4 a[28:31], v235, s[44:47], s71 offen offset:3072 sc1
s_add_u32 s71, s71, 4096
s_waitcnt lgkmcnt(10)
v_mfma_f32_16x16x32_bf16 v[112:115], a[156:159], a[96:99], v[112:115]
ds_read_b64_tr_b16 a[144:145], v233 offset:27776
v_mfma_f32_16x16x32_bf16 v[116:119], a[156:159], a[100:103], v[116:119]
ds_read_b64_tr_b16 a[146:147], v233 offset:32384
v_mfma_f32_16x16x32_bf16 v[120:123], a[156:159], a[104:107], v[120:123]
v_mfma_f32_16x16x32_bf16 v[124:127], a[156:159], a[108:111], v[124:127]
s_waitcnt lgkmcnt(10)
v_mfma_f32_16x16x32_bf16 v[128:131], a[160:163], a[96:99], v[128:131]
ds_read_b64_tr_b16 a[148:149], v233 offset:27808
v_mfma_f32_16x16x32_bf16 v[132:135], a[160:163], a[100:103], v[132:135]
ds_read_b64_tr_b16 a[150:151], v233 offset:32416
v_mfma_f32_16x16x32_bf16 v[136:139], a[160:163], a[104:107], v[136:139]
v_mfma_f32_16x16x32_bf16 v[140:143], a[160:163], a[108:111], v[140:143]
)"
R"(
s_waitcnt vmcnt(11) lgkmcnt(10)
v_mfma_f32_16x16x32_bf16 v[0:3], a[128:131], a[112:115], v[0:3]
ds_read_b64_tr_b16 a[152:153], v233 offset:27840
v_mfma_f32_16x16x32_bf16 v[4:7], a[128:131], a[116:119], v[4:7]
ds_read_b64_tr_b16 a[154:155], v233 offset:32448
v_mfma_f32_16x16x32_bf16 v[8:11], a[128:131], a[120:123], v[8:11]
v_mfma_f32_16x16x32_bf16 v[12:15], a[128:131], a[124:127], v[12:15]
buffer_load_dwordx4 a[32:35], v235, s[44:47], s71 offen offset:0 sc1
s_waitcnt lgkmcnt(10)
v_mfma_f32_16x16x32_bf16 v[16:19], a[132:135], a[112:115], v[16:19]
ds_read_b64_tr_b16 a[156:157], v233 offset:27872
v_mfma_f32_16x16x32_bf16 v[20:23], a[132:135], a[116:119], v[20:23]
ds_read_b64_tr_b16 a[158:159], v233 offset:32480
v_mfma_f32_16x16x32_bf16 v[24:27], a[132:135], a[120:123], v[24:27]
buffer_store_dwordx4 v[200:203], v235, s[48:51], s72 offen offset:2048 sc1
v_mfma_f32_16x16x32_bf16 v[28:31], a[132:135], a[124:127], v[28:31]
s_waitcnt lgkmcnt(10)
v_mfma_f32_16x16x32_bf16 v[32:35], a[136:139], a[112:115], v[32:35]
ds_read_b64_tr_b16 a[160:161], v233 offset:27904
v_mfma_f32_16x16x32_bf16 v[36:39], a[136:139], a[116:119], v[36:39]
ds_read_b64_tr_b16 a[162:163], v233 offset:32512
v_mfma_f32_16x16x32_bf16 v[40:43], a[136:139], a[120:123], v[40:43]
v_mfma_f32_16x16x32_bf16 v[44:47], a[136:139], a[124:127], v[44:47]
buffer_load_dwordx4 a[36:39], v235, s[44:47], s71 offen offset:1024 sc1
s_waitcnt lgkmcnt(10)
v_mfma_f32_16x16x32_bf16 v[48:51], a[140:143], a[112:115], v[48:51]
s_add_u32 s67, s67, 36864
s_cmp_eq_u32 s67, 147456
s_cselect_b32 s66, s76, 36864
s_cselect_b32 s67, 0, s67
v_mfma_f32_16x16x32_bf16 v[52:55], a[140:143], a[116:119], v[52:55]
v_mfma_f32_16x16x32_bf16 v[56:59], a[140:143], a[120:123], v[56:59]
buffer_store_dwordx4 v[204:207], v235, s[48:51], s72 offen offset:3072 sc1
s_add_u32 s72, s72, 4096
v_mfma_f32_16x16x32_bf16 v[60:63], a[140:143], a[124:127], v[60:63]
s_waitcnt lgkmcnt(8)
v_mfma_f32_16x16x32_bf16 v[64:67], a[144:147], a[112:115], v[64:67]
v_add_u32 v233, s66, v233
v_mfma_f32_16x16x32_bf16 v[68:71], a[144:147], a[116:119], v[68:71]
v_mfma_f32_16x16x32_bf16 v[72:75], a[144:147], a[120:123], v[72:75]
v_mfma_f32_16x16x32_bf16 v[76:79], a[144:147], a[124:127], v[76:79]
buffer_load_dwordx4 a[40:43], v235, s[44:47], s71 offen offset:2048 sc1
s_waitcnt vmcnt(63) lgkmcnt(0)
s_barrier
v_mfma_f32_16x16x32_bf16 v[80:83], a[148:151], a[112:115], v[80:83]
ds_read_b64_tr_b16 a[128:129], v233 offset:0
ds_read_b64_tr_b16 a[130:131], v233 offset:4608
v_mfma_f32_16x16x32_bf16 v[84:87], a[148:151], a[116:119], v[84:87]
ds_read_b64_tr_b16 a[132:133], v233 offset:32
ds_read_b64_tr_b16 a[134:135], v233 offset:4640
v_mfma_f32_16x16x32_bf16 v[88:91], a[148:151], a[120:123], v[88:91]
v_mfma_f32_16x16x32_bf16 v[92:95], a[148:151], a[124:127], v[92:95]
s_waitcnt lgkmcnt(8)
v_mfma_f32_16x16x32_bf16 v[96:99], a[152:155], a[112:115], v[96:99]
ds_read_b64_tr_b16 a[136:137], v233 offset:64
ds_read_b64_tr_b16 a[138:139], v233 offset:4672
v_mfma_f32_16x16x32_bf16 v[100:103], a[152:155], a[116:119], v[100:103]
ds_read_b64_tr_b16 a[140:141], v233 offset:96
ds_read_b64_tr_b16 a[142:143], v233 offset:4704
v_mfma_f32_16x16x32_bf16 v[104:107], a[152:155], a[120:123], v[104:107]
v_mfma_f32_16x16x32_bf16 v[108:111], a[152:155], a[124:127], v[108:111]
buffer_load_dwordx4 a[44:47], v235, s[44:47], s71 offen offset:3072 sc1
s_add_u32 s71, s71, 4096
s_waitcnt lgkmcnt(10)
v_mfma_f32_16x16x32_bf16 v[112:115], a[156:159], a[112:115], v[112:115]
ds_read_b64_tr_b16 a[144:145], v233 offset:128
v_mfma_f32_16x16x32_bf16 v[116:119], a[156:159], a[116:119], v[116:119]
ds_read_b64_tr_b16 a[146:147], v233 offset:4736
v_mfma_f32_16x16x32_bf16 v[120:123], a[156:159], a[120:123], v[120:123]
v_mfma_f32_16x16x32_bf16 v[124:127], a[156:159], a[124:127], v[124:127]
s_waitcnt lgkmcnt(10)
v_mfma_f32_16x16x32_bf16 v[128:131], a[160:163], a[112:115], v[128:131]
ds_read_b64_tr_b16 a[148:149], v233 offset:160
v_mfma_f32_16x16x32_bf16 v[132:135], a[160:163], a[116:119], v[132:135]
ds_read_b64_tr_b16 a[150:151], v233 offset:4768
v_mfma_f32_16x16x32_bf16 v[136:139], a[160:163], a[120:123], v[136:139]
v_mfma_f32_16x16x32_bf16 v[140:143], a[160:163], a[124:127], v[140:143]
s_branch G2
FROLE:
s_mov_b32 s79, 0
s_waitcnt vmcnt(29) lgkmcnt(10)
v_mfma_f32_16x16x32_bf16 v[0:3], a[128:131], a[0:3], 0
ds_read_b64_tr_b16 a[152:153], v233 offset:192
v_mfma_f32_16x16x32_bf16 v[4:7], a[128:131], a[4:7], 0
ds_read_b64_tr_b16 a[154:155], v233 offset:4800
v_mfma_f32_16x16x32_bf16 v[8:11], a[128:131], a[8:11], 0
v_mfma_f32_16x16x32_bf16 v[12:15], a[128:131], a[12:15], 0
buffer_load_dwordx4 a[48:51], v235, s[44:47], s71 offen offset:0 sc1
s_waitcnt lgkmcnt(10)
v_mfma_f32_16x16x32_bf16 v[16:19], a[132:135], a[0:3], 0
ds_read_b64_tr_b16 a[156:157], v233 offset:224
v_mfma_f32_16x16x32_bf16 v[20:23], a[132:135], a[4:7], 0
ds_read_b64_tr_b16 a[158:159], v233 offset:4832
v_mfma_f32_16x16x32_bf16 v[24:27], a[132:135], a[8:11], 0
v_mfma_f32_16x16x32_bf16 v[28:31], a[132:135], a[12:15], 0
s_waitcnt lgkmcnt(10)
v_mfma_f32_16x16x32_bf16 v[32:35], a[136:139], a[0:3], 0
ds_read_b64_tr_b16 a[160:161], v233 offset:256
v_mfma_f32_16x16x32_bf16 v[36:39], a[136:139], a[4:7], 0
ds_read_b64_tr_b16 a[162:163], v233 offset:4864
v_mfma_f32_16x16x32_bf16 v[40:43], a[136:139], a[8:11], 0
v_mfma_f32_16x16x32_bf16 v[44:47], a[136:139], a[12:15], 0
buffer_load_dwordx4 a[52:55], v235, s[44:47], s71 offen offset:1024 sc1
s_waitcnt lgkmcnt(10)
v_mfma_f32_16x16x32_bf16 v[48:51], a[140:143], a[0:3], 0
ds_read_b64_tr_b16 a[128:129], v233 offset:9216
v_mfma_f32_16x16x32_bf16 v[52:55], a[140:143], a[4:7], 0
ds_read_b64_tr_b16 a[130:131], v233 offset:13824
v_mfma_f32_16x16x32_bf16 v[56:59], a[140:143], a[8:11], 0
v_mfma_f32_16x16x32_bf16 v[60:63], a[140:143], a[12:15], 0
s_waitcnt lgkmcnt(10)
v_mfma_f32_16x16x32_bf16 v[64:67], a[144:147], a[0:3], 0
ds_read_b64_tr_b16 a[132:133], v233 offset:9248
v_mfma_f32_16x16x32_bf16 v[68:71], a[144:147], a[4:7], 0
ds_read_b64_tr_b16 a[134:135], v233 offset:13856
v_mfma_f32_16x16x32_bf16 v[72:75], a[144:147], a[8:11], 0
v_mfma_f32_16x16x32_bf16 v[76:79], a[144:147], a[12:15], 0
buffer_load_dwordx4 a[56:59], v235, s[44:47], s71 offen offset:2048 sc1
s_waitcnt lgkmcnt(10)
v_mfma_f32_16x16x32_bf16 v[80:83], a[148:151], a[0:3], 0
ds_read_b64_tr_b16 a[136:137], v233 offset:9280
v_mfma_f32_16x16x32_bf16 v[84:87], a[148:151], a[4:7], 0
ds_read_b64_tr_b16 a[138:139], v233 offset:13888
v_mfma_f32_16x16x32_bf16 v[88:91], a[148:151], a[8:11], 0
buffer_store_dwordx4 v[176:179], v235, s[48:51], s72 offen offset:0 sc1
v_mfma_f32_16x16x32_bf16 v[92:95], a[148:151], a[12:15], 0
s_waitcnt lgkmcnt(10)
v_mfma_f32_16x16x32_bf16 v[96:99], a[152:155], a[0:3], 0
ds_read_b64_tr_b16 a[140:141], v233 offset:9312
v_mfma_f32_16x16x32_bf16 v[100:103], a[152:155], a[4:7], 0
ds_read_b64_tr_b16 a[142:143], v233 offset:13920
v_mfma_f32_16x16x32_bf16 v[104:107], a[152:155], a[8:11], 0
v_mfma_f32_16x16x32_bf16 v[108:111], a[152:155], a[12:15], 0
buffer_load_dwordx4 a[60:63], v235, s[44:47], s71 offen offset:3072 sc1
s_add_u32 s71, s71, 4096
s_waitcnt lgkmcnt(10)
v_mfma_f32_16x16x32_bf16 v[112:115], a[156:159], a[0:3], 0
ds_read_b64_tr_b16 a[144:145], v233 offset:9344
v_mfma_f32_16x16x32_bf16 v[116:119], a[156:159], a[4:7], 0
ds_read_b64_tr_b16 a[146:147], v233 offset:13952
v_mfma_f32_16x16x32_bf16 v[120:123], a[156:159], a[8:11], 0
v_mfma_f32_16x16x32_bf16 v[124:127], a[156:159], a[12:15], 0
s_waitcnt lgkmcnt(10)
v_mfma_f32_16x16x32_bf16 v[128:131], a[160:163], a[0:3], 0
ds_read_b64_tr_b16 a[148:149], v233 offset:9376
v_mfma_f32_16x16x32_bf16 v[132:135], a[160:163], a[4:7], 0
ds_read_b64_tr_b16 a[150:151], v233 offset:13984
v_mfma_f32_16x16x32_bf16 v[136:139], a[160:163], a[8:11], 0
v_mfma_f32_16x16x32_bf16 v[140:143], a[160:163], a[12:15], 0
s_waitcnt vmcnt(26) lgkmcnt(10)
v_mfma_f32_16x16x32_bf16 v[0:3], a[128:131], a[16:19], v[0:3]
ds_read_b64_tr_b16 a[152:153], v233 offset:9408
v_mfma_f32_16x16x32_bf16 v[4:7], a[128:131], a[20:23], v[4:7]
ds_read_b64_tr_b16 a[154:155], v233 offset:14016
v_mfma_f32_16x16x32_bf16 v[8:11], a[128:131], a[24:27], v[8:11]
v_mfma_f32_16x16x32_bf16 v[12:15], a[128:131], a[28:31], v[12:15]
buffer_load_dwordx4 a[64:67], v235, s[44:47], s71 offen offset:0 sc1
s_waitcnt lgkmcnt(10)
v_mfma_f32_16x16x32_bf16 v[16:19], a[132:135], a[16:19], v[16:19]
ds_read_b64_tr_b16 a[156:157], v233 offset:9440
v_mfma_f32_16x16x32_bf16 v[20:23], a[132:135], a[20:23], v[20:23]
ds_read_b64_tr_b16 a[158:159], v233 offset:14048
v_mfma_f32_16x16x32_bf16 v[24:27], a[132:135], a[24:27], v[24:27]
v_mfma_f32_16x16x32_bf16 v[28:31], a[132:135], a[28:31], v[28:31]
s_waitcnt lgkmcnt(10)
v_mfma_f32_16x16x32_bf16 v[32:35], a[136:139], a[16:19], v[32:35]
ds_read_b64_tr_b16 a[160:161], v233 offset:9472
v_mfma_f32_16x16x32_bf16 v[36:39], a[136:139], a[20:23], v[36:39]
ds_read_b64_tr_b16 a[162:163], v233 offset:14080
v_mfma_f32_16x16x32_bf16 v[40:43], a[136:139], a[24:27], v[40:43]
v_mfma_f32_16x16x32_bf16 v[44:47], a[136:139], a[28:31], v[44:47]
buffer_load_dwordx4 a[68:71], v235, s[44:47], s71 offen offset:1024 sc1
s_waitcnt lgkmcnt(10)
v_mfma_f32_16x16x32_bf16 v[48:51], a[140:143], a[16:19], v[48:51]
ds_read_b64_tr_b16 a[128:129], v233 offset:18432
v_mfma_f32_16x16x32_bf16 v[52:55], a[140:143], a[20:23], v[52:55]
ds_read_b64_tr_b16 a[130:131], v233 offset:23040
v_mfma_f32_16x16x32_bf16 v[56:59], a[140:143], a[24:27], v[56:59]
v_mfma_f32_16x16x32_bf16 v[60:63], a[140:143], a[28:31], v[60:63]
s_waitcnt lgkmcnt(10)
v_mfma_f32_16x16x32_bf16 v[64:67], a[144:147], a[16:19], v[64:67]
ds_read_b64_tr_b16 a[132:133], v233 offset:18464
v_mfma_f32_16x16x32_bf16 v[68:71], a[144:147], a[20:23], v[68:71]
ds_read_b64_tr_b16 a[134:135], v233 offset:23072
v_mfma_f32_16x16x32_bf16 v[72:75], a[144:147], a[24:27], v[72:75]
buffer_store_dwordx4 v[180:183], v235, s[48:51], s72 offen offset:1024 sc1
v_mfma_f32_16x16x32_bf16 v[76:79], a[144:147], a[28:31], v[76:79]
buffer_load_dwordx4 a[72:75], v235, s[44:47], s71 offen offset:2048 sc1
s_waitcnt lgkmcnt(10)
v_mfma_f32_16x16x32_bf16 v[80:83], a[148:151], a[16:19], v[80:83]
ds_read_b64_tr_b16 a[136:137], v233 offset:18496
v_mfma_f32_16x16x32_bf16 v[84:87], a[148:151], a[20:23], v[84:87]
ds_read_b64_tr_b16 a[138:139], v233 offset:23104
v_mfma_f32_16x16x32_bf16 v[88:91], a[148:151], a[24:27], v[88:91]
s_waitcnt vmcnt(9)
s_add_u32 s77, s39, 2048
v_mov_b32 v238, s82
v_mov_b32 v239, s83
s_mov_b64 exec, 1
buffer_store_dwordx2 v[238:239], off, s[52:55], s77 offset:0 sc1
s_mov_b64 exec, -1
s_nop 1
v_mfma_f32_16x16x32_bf16 v[92:95], a[148:151], a[28:31], v[92:95]
s_waitcnt lgkmcnt(10)
v_mfma_f32_16x16x32_bf16 v[96:99], a[152:155], a[16:19], v[96:99]
ds_read_b64_tr_b16 a[140:141], v233 offset:18528
v_mfma_f32_16x16x32_bf16 v[100:103], a[152:155], a[20:23], v[100:103]
ds_read_b64_tr_b16 a[142:143], v233 offset:23136
v_mfma_f32_16x16x32_bf16 v[104:107], a[152:155], a[24:27], v[104:107]
v_mfma_f32_16x16x32_bf16 v[108:111], a[152:155], a[28:31], v[108:111]
buffer_load_dwordx4 a[76:79], v235, s[44:47], s71 offen offset:3072 sc1
s_add_u32 s71, s71, 4096
s_waitcnt lgkmcnt(10)
v_mfma_f32_16x16x32_bf16 v[112:115], a[156:159], a[16:19], v[112:115]
ds_read_b64_tr_b16 a[144:145], v233 offset:18560
v_mfma_f32_16x16x32_bf16 v[116:119], a[156:159], a[20:23], v[116:119]
ds_read_b64_tr_b16 a[146:147], v233 offset:23168
v_mfma_f32_16x16x32_bf16 v[120:123], a[156:159], a[24:27], v[120:123]
v_mfma_f32_16x16x32_bf16 v[124:127], a[156:159], a[28:31], v[124:127]
s_waitcnt lgkmcnt(10)
v_mfma_f32_16x16x32_bf16 v[128:131], a[160:163], a[16:19], v[128:131]
ds_read_b64_tr_b16 a[148:149], v233 offset:18592
v_mfma_f32_16x16x32_bf16 v[132:135], a[160:163], a[20:23], v[132:135]
ds_read_b64_tr_b16 a[150:151], v233 offset:23200
v_mfma_f32_16x16x32_bf16 v[136:139], a[160:163], a[24:27], v[136:139]
v_mfma_f32_16x16x32_bf16 v[140:143], a[160:163], a[28:31], v[140:143]
s_waitcnt vmcnt(19) lgkmcnt(10)
v_mfma_f32_16x16x32_bf16 v[0:3], a[128:131], a[32:35], v[0:3]
ds_read_b64_tr_b16 a[152:153], v233 offset:18624
v_mfma_f32_16x16x32_bf16 v[4:7], a[128:131], a[36:39], v[4:7]
ds_read_b64_tr_b16 a[154:155], v233 offset:23232
v_mfma_f32_16x16x32_bf16 v[8:11], a[128:131], a[40:43], v[8:11]
v_mfma_f32_16x16x32_bf16 v[12:15], a[128:131], a[44:47], v[12:15]
buffer_load_dwordx4 a[80:83], v235, s[44:47], s71 offen offset:0 sc1
s_waitcnt lgkmcnt(10)
v_mfma_f32_16x16x32_bf16 v[16:19], a[132:135], a[32:35], v[16:19]
ds_read_b64_tr_b16 a[156:157], v233 offset:18656
v_mfma_f32_16x16x32_bf16 v[20:23], a[132:135], a[36:39], v[20:23]
ds_read_b64_tr_b16 a[158:159], v233 offset:23264
v_mfma_f32_16x16x32_bf16 v[24:27], a[132:135], a[40:43], v[24:27]
v_mfma_f32_16x16x32_bf16 v[28:31], a[132:135], a[44:47], v[28:31]
s_waitcnt lgkmcnt(10)
v_mfma_f32_16x16x32_bf16 v[32:35], a[136:139], a[32:35], v[32:35]
ds_read_b64_tr_b16 a[160:161], v233 offset:18688
v_mfma_f32_16x16x32_bf16 v[36:39], a[136:139], a[36:39], v[36:39]
ds_read_b64_tr_b16 a[162:163], v233 offset:23296
v_mfma_f32_16x16x32_bf16 v[40:43], a[136:139], a[40:43], v[40:43]
v_mfma_f32_16x16x32_bf16 v[44:47], a[136:139], a[44:47], v[44:47]
buffer_load_dwordx4 a[84:87], v235, s[44:47], s71 offen offset:1024 sc1
s_waitcnt lgkmcnt(10)
v_mfma_f32_16x16x32_bf16 v[48:51], a[140:143], a[32:35], v[48:51]
ds_read_b64_tr_b16 a[128:129], v233 offset:27648
v_mfma_f32_16x16x32_bf16 v[52:55], a[140:143], a[36:39], v[52:55]
ds_read_b64_tr_b16 a[130:131], v233 offset:32256
v_mfma_f32_16x16x32_bf16 v[56:59], a[140:143], a[40:43], v[56:59]
buffer_store_dwordx4 v[184:187], v235, s[48:51], s72 offen offset:2048 sc1
v_mfma_f32_16x16x32_bf16 v[60:63], a[140:143], a[44:47], v[60:63]
s_waitcnt lgkmcnt(10)
v_mfma_f32_16x16x32_bf16 v[64:67], a[144:147], a[32:35], v[64:67]
ds_read_b64_tr_b16 a[132:133], v233 offset:27680
v_mfma_f32_16x16x32_bf16 v[68:71], a[144:147], a[36:39], v[68:71]
ds_read_b64_tr_b16 a[134:135], v233 offset:32288
v_mfma_f32_16x16x32_bf16 v[72:75], a[144:147], a[40:43], v[72:75]
v_mfma_f32_16x16x32_bf16 v[76:79], a[144:147], a[44:47], v[76:79]
buffer_load_dwordx4 a[88:91], v235, s[44:47], s71 offen offset:2048 sc1
s_waitcnt lgkmcnt(10)
v_mfma_f32_16x16x32_bf16 v[80:83], a[148:151], a[32:35], v[80:83]
ds_read_b64_tr_b16 a[136:137], v233 offset:27712
v_mfma_f32_16x16x32_bf16 v[84:87], a[148:151], a[36:39], v[84:87]
ds_read_b64_tr_b16 a[138:139], v233 offset:32320
v_mfma_f32_16x16x32_bf16 v[88:91], a[148:151], a[40:43], v[88:91]
v_mfma_f32_16x16x32_bf16 v[92:95], a[148:151], a[44:47], v[92:95]
s_waitcnt lgkmcnt(10)
v_mfma_f32_16x16x32_bf16 v[96:99], a[152:155], a[32:35], v[96:99]
ds_read_b64_tr_b16 a[140:141], v233 offset:27744
v_mfma_f32_16x16x32_bf16 v[100:103], a[152:155], a[36:39], v[100:103]
ds_read_b64_tr_b16 a[142:143], v233 offset:32352
v_mfma_f32_16x16x32_bf16 v[104:107], a[152:155], a[40:43], v[104:107]
v_mfma_f32_16x16x32_bf16 v[108:111], a[152:155], a[44:47], v[108:111]
buffer_load_dwordx4 a[92:95], v235, s[44:47], s71 offen offset:3072 sc1
s_add_u32 s71, s71, 4096
s_waitcnt lgkmcnt(10)
v_mfma_f32_16x16x32_bf16 v[112:115], a[156:159], a[32:35], v[112:115]
ds_read_b64_tr_b16 a[144:145], v233 offset:27776
v_mfma_f32_16x16x32_bf16 v[116:119], a[156:159], a[36:39], v[116:119]
ds_read_b64_tr_b16 a[146:147], v233 offset:32384
v_mfma_f32_16x16x32_bf16 v[120:123], a[156:159], a[40:43], v[120:123]
v_mfma_f32_16x16x32_bf16 v[124:127], a[156:159], a[44:47], v[124:127]
s_waitcnt lgkmcnt(10)
v_mfma_f32_16x16x32_bf16 v[128:131], a[160:163], a[32:35], v[128:131]
ds_read_b64_tr_b16 a[148:149], v233 offset:27808
v_mfma_f32_16x16x32_bf16 v[132:135], a[160:163], a[36:39], v[132:135]
ds_read_b64_tr_b16 a[150:151], v233 offset:32416
v_mfma_f32_16x16x32_bf16 v[136:139], a[160:163], a[40:43], v[136:139]
v_mfma_f32_16x16x32_bf16 v[140:143], a[160:163], a[44:47], v[140:143]
s_waitcnt vmcnt(11) lgkmcnt(10)
v_mfma_f32_16x16x32_bf16 v[0:3], a[128:131], a[48:51], v[0:3]
ds_read_b64_tr_b16 a[152:153], v233 offset:27840
v_mfma_f32_16x16x32_bf16 v[4:7], a[128:131], a[52:55], v[4:7]
ds_read_b64_tr_b16 a[154:155], v233 offset:32448
v_mfma_f32_16x16x32_bf16 v[8:11], a[128:131], a[56:59], v[8:11]
v_mfma_f32_16x16x32_bf16 v[12:15], a[128:131], a[60:63], v[12:15]
buffer_load_dwordx4 a[96:99], v235, s[44:47], s71 offen offset:0 sc1
s_waitcnt lgkmcnt(10)
v_mfma_f32_16x16x32_bf16 v[16:19], a[132:135], a[48:51], v[16:19]
ds_read_b64_tr_b16 a[156:157], v233 offset:27872
v_mfma_f32_16x16x32_bf16 v[20:23], a[132:135], a[52:55], v[20:23]
ds_read_b64_tr_b16 a[158:159], v233 offset:32480
v_mfma_f32_16x16x32_bf16 v[24:27], a[132:135], a[56:59], v[24:27]
v_mfma_f32_16x16x32_bf16 v[28:31], a[132:135], a[60:63], v[28:31]
s_waitcnt lgkmcnt(10)
v_mfma_f32_16x16x32_bf16 v[32:35], a[136:139], a[48:51], v[32:35]
ds_read_b64_tr_b16 a[160:161], v233 offset:27904
v_mfma_f32_16x16x32_bf16 v[36:39], a[136:139], a[52:55], v[36:39]
ds_read_b64_tr_b16 a[162:163], v233 offset:32512
v_mfma_f32_16x16x32_bf16 v[40:43], a[136:139], a[56:59], v[40:43]
buffer_store_dwordx4 v[188:191], v235, s[48:51], s72 offen offset:3072 sc1
s_add_u32 s72, s72, 4096
v_mfma_f32_16x16x32_bf16 v[44:47], a[136:139], a[60:63], v[44:47]
buffer_load_dwordx4 a[100:103], v235, s[44:47], s71 offen offset:1024 sc1
s_waitcnt lgkmcnt(10)
v_mfma_f32_16x16x32_bf16 v[48:51], a[140:143], a[48:51], v[48:51]
s_add_u32 s67, s67, 36864
s_cmp_eq_u32 s67, 147456
s_cselect_b32 s66, s76, 36864
s_cselect_b32 s67, 0, s67
v_mfma_f32_16x16x32_bf16 v[52:55], a[140:143], a[52:55], v[52:55]
v_mfma_f32_16x16x32_bf16 v[56:59], a[140:143], a[56:59], v[56:59]
v_mfma_f32_16x16x32_bf16 v[60:63], a[140:143], a[60:63], v[60:63]
s_waitcnt lgkmcnt(8)
v_mfma_f32_16x16x32_bf16 v[64:67], a[144:147], a[48:51], v[64:67]
v_add_u32 v233, s66, v233
v_mfma_f32_16x16x32_bf16 v[68:71], a[144:147], a[52:55], v[68:71]
v_mfma_f32_16x16x32_bf16 v[72:75], a[144:147], a[56:59], v[72:75]
v_mfma_f32_16x16x32_bf16 v[76:79], a[144:147], a[60:63], v[76:79]
buffer_load_dwordx4 a[104:107], v235, s[44:47], s71 offen offset:2048 sc1
s_waitcnt vmcnt(58) lgkmcnt(0)
s_barrier
v_mfma_f32_16x16x32_bf16 v[80:83], a[148:151], a[48:51], v[80:83]
ds_read_b64_tr_b16 a[128:129], v233 offset:0
ds_read_b64_tr_b16 a[130:131], v233 offset:4608
v_mfma_f32_16x16x32_bf16 v[84:87], a[148:151], a[52:55], v[84:87]
ds_read_b64_tr_b16 a[132:133], v233 offset:32
ds_read_b64_tr_b16 a[134:135], v233 offset:4640
v_mfma_f32_16x16x32_bf16 v[88:91], a[148:151], a[56:59], v[88:91]
v_mfma_f32_16x16x32_bf16 v[92:95], a[148:151], a[60:63], v[92:95]
s_waitcnt lgkmcnt(8)
v_mfma_f32_16x16x32_bf16 v[96:99], a[152:155], a[48:51], v[96:99]
ds_read_b64_tr_b16 a[136:137], v233 offset:64
ds_read_b64_tr_b16 a[138:139], v233 offset:4672
v_mfma_f32_16x16x32_bf16 v[100:103], a[152:155], a[52:55], v[100:103]
ds_read_b64_tr_b16 a[140:141], v233 offset:96
ds_read_b64_tr_b16 a[142:143], v233 offset:4704
v_mfma_f32_16x16x32_bf16 v[104:107], a[152:155], a[56:59], v[104:107]
v_mfma_f32_16x16x32_bf16 v[108:111], a[152:155], a[60:63], v[108:111]
buffer_load_dwordx4 a[108:111], v235, s[44:47], s71 offen offset:3072 sc1
s_add_u32 s71, s71, 4096
s_waitcnt lgkmcnt(10)
v_mfma_f32_16x16x32_bf16 v[112:115], a[156:159], a[48:51], v[112:115]
ds_read_b64_tr_b16 a[144:145], v233 offset:128
v_mfma_f32_16x16x32_bf16 v[116:119], a[156:159], a[52:55], v[116:119]
ds_read_b64_tr_b16 a[146:147], v233 offset:4736
v_mfma_f32_16x16x32_bf16 v[120:123], a[156:159], a[56:59], v[120:123]
v_mfma_f32_16x16x32_bf16 v[124:127], a[156:159], a[60:63], v[124:127]
s_waitcnt lgkmcnt(10)
v_mfma_f32_16x16x32_bf16 v[128:131], a[160:163], a[48:51], v[128:131]
ds_read_b64_tr_b16 a[148:149], v233 offset:160
v_mfma_f32_16x16x32_bf16 v[132:135], a[160:163], a[52:55], v[132:135]
ds_read_b64_tr_b16 a[150:151], v233 offset:4768
v_mfma_f32_16x16x32_bf16 v[136:139], a[160:163], a[56:59], v[136:139]
v_mfma_f32_16x16x32_bf16 v[140:143], a[160:163], a[60:63], v[140:143]
s_waitcnt vmcnt(10) lgkmcnt(10)
v_mfma_f32_16x16x32_bf16 v[0:3], a[128:131], a[64:67], v[0:3]
ds_read_b64_tr_b16 a[152:153], v233 offset:192
v_mfma_f32_16x16x32_bf16 v[4:7], a[128:131], a[68:71], v[4:7]
ds_read_b64_tr_b16 a[154:155], v233 offset:4800
v_mfma_f32_16x16x32_bf16 v[8:11], a[128:131], a[72:75], v[8:11]
v_mfma_f32_16x16x32_bf16 v[12:15], a[128:131], a[76:79], v[12:15]
buffer_load_dwordx4 a[112:115], v235, s[44:47], s71 offen offset:0 sc1
s_waitcnt lgkmcnt(10)
v_mfma_f32_16x16x32_bf16 v[16:19], a[132:135], a[64:67], v[16:19]
ds_read_b64_tr_b16 a[156:157], v233 offset:224
v_mfma_f32_16x16x32_bf16 v[20:23], a[132:135], a[68:71], v[20:23]
ds_read_b64_tr_b16 a[158:159], v233 offset:4832
s_add_u32 m0, s70, s69
v_mfma_f32_16x16x32_bf16 v[24:27], a[132:135], a[72:75], v[24:27]
buffer_load_dwordx4 v240, s[40:43], s64 offen lds
v_mfma_f32_16x16x32_bf16 v[28:31], a[132:135], a[76:79], v[28:31]
s_waitcnt lgkmcnt(10)
v_mfma_f32_16x16x32_bf16 v[32:35], a[136:139], a[64:67], v[32:35]
ds_read_b64_tr_b16 a[160:161], v233 offset:256
v_mfma_f32_16x16x32_bf16 v[36:39], a[136:139], a[68:71], v[36:39]
ds_read_b64_tr_b16 a[162:163], v233 offset:4864
v_mfma_f32_16x16x32_bf16 v[40:43], a[136:139], a[72:75], v[40:43]
buffer_store_dwordx4 v[192:195], v235, s[48:51], s72 offen offset:0 sc1
v_mfma_f32_16x16x32_bf16 v[44:47], a[136:139], a[76:79], v[44:47]
buffer_load_dwordx4 a[116:119], v235, s[44:47], s71 offen offset:1024 sc1
s_waitcnt lgkmcnt(10)
v_mfma_f32_16x16x32_bf16 v[48:51], a[140:143], a[64:67], v[48:51]
ds_read_b64_tr_b16 a[128:129], v233 offset:9216
)"
R"(
v_mfma_f32_16x16x32_bf16 v[52:55], a[140:143], a[68:71], v[52:55]
ds_read_b64_tr_b16 a[130:131], v233 offset:13824
v_mfma_f32_16x16x32_bf16 v[56:59], a[140:143], a[72:75], v[56:59]
v_mfma_f32_16x16x32_bf16 v[60:63], a[140:143], a[76:79], v[60:63]
s_waitcnt lgkmcnt(10)
v_mfma_f32_16x16x32_bf16 v[64:67], a[144:147], a[64:67], v[64:67]
ds_read_b64_tr_b16 a[132:133], v233 offset:9248
v_mfma_f32_16x16x32_bf16 v[68:71], a[144:147], a[68:71], v[68:71]
ds_read_b64_tr_b16 a[134:135], v233 offset:13856
s_add_u32 m0, m0, 1024
v_mfma_f32_16x16x32_bf16 v[72:75], a[144:147], a[72:75], v[72:75]
buffer_load_dwordx4 v241, s[40:43], s64 offen lds
v_mfma_f32_16x16x32_bf16 v[76:79], a[144:147], a[76:79], v[76:79]
buffer_load_dwordx4 a[120:123], v235, s[44:47], s71 offen offset:2048 sc1
s_waitcnt lgkmcnt(10)
v_mfma_f32_16x16x32_bf16 v[80:83], a[148:151], a[64:67], v[80:83]
ds_read_b64_tr_b16 a[136:137], v233 offset:9280
v_mfma_f32_16x16x32_bf16 v[84:87], a[148:151], a[68:71], v[84:87]
ds_read_b64_tr_b16 a[138:139], v233 offset:13888
v_mfma_f32_16x16x32_bf16 v[88:91], a[148:151], a[72:75], v[88:91]
v_mfma_f32_16x16x32_bf16 v[92:95], a[148:151], a[76:79], v[92:95]
s_waitcnt lgkmcnt(10)
v_mfma_f32_16x16x32_bf16 v[96:99], a[152:155], a[64:67], v[96:99]
ds_read_b64_tr_b16 a[140:141], v233 offset:9312
v_mfma_f32_16x16x32_bf16 v[100:103], a[152:155], a[68:71], v[100:103]
ds_read_b64_tr_b16 a[142:143], v233 offset:13920
v_mfma_f32_16x16x32_bf16 v[104:107], a[152:155], a[72:75], v[104:107]
v_mfma_f32_16x16x32_bf16 v[108:111], a[152:155], a[76:79], v[108:111]
buffer_load_dwordx4 a[124:127], v235, s[44:47], s71 offen offset:3072 sc1
s_add_u32 s71, s71, 4096
s_waitcnt lgkmcnt(10)
v_mfma_f32_16x16x32_bf16 v[112:115], a[156:159], a[64:67], v[112:115]
ds_read_b64_tr_b16 a[144:145], v233 offset:9344
v_mfma_f32_16x16x32_bf16 v[116:119], a[156:159], a[68:71], v[116:119]
ds_read_b64_tr_b16 a[146:147], v233 offset:13952
s_add_u32 m0, m0, 1024
v_mfma_f32_16x16x32_bf16 v[120:123], a[156:159], a[72:75], v[120:123]
buffer_load_dwordx4 v242, s[40:43], s64 offen lds
v_mfma_f32_16x16x32_bf16 v[124:127], a[156:159], a[76:79], v[124:127]
s_waitcnt lgkmcnt(10)
v_mfma_f32_16x16x32_bf16 v[128:131], a[160:163], a[64:67], v[128:131]
ds_read_b64_tr_b16 a[148:149], v233 offset:9376
v_mfma_f32_16x16x32_bf16 v[132:135], a[160:163], a[68:71], v[132:135]
ds_read_b64_tr_b16 a[150:151], v233 offset:13984
v_mfma_f32_16x16x32_bf16 v[136:139], a[160:163], a[72:75], v[136:139]
buffer_store_dwordx4 v[196:199], v235, s[48:51], s72 offen offset:1024 sc1
v_mfma_f32_16x16x32_bf16 v[140:143], a[160:163], a[76:79], v[140:143]
s_waitcnt vmcnt(14) lgkmcnt(10)
v_mfma_f32_16x16x32_bf16 v[0:3], a[128:131], a[80:83], v[0:3]
ds_read_b64_tr_b16 a[152:153], v233 offset:9408
v_mfma_f32_16x16x32_bf16 v[4:7], a[128:131], a[84:87], v[4:7]
ds_read_b64_tr_b16 a[154:155], v233 offset:14016
v_mfma_f32_16x16x32_bf16 v[8:11], a[128:131], a[88:91], v[8:11]
v_mfma_f32_16x16x32_bf16 v[12:15], a[128:131], a[92:95], v[12:15]
buffer_load_dwordx4 a[0:3], v235, s[44:47], s71 offen offset:0 sc1
s_waitcnt lgkmcnt(10)
v_mfma_f32_16x16x32_bf16 v[16:19], a[132:135], a[80:83], v[16:19]
ds_read_b64_tr_b16 a[156:157], v233 offset:9440
v_mfma_f32_16x16x32_bf16 v[20:23], a[132:135], a[84:87], v[20:23]
ds_read_b64_tr_b16 a[158:159], v233 offset:14048
s_add_u32 m0, m0, 1024
v_mfma_f32_16x16x32_bf16 v[24:27], a[132:135], a[88:91], v[24:27]
buffer_load_dwordx4 v243, s[40:43], s64 offen lds
v_mfma_f32_16x16x32_bf16 v[28:31], a[132:135], a[92:95], v[28:31]
s_waitcnt lgkmcnt(10)
v_mfma_f32_16x16x32_bf16 v[32:35], a[136:139], a[80:83], v[32:35]
ds_read_b64_tr_b16 a[160:161], v233 offset:9472
v_mfma_f32_16x16x32_bf16 v[36:39], a[136:139], a[84:87], v[36:39]
ds_read_b64_tr_b16 a[162:163], v233 offset:14080
v_mfma_f32_16x16x32_bf16 v[40:43], a[136:139], a[88:91], v[40:43]
v_mfma_f32_16x16x32_bf16 v[44:47], a[136:139], a[92:95], v[44:47]
buffer_load_dwordx4 a[4:7], v235, s[44:47], s71 offen offset:1024 sc1
s_waitcnt lgkmcnt(10)
v_mfma_f32_16x16x32_bf16 v[48:51], a[140:143], a[80:83], v[48:51]
ds_read_b64_tr_b16 a[128:129], v233 offset:18432
v_mfma_f32_16x16x32_bf16 v[52:55], a[140:143], a[84:87], v[52:55]
ds_read_b64_tr_b16 a[130:131], v233 offset:23040
v_mfma_f32_16x16x32_bf16 v[56:59], a[140:143], a[88:91], v[56:59]
v_mfma_f32_16x16x32_bf16 v[60:63], a[140:143], a[92:95], v[60:63]
s_waitcnt lgkmcnt(10)
v_mfma_f32_16x16x32_bf16 v[64:67], a[144:147], a[80:83], v[64:67]
ds_read_b64_tr_b16 a[132:133], v233 offset:18464
v_mfma_f32_16x16x32_bf16 v[68:71], a[144:147], a[84:87], v[68:71]
ds_read_b64_tr_b16 a[134:135], v233 offset:23072
s_add_u32 m0, m0, 1024
v_mfma_f32_16x16x32_bf16 v[72:75], a[144:147], a[88:91], v[72:75]
buffer_load_dwordx4 v244, s[40:43], s64 offen lds
v_mfma_f32_16x16x32_bf16 v[76:79], a[144:147], a[92:95], v[76:79]
buffer_load_dwordx4 a[8:11], v235, s[44:47], s71 offen offset:2048 sc1
s_waitcnt lgkmcnt(10)
v_mfma_f32_16x16x32_bf16 v[80:83], a[148:151], a[80:83], v[80:83]
ds_read_b64_tr_b16 a[136:137], v233 offset:18496
v_mfma_f32_16x16x32_bf16 v[84:87], a[148:151], a[84:87], v[84:87]
ds_read_b64_tr_b16 a[138:139], v233 offset:23104
v_mfma_f32_16x16x32_bf16 v[88:91], a[148:151], a[88:91], v[88:91]
buffer_store_dwordx4 v[200:203], v235, s[48:51], s72 offen offset:2048 sc1
v_mfma_f32_16x16x32_bf16 v[92:95], a[148:151], a[92:95], v[92:95]
s_waitcnt lgkmcnt(10)
v_mfma_f32_16x16x32_bf16 v[96:99], a[152:155], a[80:83], v[96:99]
ds_read_b64_tr_b16 a[140:141], v233 offset:18528
v_mfma_f32_16x16x32_bf16 v[100:103], a[152:155], a[84:87], v[100:103]
ds_read_b64_tr_b16 a[142:143], v233 offset:23136
v_mfma_f32_16x16x32_bf16 v[104:107], a[152:155], a[88:91], v[104:107]
v_mfma_f32_16x16x32_bf16 v[108:111], a[152:155], a[92:95], v[108:111]
buffer_load_dwordx4 a[12:15], v235, s[44:47], s71 offen offset:3072 sc1
s_add_u32 s71, s71, 4096
s_waitcnt lgkmcnt(10)
v_mfma_f32_16x16x32_bf16 v[112:115], a[156:159], a[80:83], v[112:115]
ds_read_b64_tr_b16 a[144:145], v233 offset:18560
v_mfma_f32_16x16x32_bf16 v[116:119], a[156:159], a[84:87], v[116:119]
ds_read_b64_tr_b16 a[146:147], v233 offset:23168
s_add_u32 m0, m0, 1024
v_mfma_f32_16x16x32_bf16 v[120:123], a[156:159], a[88:91], v[120:123]
buffer_load_dwordx4 v245, s[40:43], s64 offen lds
v_mfma_f32_16x16x32_bf16 v[124:127], a[156:159], a[92:95], v[124:127]
s_waitcnt lgkmcnt(10)
v_mfma_f32_16x16x32_bf16 v[128:131], a[160:163], a[80:83], v[128:131]
ds_read_b64_tr_b16 a[148:149], v233 offset:18592
v_mfma_f32_16x16x32_bf16 v[132:135], a[160:163], a[84:87], v[132:135]
ds_read_b64_tr_b16 a[150:151], v233 offset:23200
v_mfma_f32_16x16x32_bf16 v[136:139], a[160:163], a[88:91], v[136:139]
v_mfma_f32_16x16x32_bf16 v[140:143], a[160:163], a[92:95], v[140:143]
s_waitcnt vmcnt(17) lgkmcnt(10)
v_mfma_f32_16x16x32_bf16 v[0:3], a[128:131], a[96:99], v[0:3]
ds_read_b64_tr_b16 a[152:153], v233 offset:18624
v_mfma_f32_16x16x32_bf16 v[4:7], a[128:131], a[100:103], v[4:7]
ds_read_b64_tr_b16 a[154:155], v233 offset:23232
v_mfma_f32_16x16x32_bf16 v[8:11], a[128:131], a[104:107], v[8:11]
v_mfma_f32_16x16x32_bf16 v[12:15], a[128:131], a[108:111], v[12:15]
buffer_load_dwordx4 a[16:19], v235, s[44:47], s71 offen offset:0 sc1
s_waitcnt lgkmcnt(10)
v_mfma_f32_16x16x32_bf16 v[16:19], a[132:135], a[96:99], v[16:19]
ds_read_b64_tr_b16 a[156:157], v233 offset:18656
v_mfma_f32_16x16x32_bf16 v[20:23], a[132:135], a[100:103], v[20:23]
ds_read_b64_tr_b16 a[158:159], v233 offset:23264
s_add_u32 m0, m0, 1024
v_mfma_f32_16x16x32_bf16 v[24:27], a[132:135], a[104:107], v[24:27]
buffer_load_dwordx4 v246, s[40:43], s64 offen lds
v_mfma_f32_16x16x32_bf16 v[28:31], a[132:135], a[108:111], v[28:31]
s_waitcnt lgkmcnt(10)
v_mfma_f32_16x16x32_bf16 v[32:35], a[136:139], a[96:99], v[32:35]
ds_read_b64_tr_b16 a[160:161], v233 offset:18688
v_mfma_f32_16x16x32_bf16 v[36:39], a[136:139], a[100:103], v[36:39]
ds_read_b64_tr_b16 a[162:163], v233 offset:23296
v_mfma_f32_16x16x32_bf16 v[40:43], a[136:139], a[104:107], v[40:43]
buffer_store_dwordx4 v[204:207], v235, s[48:51], s72 offen offset:3072 sc1
s_add_u32 s72, s72, 4096
v_mfma_f32_16x16x32_bf16 v[44:47], a[136:139], a[108:111], v[44:47]
buffer_load_dwordx4 a[20:23], v235, s[44:47], s71 offen offset:1024 sc1
s_waitcnt lgkmcnt(10)
v_mfma_f32_16x16x32_bf16 v[48:51], a[140:143], a[96:99], v[48:51]
ds_read_b64_tr_b16 a[128:129], v233 offset:27648
v_mfma_f32_16x16x32_bf16 v[52:55], a[140:143], a[100:103], v[52:55]
ds_read_b64_tr_b16 a[130:131], v233 offset:32256
v_mfma_f32_16x16x32_bf16 v[56:59], a[140:143], a[104:107], v[56:59]
v_mfma_f32_16x16x32_bf16 v[60:63], a[140:143], a[108:111], v[60:63]
s_waitcnt lgkmcnt(10)
v_mfma_f32_16x16x32_bf16 v[64:67], a[144:147], a[96:99], v[64:67]
ds_read_b64_tr_b16 a[132:133], v233 offset:27680
v_mfma_f32_16x16x32_bf16 v[68:71], a[144:147], a[100:103], v[68:71]
ds_read_b64_tr_b16 a[134:135], v233 offset:32288
s_add_u32 m0, m0, 1024
v_mfma_f32_16x16x32_bf16 v[72:75], a[144:147], a[104:107], v[72:75]
buffer_load_dwordx4 v247, s[40:43], s64 offen lds
v_mfma_f32_16x16x32_bf16 v[76:79], a[144:147], a[108:111], v[76:79]
buffer_load_dwordx4 a[24:27], v235, s[44:47], s71 offen offset:2048 sc1
s_waitcnt lgkmcnt(10)
v_mfma_f32_16x16x32_bf16 v[80:83], a[148:151], a[96:99], v[80:83]
ds_read_b64_tr_b16 a[136:137], v233 offset:27712
v_mfma_f32_16x16x32_bf16 v[84:87], a[148:151], a[100:103], v[84:87]
ds_read_b64_tr_b16 a[138:139], v233 offset:32320
v_mfma_f32_16x16x32_bf16 v[88:91], a[148:151], a[104:107], v[88:91]
v_mfma_f32_16x16x32_bf16 v[92:95], a[148:151], a[108:111], v[92:95]
s_waitcnt lgkmcnt(10)
v_mfma_f32_16x16x32_bf16 v[96:99], a[152:155], a[96:99], v[96:99]
ds_read_b64_tr_b16 a[140:141], v233 offset:27744
v_mfma_f32_16x16x32_bf16 v[100:103], a[152:155], a[100:103], v[100:103]
ds_read_b64_tr_b16 a[142:143], v233 offset:32352
v_mfma_f32_16x16x32_bf16 v[104:107], a[152:155], a[104:107], v[104:107]
v_mfma_f32_16x16x32_bf16 v[108:111], a[152:155], a[108:111], v[108:111]
buffer_load_dwordx4 a[28:31], v235, s[44:47], s71 offen offset:3072 sc1
s_add_u32 s71, s71, 4096
s_waitcnt lgkmcnt(10)
v_mfma_f32_16x16x32_bf16 v[112:115], a[156:159], a[96:99], v[112:115]
ds_read_b64_tr_b16 a[144:145], v233 offset:27776
v_mfma_f32_16x16x32_bf16 v[116:119], a[156:159], a[100:103], v[116:119]
ds_read_b64_tr_b16 a[146:147], v233 offset:32384
s_add_u32 m0, m0, 1024
v_mfma_f32_16x16x32_bf16 v[120:123], a[156:159], a[104:107], v[120:123]
buffer_load_dwordx4 v248, s[40:43], s64 offen lds
v_mfma_f32_16x16x32_bf16 v[124:127], a[156:159], a[108:111], v[124:127]
s_waitcnt lgkmcnt(10)
v_mfma_f32_16x16x32_bf16 v[128:131], a[160:163], a[96:99], v[128:131]
ds_read_b64_tr_b16 a[148:149], v233 offset:27808
v_mfma_f32_16x16x32_bf16 v[132:135], a[160:163], a[100:103], v[132:135]
ds_read_b64_tr_b16 a[150:151], v233 offset:32416
v_mfma_f32_16x16x32_bf16 v[136:139], a[160:163], a[104:107], v[136:139]
v_mfma_f32_16x16x32_bf16 v[140:143], a[160:163], a[108:111], v[140:143]
s_waitcnt vmcnt(18) lgkmcnt(10)
v_mfma_f32_16x16x32_bf16 v[0:3], a[128:131], a[112:115], v[0:3]
ds_read_b64_tr_b16 a[152:153], v233 offset:27840
v_mfma_f32_16x16x32_bf16 v[4:7], a[128:131], a[116:119], v[4:7]
ds_read_b64_tr_b16 a[154:155], v233 offset:32448
v_mfma_f32_16x16x32_bf16 v[8:11], a[128:131], a[120:123], v[8:11]
v_mfma_f32_16x16x32_bf16 v[12:15], a[128:131], a[124:127], v[12:15]
s_add_u32 s64, s64, s65
s_add_u32 s69, s69, 36864
s_cmp_eq_u32 s69, 147456
s_cselect_b32 s69, 0, s69
buffer_load_dwordx4 a[32:35], v235, s[44:47], s71 offen offset:0 sc1
s_waitcnt lgkmcnt(10)
v_mfma_f32_16x16x32_bf16 v[16:19], a[132:135], a[112:115], v[16:19]
ds_read_b64_tr_b16 a[156:157], v233 offset:27872
v_mfma_f32_16x16x32_bf16 v[20:23], a[132:135], a[116:119], v[20:23]
ds_read_b64_tr_b16 a[158:159], v233 offset:32480
v_mfma_f32_16x16x32_bf16 v[24:27], a[132:135], a[120:123], v[24:27]
v_mfma_f32_16x16x32_bf16 v[28:31], a[132:135], a[124:127], v[28:31]
s_waitcnt lgkmcnt(10)
v_mfma_f32_16x16x32_bf16 v[32:35], a[136:139], a[112:115], v[32:35]
ds_read_b64_tr_b16 a[160:161], v233 offset:27904
v_mfma_f32_16x16x32_bf16 v[36:39], a[136:139], a[116:119], v[36:39]
ds_read_b64_tr_b16 a[162:163], v233 offset:32512
v_mfma_f32_16x16x32_bf16 v[40:43], a[136:139], a[120:123], v[40:43]
v_mfma_f32_16x16x32_bf16 v[44:47], a[136:139], a[124:127], v[44:47]
buffer_load_dwordx4 a[36:39], v235, s[44:47], s71 offen offset:1024 sc1
s_waitcnt lgkmcnt(10)
v_mfma_f32_16x16x32_bf16 v[48:51], a[140:143], a[112:115], v[48:51]
s_add_u32 s67, s67, 36864
s_cmp_eq_u32 s67, 147456
s_cselect_b32 s66, s76, 36864
s_cselect_b32 s67, 0, s67
v_mfma_f32_16x16x32_bf16 v[52:55], a[140:143], a[116:119], v[52:55]
v_mfma_f32_16x16x32_bf16 v[56:59], a[140:143], a[120:123], v[56:59]
v_mfma_f32_16x16x32_bf16 v[60:63], a[140:143], a[124:127], v[60:63]
s_waitcnt lgkmcnt(8)
v_mfma_f32_16x16x32_bf16 v[64:67], a[144:147], a[112:115], v[64:67]
v_add_u32 v233, s66, v233
v_mfma_f32_16x16x32_bf16 v[68:71], a[144:147], a[116:119], v[68:71]
v_mfma_f32_16x16x32_bf16 v[72:75], a[144:147], a[120:123], v[72:75]
v_mfma_f32_16x16x32_bf16 v[76:79], a[144:147], a[124:127], v[76:79]
buffer_load_dwordx4 a[40:43], v235, s[44:47], s71 offen offset:2048 sc1
s_waitcnt vmcnt(63) lgkmcnt(0)
s_barrier
v_mfma_f32_16x16x32_bf16 v[80:83], a[148:151], a[112:115], v[80:83]
ds_read_b64_tr_b16 a[128:129], v233 offset:0
ds_read_b64_tr_b16 a[130:131], v233 offset:4608
v_mfma_f32_16x16x32_bf16 v[84:87], a[148:151], a[116:119], v[84:87]
ds_read_b64_tr_b16 a[132:133], v233 offset:32
ds_read_b64_tr_b16 a[134:135], v233 offset:4640
v_mfma_f32_16x16x32_bf16 v[88:91], a[148:151], a[120:123], v[88:91]
v_mfma_f32_16x16x32_bf16 v[92:95], a[148:151], a[124:127], v[92:95]
s_waitcnt lgkmcnt(8)
v_mfma_f32_16x16x32_bf16 v[96:99], a[152:155], a[112:115], v[96:99]
ds_read_b64_tr_b16 a[136:137], v233 offset:64
ds_read_b64_tr_b16 a[138:139], v233 offset:4672
v_mfma_f32_16x16x32_bf16 v[100:103], a[152:155], a[116:119], v[100:103]
ds_read_b64_tr_b16 a[140:141], v233 offset:96
ds_read_b64_tr_b16 a[142:143], v233 offset:4704
v_mfma_f32_16x16x32_bf16 v[104:107], a[152:155], a[120:123], v[104:107]
v_mfma_f32_16x16x32_bf16 v[108:111], a[152:155], a[124:127], v[108:111]
buffer_load_dwordx4 a[44:47], v235, s[44:47], s71 offen offset:3072 sc1
s_add_u32 s71, s71, 4096
s_waitcnt lgkmcnt(10)
v_mfma_f32_16x16x32_bf16 v[112:115], a[156:159], a[112:115], v[112:115]
ds_read_b64_tr_b16 a[144:145], v233 offset:128
v_mfma_f32_16x16x32_bf16 v[116:119], a[156:159], a[116:119], v[116:119]
ds_read_b64_tr_b16 a[146:147], v233 offset:4736
v_mfma_f32_16x16x32_bf16 v[120:123], a[156:159], a[120:123], v[120:123]
v_mfma_f32_16x16x32_bf16 v[124:127], a[156:159], a[124:127], v[124:127]
s_waitcnt lgkmcnt(10)
v_mfma_f32_16x16x32_bf16 v[128:131], a[160:163], a[112:115], v[128:131]
ds_read_b64_tr_b16 a[148:149], v233 offset:160
v_mfma_f32_16x16x32_bf16 v[132:135], a[160:163], a[116:119], v[132:135]
ds_read_b64_tr_b16 a[150:151], v233 offset:4768
v_mfma_f32_16x16x32_bf16 v[136:139], a[160:163], a[120:123], v[136:139]
v_mfma_f32_16x16x32_bf16 v[140:143], a[160:163], a[124:127], v[140:143]
G2:
s_sub_u32 s64, s64, s79
s_waitcnt vmcnt(12) lgkmcnt(10)
v_mfma_f32_16x16x32_bf16 v[0:3], a[128:131], a[0:3], v[0:3]
ds_read_b64_tr_b16 a[152:153], v233 offset:192
v_mfma_f32_16x16x32_bf16 v[4:7], a[128:131], a[4:7], v[4:7]
ds_read_b64_tr_b16 a[154:155], v233 offset:4800
v_mfma_f32_16x16x32_bf16 v[8:11], a[128:131], a[8:11], v[8:11]
v_mfma_f32_16x16x32_bf16 v[12:15], a[128:131], a[12:15], v[12:15]
buffer_load_dwordx4 a[48:51], v235, s[44:47], s71 offen offset:0 sc1
s_waitcnt lgkmcnt(10)
v_mfma_f32_16x16x32_bf16 v[16:19], a[132:135], a[0:3], v[16:19]
ds_read_b64_tr_b16 a[156:157], v233 offset:224
v_mfma_f32_16x16x32_bf16 v[20:23], a[132:135], a[4:7], v[20:23]
ds_read_b64_tr_b16 a[158:159], v233 offset:4832
s_add_u32 m0, s70, s69
v_mfma_f32_16x16x32_bf16 v[24:27], a[132:135], a[8:11], v[24:27]
buffer_load_dwordx4 v240, s[40:43], s64 offen lds
v_mfma_f32_16x16x32_bf16 v[28:31], a[132:135], a[12:15], v[28:31]
s_waitcnt lgkmcnt(10)
v_mfma_f32_16x16x32_bf16 v[32:35], a[136:139], a[0:3], v[32:35]
ds_read_b64_tr_b16 a[160:161], v233 offset:256
v_mfma_f32_16x16x32_bf16 v[36:39], a[136:139], a[4:7], v[36:39]
ds_read_b64_tr_b16 a[162:163], v233 offset:4864
v_mfma_f32_16x16x32_bf16 v[40:43], a[136:139], a[8:11], v[40:43]
v_mfma_f32_16x16x32_bf16 v[44:47], a[136:139], a[12:15], v[44:47]
buffer_load_dwordx4 a[52:55], v235, s[44:47], s71 offen offset:1024 sc1
s_waitcnt lgkmcnt(10)
v_mfma_f32_16x16x32_bf16 v[48:51], a[140:143], a[0:3], v[48:51]
ds_read_b64_tr_b16 a[128:129], v233 offset:9216
v_mfma_f32_16x16x32_bf16 v[52:55], a[140:143], a[4:7], v[52:55]
ds_read_b64_tr_b16 a[130:131], v233 offset:13824
v_mfma_f32_16x16x32_bf16 v[56:59], a[140:143], a[8:11], v[56:59]
v_mfma_f32_16x16x32_bf16 v[60:63], a[140:143], a[12:15], v[60:63]
s_waitcnt lgkmcnt(10)
v_mfma_f32_16x16x32_bf16 v[64:67], a[144:147], a[0:3], v[64:67]
ds_read_b64_tr_b16 a[132:133], v233 offset:9248
v_mfma_f32_16x16x32_bf16 v[68:71], a[144:147], a[4:7], v[68:71]
ds_read_b64_tr_b16 a[134:135], v233 offset:13856
s_add_u32 m0, m0, 1024
v_mfma_f32_16x16x32_bf16 v[72:75], a[144:147], a[8:11], v[72:75]
buffer_load_dwordx4 v241, s[40:43], s64 offen lds
v_mfma_f32_16x16x32_bf16 v[76:79], a[144:147], a[12:15], v[76:79]
buffer_load_dwordx4 a[56:59], v235, s[44:47], s71 offen offset:2048 sc1
s_waitcnt lgkmcnt(10)
v_mfma_f32_16x16x32_bf16 v[80:83], a[148:151], a[0:3], v[80:83]
ds_read_b64_tr_b16 a[136:137], v233 offset:9280
v_mfma_f32_16x16x32_bf16 v[84:87], a[148:151], a[4:7], v[84:87]
ds_read_b64_tr_b16 a[138:139], v233 offset:13888
v_mfma_f32_16x16x32_bf16 v[88:91], a[148:151], a[8:11], v[88:91]
v_mfma_f32_16x16x32_bf16 v[92:95], a[148:151], a[12:15], v[92:95]
s_waitcnt lgkmcnt(10)
v_mfma_f32_16x16x32_bf16 v[96:99], a[152:155], a[0:3], v[96:99]
ds_read_b64_tr_b16 a[140:141], v233 offset:9312
v_mfma_f32_16x16x32_bf16 v[100:103], a[152:155], a[4:7], v[100:103]
ds_read_b64_tr_b16 a[142:143], v233 offset:13920
v_mfma_f32_16x16x32_bf16 v[104:107], a[152:155], a[8:11], v[104:107]
v_mfma_f32_16x16x32_bf16 v[108:111], a[152:155], a[12:15], v[108:111]
buffer_load_dwordx4 a[60:63], v235, s[44:47], s71 offen offset:3072 sc1
s_add_u32 s71, s71, 4096
s_waitcnt lgkmcnt(10)
v_mfma_f32_16x16x32_bf16 v[112:115], a[156:159], a[0:3], v[112:115]
ds_read_b64_tr_b16 a[144:145], v233 offset:9344
v_mfma_f32_16x16x32_bf16 v[116:119], a[156:159], a[4:7], v[116:119]
ds_read_b64_tr_b16 a[146:147], v233 offset:13952
s_add_u32 m0, m0, 1024
v_mfma_f32_16x16x32_bf16 v[120:123], a[156:159], a[8:11], v[120:123]
buffer_load_dwordx4 v242, s[40:43], s64 offen lds
v_mfma_f32_16x16x32_bf16 v[124:127], a[156:159], a[12:15], v[124:127]
s_waitcnt lgkmcnt(10)
v_mfma_f32_16x16x32_bf16 v[128:131], a[160:163], a[0:3], v[128:131]
ds_read_b64_tr_b16 a[148:149], v233 offset:9376
v_mfma_f32_16x16x32_bf16 v[132:135], a[160:163], a[4:7], v[132:135]
ds_read_b64_tr_b16 a[150:151], v233 offset:13984
v_mfma_f32_16x16x32_bf16 v[136:139], a[160:163], a[8:11], v[136:139]
v_mfma_f32_16x16x32_bf16 v[140:143], a[160:163], a[12:15], v[140:143]
s_waitcnt vmcnt(12) lgkmcnt(10)
v_mfma_f32_16x16x32_bf16 v[0:3], a[128:131], a[16:19], v[0:3]
ds_read_b64_tr_b16 a[152:153], v233 offset:9408
v_mfma_f32_16x16x32_bf16 v[4:7], a[128:131], a[20:23], v[4:7]
ds_read_b64_tr_b16 a[154:155], v233 offset:14016
v_mfma_f32_16x16x32_bf16 v[8:11], a[128:131], a[24:27], v[8:11]
v_mfma_f32_16x16x32_bf16 v[12:15], a[128:131], a[28:31], v[12:15]
buffer_load_dwordx4 a[64:67], v235, s[44:47], s71 offen offset:0 sc1
s_waitcnt lgkmcnt(10)
v_mfma_f32_16x16x32_bf16 v[16:19], a[132:135], a[16:19], v[16:19]
ds_read_b64_tr_b16 a[156:157], v233 offset:9440
v_mfma_f32_16x16x32_bf16 v[20:23], a[132:135], a[20:23], v[20:23]
ds_read_b64_tr_b16 a[158:159], v233 offset:14048
s_add_u32 m0, m0, 1024
v_mfma_f32_16x16x32_bf16 v[24:27], a[132:135], a[24:27], v[24:27]
buffer_load_dwordx4 v243, s[40:43], s64 offen lds
v_mfma_f32_16x16x32_bf16 v[28:31], a[132:135], a[28:31], v[28:31]
s_waitcnt lgkmcnt(10)
v_mfma_f32_16x16x32_bf16 v[32:35], a[136:139], a[16:19], v[32:35]
ds_read_b64_tr_b16 a[160:161], v233 offset:9472
v_mfma_f32_16x16x32_bf16 v[36:39], a[136:139], a[20:23], v[36:39]
ds_read_b64_tr_b16 a[162:163], v233 offset:14080
v_mfma_f32_16x16x32_bf16 v[40:43], a[136:139], a[24:27], v[40:43]
v_mfma_f32_16x16x32_bf16 v[44:47], a[136:139], a[28:31], v[44:47]
buffer_load_dwordx4 a[68:71], v235, s[44:47], s71 offen offset:1024 sc1
s_waitcnt lgkmcnt(10)
v_mfma_f32_16x16x32_bf16 v[48:51], a[140:143], a[16:19], v[48:51]
ds_read_b64_tr_b16 a[128:129], v233 offset:18432
v_mfma_f32_16x16x32_bf16 v[52:55], a[140:143], a[20:23], v[52:55]
ds_read_b64_tr_b16 a[130:131], v233 offset:23040
v_mfma_f32_16x16x32_bf16 v[56:59], a[140:143], a[24:27], v[56:59]
v_mfma_f32_16x16x32_bf16 v[60:63], a[140:143], a[28:31], v[60:63]
s_waitcnt lgkmcnt(10)
v_mfma_f32_16x16x32_bf16 v[64:67], a[144:147], a[16:19], v[64:67]
ds_read_b64_tr_b16 a[132:133], v233 offset:18464
v_mfma_f32_16x16x32_bf16 v[68:71], a[144:147], a[20:23], v[68:71]
ds_read_b64_tr_b16 a[134:135], v233 offset:23072
s_add_u32 m0, m0, 1024
v_mfma_f32_16x16x32_bf16 v[72:75], a[144:147], a[24:27], v[72:75]
buffer_load_dwordx4 v244, s[40:43], s64 offen lds
v_mfma_f32_16x16x32_bf16 v[76:79], a[144:147], a[28:31], v[76:79]
buffer_load_dwordx4 a[72:75], v235, s[44:47], s71 offen offset:2048 sc1
s_waitcnt lgkmcnt(10)
v_mfma_f32_16x16x32_bf16 v[80:83], a[148:151], a[16:19], v[80:83]
ds_read_b64_tr_b16 a[136:137], v233 offset:18496
v_mfma_f32_16x16x32_bf16 v[84:87], a[148:151], a[20:23], v[84:87]
ds_read_b64_tr_b16 a[138:139], v233 offset:23104
v_mfma_f32_16x16x32_bf16 v[88:91], a[148:151], a[24:27], v[88:91]
)"
R"(
v_mfma_f32_16x16x32_bf16 v[92:95], a[148:151], a[28:31], v[92:95]
s_waitcnt lgkmcnt(10)
v_mfma_f32_16x16x32_bf16 v[96:99], a[152:155], a[16:19], v[96:99]
ds_read_b64_tr_b16 a[140:141], v233 offset:18528
v_mfma_f32_16x16x32_bf16 v[100:103], a[152:155], a[20:23], v[100:103]
ds_read_b64_tr_b16 a[142:143], v233 offset:23136
v_mfma_f32_16x16x32_bf16 v[104:107], a[152:155], a[24:27], v[104:107]
v_mfma_f32_16x16x32_bf16 v[108:111], a[152:155], a[28:31], v[108:111]
buffer_load_dwordx4 a[76:79], v235, s[44:47], s71 offen offset:3072 sc1
s_add_u32 s71, s71, 4096
s_waitcnt lgkmcnt(10)
v_mfma_f32_16x16x32_bf16 v[112:115], a[156:159], a[16:19], v[112:115]
ds_read_b64_tr_b16 a[144:145], v233 offset:18560
v_mfma_f32_16x16x32_bf16 v[116:119], a[156:159], a[20:23], v[116:119]
ds_read_b64_tr_b16 a[146:147], v233 offset:23168
s_add_u32 m0, m0, 1024
v_mfma_f32_16x16x32_bf16 v[120:123], a[156:159], a[24:27], v[120:123]
buffer_load_dwordx4 v245, s[40:43], s64 offen lds
v_mfma_f32_16x16x32_bf16 v[124:127], a[156:159], a[28:31], v[124:127]
s_waitcnt lgkmcnt(10)
v_mfma_f32_16x16x32_bf16 v[128:131], a[160:163], a[16:19], v[128:131]
ds_read_b64_tr_b16 a[148:149], v233 offset:18592
v_mfma_f32_16x16x32_bf16 v[132:135], a[160:163], a[20:23], v[132:135]
ds_read_b64_tr_b16 a[150:151], v233 offset:23200
v_mfma_f32_16x16x32_bf16 v[136:139], a[160:163], a[24:27], v[136:139]
v_mfma_f32_16x16x32_bf16 v[140:143], a[160:163], a[28:31], v[140:143]
s_waitcnt vmcnt(14) lgkmcnt(10)
v_mfma_f32_16x16x32_bf16 v[0:3], a[128:131], a[32:35], v[0:3]
ds_read_b64_tr_b16 a[152:153], v233 offset:18624
v_mfma_f32_16x16x32_bf16 v[4:7], a[128:131], a[36:39], v[4:7]
ds_read_b64_tr_b16 a[154:155], v233 offset:23232
v_mfma_f32_16x16x32_bf16 v[8:11], a[128:131], a[40:43], v[8:11]
v_mfma_f32_16x16x32_bf16 v[12:15], a[128:131], a[44:47], v[12:15]
buffer_load_dwordx4 a[80:83], v235, s[44:47], s71 offen offset:0 sc1
s_waitcnt lgkmcnt(10)
v_mfma_f32_16x16x32_bf16 v[16:19], a[132:135], a[32:35], v[16:19]
ds_read_b64_tr_b16 a[156:157], v233 offset:18656
v_mfma_f32_16x16x32_bf16 v[20:23], a[132:135], a[36:39], v[20:23]
ds_read_b64_tr_b16 a[158:159], v233 offset:23264
s_add_u32 m0, m0, 1024
v_mfma_f32_16x16x32_bf16 v[24:27], a[132:135], a[40:43], v[24:27]
buffer_load_dwordx4 v246, s[40:43], s64 offen lds
v_mfma_f32_16x16x32_bf16 v[28:31], a[132:135], a[44:47], v[28:31]
buffer_load_dwordx4 a[84:87], v235, s[44:47], s71 offen offset:1024 sc1
s_waitcnt lgkmcnt(10)
v_mfma_f32_16x16x32_bf16 v[32:35], a[136:139], a[32:35], v[32:35]
ds_read_b64_tr_b16 a[160:161], v233 offset:18688
v_mfma_f32_16x16x32_bf16 v[36:39], a[136:139], a[36:39], v[36:39]
ds_read_b64_tr_b16 a[162:163], v233 offset:23296
v_mfma_f32_16x16x32_bf16 v[40:43], a[136:139], a[40:43], v[40:43]
s_waitcnt vmcnt(19)
s_add_u32 s77, s39, 3072
v_mov_b32 v238, s82
v_mov_b32 v239, s83
s_mov_b64 exec, 1
buffer_store_dwordx2 v[238:239], off, s[52:55], s77 offset:0 sc1
s_mov_b64 exec, -1
s_nop 1
v_mfma_f32_16x16x32_bf16 v[44:47], a[136:139], a[44:47], v[44:47]
buffer_load_dwordx4 a[88:91], v235, s[44:47], s71 offen offset:2048 sc1
s_waitcnt lgkmcnt(10)
v_mfma_f32_16x16x32_bf16 v[48:51], a[140:143], a[32:35], v[48:51]
ds_read_b64_tr_b16 a[128:129], v233 offset:27648
v_mfma_f32_16x16x32_bf16 v[52:55], a[140:143], a[36:39], v[52:55]
ds_read_b64_tr_b16 a[130:131], v233 offset:32256
v_mfma_f32_16x16x32_bf16 v[56:59], a[140:143], a[40:43], v[56:59]
v_mfma_f32_16x16x32_bf16 v[60:63], a[140:143], a[44:47], v[60:63]
buffer_load_dwordx4 a[92:95], v235, s[44:47], s71 offen offset:3072 sc1
s_add_u32 s71, s71, 4096
s_waitcnt lgkmcnt(10)
v_mfma_f32_16x16x32_bf16 v[64:67], a[144:147], a[32:35], v[64:67]
ds_read_b64_tr_b16 a[132:133], v233 offset:27680
v_mfma_f32_16x16x32_bf16 v[68:71], a[144:147], a[36:39], v[68:71]
ds_read_b64_tr_b16 a[134:135], v233 offset:32288
s_add_u32 m0, m0, 1024
v_mfma_f32_16x16x32_bf16 v[72:75], a[144:147], a[40:43], v[72:75]
buffer_load_dwordx4 v247, s[40:43], s64 offen lds
v_mfma_f32_16x16x32_bf16 v[76:79], a[144:147], a[44:47], v[76:79]
buffer_load_dwordx4 a[96:99], v235, s[44:47], s71 offen offset:0 sc1
s_waitcnt lgkmcnt(10)
v_mfma_f32_16x16x32_bf16 v[80:83], a[148:151], a[32:35], v[80:83]
ds_read_b64_tr_b16 a[136:137], v233 offset:27712
v_mfma_f32_16x16x32_bf16 v[84:87], a[148:151], a[36:39], v[84:87]
ds_read_b64_tr_b16 a[138:139], v233 offset:32320
v_mfma_f32_16x16x32_bf16 v[88:91], a[148:151], a[40:43], v[88:91]
v_mfma_f32_16x16x32_bf16 v[92:95], a[148:151], a[44:47], v[92:95]
buffer_load_dwordx4 a[100:103], v235, s[44:47], s71 offen offset:1024 sc1
s_waitcnt lgkmcnt(10)
v_mfma_f32_16x16x32_bf16 v[96:99], a[152:155], a[32:35], v[96:99]
ds_read_b64_tr_b16 a[140:141], v233 offset:27744
v_mfma_f32_16x16x32_bf16 v[100:103], a[152:155], a[36:39], v[100:103]
ds_read_b64_tr_b16 a[142:143], v233 offset:32352
v_mfma_f32_16x16x32_bf16 v[104:107], a[152:155], a[40:43], v[104:107]
v_mfma_f32_16x16x32_bf16 v[108:111], a[152:155], a[44:47], v[108:111]
buffer_load_dwordx4 a[104:107], v235, s[44:47], s71 offen offset:2048 sc1
s_waitcnt lgkmcnt(10)
v_mfma_f32_16x16x32_bf16 v[112:115], a[156:159], a[32:35], v[112:115]
ds_read_b64_tr_b16 a[144:145], v233 offset:27776
v_mfma_f32_16x16x32_bf16 v[116:119], a[156:159], a[36:39], v[116:119]
ds_read_b64_tr_b16 a[146:147], v233 offset:32384
s_add_u32 m0, m0, 1024
v_mfma_f32_16x16x32_bf16 v[120:123], a[156:159], a[40:43], v[120:123]
buffer_load_dwordx4 v248, s[40:43], s64 offen lds
v_mfma_f32_16x16x32_bf16 v[124:127], a[156:159], a[44:47], v[124:127]
buffer_load_dwordx4 a[108:111], v235, s[44:47], s71 offen offset:3072 sc1
s_add_u32 s71, s71, 4096
s_waitcnt lgkmcnt(10)
v_mfma_f32_16x16x32_bf16 v[128:131], a[160:163], a[32:35], v[128:131]
ds_read_b64_tr_b16 a[148:149], v233 offset:27808
v_mfma_f32_16x16x32_bf16 v[132:135], a[160:163], a[36:39], v[132:135]
ds_read_b64_tr_b16 a[150:151], v233 offset:32416
v_mfma_f32_16x16x32_bf16 v[136:139], a[160:163], a[40:43], v[136:139]
v_mfma_f32_16x16x32_bf16 v[140:143], a[160:163], a[44:47], v[140:143]
s_waitcnt vmcnt(20) lgkmcnt(10)
v_mfma_f32_16x16x32_bf16 v[0:3], a[128:131], a[48:51], v[0:3]
ds_read_b64_tr_b16 a[152:153], v233 offset:27840
v_mfma_f32_16x16x32_bf16 v[4:7], a[128:131], a[52:55], v[4:7]
ds_read_b64_tr_b16 a[154:155], v233 offset:32448
v_mfma_f32_16x16x32_bf16 v[8:11], a[128:131], a[56:59], v[8:11]
v_mfma_f32_16x16x32_bf16 v[12:15], a[128:131], a[60:63], v[12:15]
s_add_u32 s64, s64, s65
s_add_u32 s69, s69, 36864
s_cmp_eq_u32 s69, 147456
s_cselect_b32 s69, 0, s69
buffer_load_dwordx4 a[112:115], v235, s[44:47], s71 offen offset:0 sc1
s_waitcnt lgkmcnt(10)
v_mfma_f32_16x16x32_bf16 v[16:19], a[132:135], a[48:51], v[16:19]
ds_read_b64_tr_b16 a[156:157], v233 offset:27872
v_mfma_f32_16x16x32_bf16 v[20:23], a[132:135], a[52:55], v[20:23]
ds_read_b64_tr_b16 a[158:159], v233 offset:32480
v_mfma_f32_16x16x32_bf16 v[24:27], a[132:135], a[56:59], v[24:27]
v_mfma_f32_16x16x32_bf16 v[28:31], a[132:135], a[60:63], v[28:31]
s_waitcnt lgkmcnt(10)
v_mfma_f32_16x16x32_bf16 v[32:35], a[136:139], a[48:51], v[32:35]
ds_read_b64_tr_b16 a[160:161], v233 offset:27904
v_mfma_f32_16x16x32_bf16 v[36:39], a[136:139], a[52:55], v[36:39]
ds_read_b64_tr_b16 a[162:163], v233 offset:32512
v_mfma_f32_16x16x32_bf16 v[40:43], a[136:139], a[56:59], v[40:43]
v_mfma_f32_16x16x32_bf16 v[44:47], a[136:139], a[60:63], v[44:47]
buffer_load_dwordx4 a[116:119], v235, s[44:47], s71 offen offset:1024 sc1
s_waitcnt lgkmcnt(10)
v_mfma_f32_16x16x32_bf16 v[48:51], a[140:143], a[48:51], v[48:51]
s_add_u32 s67, s67, 36864
s_cmp_eq_u32 s67, 147456
s_cselect_b32 s66, s76, 36864
s_cselect_b32 s67, 0, s67
v_mfma_f32_16x16x32_bf16 v[52:55], a[140:143], a[52:55], v[52:55]
v_mfma_f32_16x16x32_bf16 v[56:59], a[140:143], a[56:59], v[56:59]
v_mfma_f32_16x16x32_bf16 v[60:63], a[140:143], a[60:63], v[60:63]
s_waitcnt lgkmcnt(8)
v_mfma_f32_16x16x32_bf16 v[64:67], a[144:147], a[48:51], v[64:67]
v_add_u32 v233, s66, v233
v_mfma_f32_16x16x32_bf16 v[68:71], a[144:147], a[52:55], v[68:71]
v_mfma_f32_16x16x32_bf16 v[72:75], a[144:147], a[56:59], v[72:75]
v_mfma_f32_16x16x32_bf16 v[76:79], a[144:147], a[60:63], v[76:79]
buffer_load_dwordx4 a[120:123], v235, s[44:47], s71 offen offset:2048 sc1
s_waitcnt vmcnt(63) lgkmcnt(0)
s_barrier
v_mfma_f32_16x16x32_bf16 v[80:83], a[148:151], a[48:51], v[80:83]
ds_read_b64_tr_b16 a[128:129], v233 offset:0
ds_read_b64_tr_b16 a[130:131], v233 offset:4608
v_mfma_f32_16x16x32_bf16 v[84:87], a[148:151], a[52:55], v[84:87]
ds_read_b64_tr_b16 a[132:133], v233 offset:32
ds_read_b64_tr_b16 a[134:135], v233 offset:4640
v_mfma_f32_16x16x32_bf16 v[88:91], a[148:151], a[56:59], v[88:91]
v_mfma_f32_16x16x32_bf16 v[92:95], a[148:151], a[60:63], v[92:95]
s_waitcnt lgkmcnt(8)
v_mfma_f32_16x16x32_bf16 v[96:99], a[152:155], a[48:51], v[96:99]
ds_read_b64_tr_b16 a[136:137], v233 offset:64
ds_read_b64_tr_b16 a[138:139], v233 offset:4672
v_mfma_f32_16x16x32_bf16 v[100:103], a[152:155], a[52:55], v[100:103]
ds_read_b64_tr_b16 a[140:141], v233 offset:96
ds_read_b64_tr_b16 a[142:143], v233 offset:4704
v_mfma_f32_16x16x32_bf16 v[104:107], a[152:155], a[56:59], v[104:107]
v_mfma_f32_16x16x32_bf16 v[108:111], a[152:155], a[60:63], v[108:111]
buffer_load_dwordx4 a[124:127], v235, s[44:47], s71 offen offset:3072 sc1
s_add_u32 s71, s71, 4096
s_waitcnt lgkmcnt(10)
v_mfma_f32_16x16x32_bf16 v[112:115], a[156:159], a[48:51], v[112:115]
ds_read_b64_tr_b16 a[144:145], v233 offset:128
v_mfma_f32_16x16x32_bf16 v[116:119], a[156:159], a[52:55], v[116:119]
ds_read_b64_tr_b16 a[146:147], v233 offset:4736
v_mfma_f32_16x16x32_bf16 v[120:123], a[156:159], a[56:59], v[120:123]
v_mfma_f32_16x16x32_bf16 v[124:127], a[156:159], a[60:63], v[124:127]
s_waitcnt lgkmcnt(10)
v_mfma_f32_16x16x32_bf16 v[128:131], a[160:163], a[48:51], v[128:131]
ds_read_b64_tr_b16 a[148:149], v233 offset:160
v_mfma_f32_16x16x32_bf16 v[132:135], a[160:163], a[52:55], v[132:135]
ds_read_b64_tr_b16 a[150:151], v233 offset:4768
v_mfma_f32_16x16x32_bf16 v[136:139], a[160:163], a[56:59], v[136:139]
v_mfma_f32_16x16x32_bf16 v[140:143], a[160:163], a[60:63], v[140:143]
s_waitcnt vmcnt(17) lgkmcnt(10)
v_mfma_f32_16x16x32_bf16 v[0:3], a[128:131], a[64:67], v[0:3]
ds_read_b64_tr_b16 a[152:153], v233 offset:192
v_mfma_f32_16x16x32_bf16 v[4:7], a[128:131], a[68:71], v[4:7]
ds_read_b64_tr_b16 a[154:155], v233 offset:4800
v_mfma_f32_16x16x32_bf16 v[8:11], a[128:131], a[72:75], v[8:11]
v_mfma_f32_16x16x32_bf16 v[12:15], a[128:131], a[76:79], v[12:15]
buffer_load_dwordx4 a[0:3], v235, s[44:47], s71 offen offset:0 sc1
s_waitcnt lgkmcnt(10)
v_mfma_f32_16x16x32_bf16 v[16:19], a[132:135], a[64:67], v[16:19]
ds_read_b64_tr_b16 a[156:157], v233 offset:224
v_mfma_f32_16x16x32_bf16 v[20:23], a[132:135], a[68:71], v[20:23]
ds_read_b64_tr_b16 a[158:159], v233 offset:4832
s_add_u32 m0, s70, s69
v_mfma_f32_16x16x32_bf16 v[24:27], a[132:135], a[72:75], v[24:27]
buffer_load_dwordx4 v240, s[40:43], s64 offen lds
v_mfma_f32_16x16x32_bf16 v[28:31], a[132:135], a[76:79], v[28:31]
s_waitcnt lgkmcnt(10)
v_mfma_f32_16x16x32_bf16 v[32:35], a[136:139], a[64:67], v[32:35]
ds_read_b64_tr_b16 a[160:161], v233 offset:256
v_mfma_f32_16x16x32_bf16 v[36:39], a[136:139], a[68:71], v[36:39]
ds_read_b64_tr_b16 a[162:163], v233 offset:4864
v_mfma_f32_16x16x32_bf16 v[40:43], a[136:139], a[72:75], v[40:43]
v_mfma_f32_16x16x32_bf16 v[44:47], a[136:139], a[76:79], v[44:47]
buffer_load_dwordx4 a[4:7], v235, s[44:47], s71 offen offset:1024 sc1
s_waitcnt lgkmcnt(10)
v_mfma_f32_16x16x32_bf16 v[48:51], a[140:143], a[64:67], v[48:51]
ds_read_b64_tr_b16 a[128:129], v233 offset:9216
v_mfma_f32_16x16x32_bf16 v[52:55], a[140:143], a[68:71], v[52:55]
ds_read_b64_tr_b16 a[130:131], v233 offset:13824
v_mfma_f32_16x16x32_bf16 v[56:59], a[140:143], a[72:75], v[56:59]
v_mfma_f32_16x16x32_bf16 v[60:63], a[140:143], a[76:79], v[60:63]
s_waitcnt lgkmcnt(10)
v_mfma_f32_16x16x32_bf16 v[64:67], a[144:147], a[64:67], v[64:67]
ds_read_b64_tr_b16 a[132:133], v233 offset:9248
v_mfma_f32_16x16x32_bf16 v[68:71], a[144:147], a[68:71], v[68:71]
ds_read_b64_tr_b16 a[134:135], v233 offset:13856
s_add_u32 m0, m0, 1024
v_mfma_f32_16x16x32_bf16 v[72:75], a[144:147], a[72:75], v[72:75]
buffer_load_dwordx4 v241, s[40:43], s64 offen lds
v_mfma_f32_16x16x32_bf16 v[76:79], a[144:147], a[76:79], v[76:79]
buffer_load_dwordx4 a[8:11], v235, s[44:47], s71 offen offset:2048 sc1
s_waitcnt lgkmcnt(10)
v_mfma_f32_16x16x32_bf16 v[80:83], a[148:151], a[64:67], v[80:83]
ds_read_b64_tr_b16 a[136:137], v233 offset:9280
v_mfma_f32_16x16x32_bf16 v[84:87], a[148:151], a[68:71], v[84:87]
ds_read_b64_tr_b16 a[138:139], v233 offset:13888
v_mfma_f32_16x16x32_bf16 v[88:91], a[148:151], a[72:75], v[88:91]
v_mfma_f32_16x16x32_bf16 v[92:95], a[148:151], a[76:79], v[92:95]
s_waitcnt lgkmcnt(10)
v_mfma_f32_16x16x32_bf16 v[96:99], a[152:155], a[64:67], v[96:99]
ds_read_b64_tr_b16 a[140:141], v233 offset:9312
v_mfma_f32_16x16x32_bf16 v[100:103], a[152:155], a[68:71], v[100:103]
ds_read_b64_tr_b16 a[142:143], v233 offset:13920
v_mfma_f32_16x16x32_bf16 v[104:107], a[152:155], a[72:75], v[104:107]
v_mfma_f32_16x16x32_bf16 v[108:111], a[152:155], a[76:79], v[108:111]
buffer_load_dwordx4 a[12:15], v235, s[44:47], s71 offen offset:3072 sc1
s_add_u32 s71, s71, 4096
s_waitcnt lgkmcnt(10)
v_mfma_f32_16x16x32_bf16 v[112:115], a[156:159], a[64:67], v[112:115]
ds_read_b64_tr_b16 a[144:145], v233 offset:9344
v_mfma_f32_16x16x32_bf16 v[116:119], a[156:159], a[68:71], v[116:119]
ds_read_b64_tr_b16 a[146:147], v233 offset:13952
s_add_u32 m0, m0, 1024
v_mfma_f32_16x16x32_bf16 v[120:123], a[156:159], a[72:75], v[120:123]
buffer_load_dwordx4 v242, s[40:43], s64 offen lds
v_mfma_f32_16x16x32_bf16 v[124:127], a[156:159], a[76:79], v[124:127]
s_waitcnt lgkmcnt(10)
v_mfma_f32_16x16x32_bf16 v[128:131], a[160:163], a[64:67], v[128:131]
ds_read_b64_tr_b16 a[148:149], v233 offset:9376
v_mfma_f32_16x16x32_bf16 v[132:135], a[160:163], a[68:71], v[132:135]
ds_read_b64_tr_b16 a[150:151], v233 offset:13984
v_mfma_f32_16x16x32_bf16 v[136:139], a[160:163], a[72:75], v[136:139]
buffer_load_dwordx2 v[250:251], v252, s[52:55], s92 offen offset:0 sc1
v_mfma_f32_16x16x32_bf16 v[140:143], a[160:163], a[76:79], v[140:143]
s_waitcnt vmcnt(18) lgkmcnt(10)
v_mfma_f32_16x16x32_bf16 v[0:3], a[128:131], a[80:83], v[0:3]
ds_read_b64_tr_b16 a[152:153], v233 offset:9408
v_mfma_f32_16x16x32_bf16 v[4:7], a[128:131], a[84:87], v[4:7]
ds_read_b64_tr_b16 a[154:155], v233 offset:14016
v_mfma_f32_16x16x32_bf16 v[8:11], a[128:131], a[88:91], v[8:11]
v_mfma_f32_16x16x32_bf16 v[12:15], a[128:131], a[92:95], v[12:15]
buffer_load_dwordx4 a[16:19], v235, s[44:47], s71 offen offset:0 sc1
s_waitcnt lgkmcnt(10)
v_mfma_f32_16x16x32_bf16 v[16:19], a[132:135], a[80:83], v[16:19]
ds_read_b64_tr_b16 a[156:157], v233 offset:9440
v_mfma_f32_16x16x32_bf16 v[20:23], a[132:135], a[84:87], v[20:23]
ds_read_b64_tr_b16 a[158:159], v233 offset:14048
s_add_u32 m0, m0, 1024
v_mfma_f32_16x16x32_bf16 v[24:27], a[132:135], a[88:91], v[24:27]
buffer_load_dwordx4 v243, s[40:43], s64 offen lds
v_mfma_f32_16x16x32_bf16 v[28:31], a[132:135], a[92:95], v[28:31]
s_waitcnt lgkmcnt(10)
v_mfma_f32_16x16x32_bf16 v[32:35], a[136:139], a[80:83], v[32:35]
ds_read_b64_tr_b16 a[160:161], v233 offset:9472
v_mfma_f32_16x16x32_bf16 v[36:39], a[136:139], a[84:87], v[36:39]
ds_read_b64_tr_b16 a[162:163], v233 offset:14080
v_mfma_f32_16x16x32_bf16 v[40:43], a[136:139], a[88:91], v[40:43]
v_mfma_f32_16x16x32_bf16 v[44:47], a[136:139], a[92:95], v[44:47]
buffer_load_dwordx4 a[20:23], v235, s[44:47], s71 offen offset:1024 sc1
s_waitcnt lgkmcnt(10)
v_mfma_f32_16x16x32_bf16 v[48:51], a[140:143], a[80:83], v[48:51]
ds_read_b64_tr_b16 a[128:129], v233 offset:18432
v_mfma_f32_16x16x32_bf16 v[52:55], a[140:143], a[84:87], v[52:55]
ds_read_b64_tr_b16 a[130:131], v233 offset:23040
v_mfma_f32_16x16x32_bf16 v[56:59], a[140:143], a[88:91], v[56:59]
v_mfma_f32_16x16x32_bf16 v[60:63], a[140:143], a[92:95], v[60:63]
s_waitcnt lgkmcnt(10)
v_mfma_f32_16x16x32_bf16 v[64:67], a[144:147], a[80:83], v[64:67]
ds_read_b64_tr_b16 a[132:133], v233 offset:18464
v_mfma_f32_16x16x32_bf16 v[68:71], a[144:147], a[84:87], v[68:71]
ds_read_b64_tr_b16 a[134:135], v233 offset:23072
s_add_u32 m0, m0, 1024
v_mfma_f32_16x16x32_bf16 v[72:75], a[144:147], a[88:91], v[72:75]
buffer_load_dwordx4 v244, s[40:43], s64 offen lds
v_mfma_f32_16x16x32_bf16 v[76:79], a[144:147], a[92:95], v[76:79]
buffer_load_dwordx4 a[24:27], v235, s[44:47], s71 offen offset:2048 sc1
s_waitcnt lgkmcnt(10)
v_mfma_f32_16x16x32_bf16 v[80:83], a[148:151], a[80:83], v[80:83]
ds_read_b64_tr_b16 a[136:137], v233 offset:18496
v_mfma_f32_16x16x32_bf16 v[84:87], a[148:151], a[84:87], v[84:87]
ds_read_b64_tr_b16 a[138:139], v233 offset:23104
v_mfma_f32_16x16x32_bf16 v[88:91], a[148:151], a[88:91], v[88:91]
v_mfma_f32_16x16x32_bf16 v[92:95], a[148:151], a[92:95], v[92:95]
s_waitcnt lgkmcnt(10)
v_mfma_f32_16x16x32_bf16 v[96:99], a[152:155], a[80:83], v[96:99]
ds_read_b64_tr_b16 a[140:141], v233 offset:18528
v_mfma_f32_16x16x32_bf16 v[100:103], a[152:155], a[84:87], v[100:103]
ds_read_b64_tr_b16 a[142:143], v233 offset:23136
v_mfma_f32_16x16x32_bf16 v[104:107], a[152:155], a[88:91], v[104:107]
v_mfma_f32_16x16x32_bf16 v[108:111], a[152:155], a[92:95], v[108:111]
buffer_load_dwordx4 a[28:31], v235, s[44:47], s71 offen offset:3072 sc1
s_add_u32 s71, s71, 4096
s_waitcnt lgkmcnt(10)
v_mfma_f32_16x16x32_bf16 v[112:115], a[156:159], a[80:83], v[112:115]
ds_read_b64_tr_b16 a[144:145], v233 offset:18560
v_mfma_f32_16x16x32_bf16 v[116:119], a[156:159], a[84:87], v[116:119]
ds_read_b64_tr_b16 a[146:147], v233 offset:23168
s_add_u32 m0, m0, 1024
v_mfma_f32_16x16x32_bf16 v[120:123], a[156:159], a[88:91], v[120:123]
buffer_load_dwordx4 v245, s[40:43], s64 offen lds
v_mfma_f32_16x16x32_bf16 v[124:127], a[156:159], a[92:95], v[124:127]
s_waitcnt lgkmcnt(10)
v_mfma_f32_16x16x32_bf16 v[128:131], a[160:163], a[80:83], v[128:131]
ds_read_b64_tr_b16 a[148:149], v233 offset:18592
v_mfma_f32_16x16x32_bf16 v[132:135], a[160:163], a[84:87], v[132:135]
ds_read_b64_tr_b16 a[150:151], v233 offset:23200
v_mfma_f32_16x16x32_bf16 v[136:139], a[160:163], a[88:91], v[136:139]
v_mfma_f32_16x16x32_bf16 v[140:143], a[160:163], a[92:95], v[140:143]
s_waitcnt vmcnt(19) lgkmcnt(10)
v_mfma_f32_16x16x32_bf16 v[0:3], a[128:131], a[96:99], v[0:3]
ds_read_b64_tr_b16 a[152:153], v233 offset:18624
v_mfma_f32_16x16x32_bf16 v[4:7], a[128:131], a[100:103], v[4:7]
ds_read_b64_tr_b16 a[154:155], v233 offset:23232
v_mfma_f32_16x16x32_bf16 v[8:11], a[128:131], a[104:107], v[8:11]
v_mfma_f32_16x16x32_bf16 v[12:15], a[128:131], a[108:111], v[12:15]
buffer_load_dwordx4 a[32:35], v235, s[44:47], s71 offen offset:0 sc1
s_waitcnt lgkmcnt(10)
v_mfma_f32_16x16x32_bf16 v[16:19], a[132:135], a[96:99], v[16:19]
ds_read_b64_tr_b16 a[156:157], v233 offset:18656
v_mfma_f32_16x16x32_bf16 v[20:23], a[132:135], a[100:103], v[20:23]
ds_read_b64_tr_b16 a[158:159], v233 offset:23264
s_add_u32 m0, m0, 1024
v_mfma_f32_16x16x32_bf16 v[24:27], a[132:135], a[104:107], v[24:27]
buffer_load_dwordx4 v246, s[40:43], s64 offen lds
v_mfma_f32_16x16x32_bf16 v[28:31], a[132:135], a[108:111], v[28:31]
s_waitcnt lgkmcnt(10)
v_mfma_f32_16x16x32_bf16 v[32:35], a[136:139], a[96:99], v[32:35]
ds_read_b64_tr_b16 a[160:161], v233 offset:18688
v_mfma_f32_16x16x32_bf16 v[36:39], a[136:139], a[100:103], v[36:39]
ds_read_b64_tr_b16 a[162:163], v233 offset:23296
v_mfma_f32_16x16x32_bf16 v[40:43], a[136:139], a[104:107], v[40:43]
v_mfma_f32_16x16x32_bf16 v[44:47], a[136:139], a[108:111], v[44:47]
buffer_load_dwordx4 a[36:39], v235, s[44:47], s71 offen offset:1024 sc1
s_waitcnt lgkmcnt(10)
v_mfma_f32_16x16x32_bf16 v[48:51], a[140:143], a[96:99], v[48:51]
ds_read_b64_tr_b16 a[128:129], v233 offset:27648
v_mfma_f32_16x16x32_bf16 v[52:55], a[140:143], a[100:103], v[52:55]
ds_read_b64_tr_b16 a[130:131], v233 offset:32256
v_mfma_f32_16x16x32_bf16 v[56:59], a[140:143], a[104:107], v[56:59]
v_mfma_f32_16x16x32_bf16 v[60:63], a[140:143], a[108:111], v[60:63]
s_waitcnt lgkmcnt(10)
v_mfma_f32_16x16x32_bf16 v[64:67], a[144:147], a[96:99], v[64:67]
ds_read_b64_tr_b16 a[132:133], v233 offset:27680
v_mfma_f32_16x16x32_bf16 v[68:71], a[144:147], a[100:103], v[68:71]
ds_read_b64_tr_b16 a[134:135], v233 offset:32288
s_add_u32 m0, m0, 1024
v_mfma_f32_16x16x32_bf16 v[72:75], a[144:147], a[104:107], v[72:75]
buffer_load_dwordx4 v247, s[40:43], s64 offen lds
v_mfma_f32_16x16x32_bf16 v[76:79], a[144:147], a[108:111], v[76:79]
buffer_load_dwordx4 a[40:43], v235, s[44:47], s71 offen offset:2048 sc1
s_waitcnt lgkmcnt(10)
v_mfma_f32_16x16x32_bf16 v[80:83], a[148:151], a[96:99], v[80:83]
ds_read_b64_tr_b16 a[136:137], v233 offset:27712
v_mfma_f32_16x16x32_bf16 v[84:87], a[148:151], a[100:103], v[84:87]
ds_read_b64_tr_b16 a[138:139], v233 offset:32320
v_mfma_f32_16x16x32_bf16 v[88:91], a[148:151], a[104:107], v[88:91]
s_waitcnt vmcnt(12)
v_cmp_ne_u64 vcc, s[82:83], v[250:251]
v_mfma_f32_16x16x32_bf16 v[92:95], a[148:151], a[108:111], v[92:95]
s_waitcnt lgkmcnt(10)
v_mfma_f32_16x16x32_bf16 v[96:99], a[152:155], a[96:99], v[96:99]
ds_read_b64_tr_b16 a[140:141], v233 offset:27744
v_mfma_f32_16x16x32_bf16 v[100:103], a[152:155], a[100:103], v[100:103]
ds_read_b64_tr_b16 a[142:143], v233 offset:32352
s_cmp_lg_u64 vcc, 0
v_mfma_f32_16x16x32_bf16 v[104:107], a[152:155], a[104:107], v[104:107]
s_cbranch_scc1 CK_O1
CK_O1_R:
)"
R"(
v_mfma_f32_16x16x32_bf16 v[108:111], a[152:155], a[108:111], v[108:111]
buffer_load_dwordx4 a[44:47], v235, s[44:47], s71 offen offset:3072 sc1
s_add_u32 s71, s71, 4096
s_waitcnt lgkmcnt(10)
v_mfma_f32_16x16x32_bf16 v[112:115], a[156:159], a[96:99], v[112:115]
ds_read_b64_tr_b16 a[144:145], v233 offset:27776
v_mfma_f32_16x16x32_bf16 v[116:119], a[156:159], a[100:103], v[116:119]
ds_read_b64_tr_b16 a[146:147], v233 offset:32384
s_add_u32 m0, m0, 1024
v_mfma_f32_16x16x32_bf16 v[120:123], a[156:159], a[104:107], v[120:123]
buffer_load_dwordx4 v248, s[40:43], s64 offen lds
v_mfma_f32_16x16x32_bf16 v[124:127], a[156:159], a[108:111], v[124:127]
s_waitcnt lgkmcnt(10)
v_mfma_f32_16x16x32_bf16 v[128:131], a[160:163], a[96:99], v[128:131]
ds_read_b64_tr_b16 a[148:149], v233 offset:27808
v_mfma_f32_16x16x32_bf16 v[132:135], a[160:163], a[100:103], v[132:135]
ds_read_b64_tr_b16 a[150:151], v233 offset:32416
v_mfma_f32_16x16x32_bf16 v[136:139], a[160:163], a[104:107], v[136:139]
v_mfma_f32_16x16x32_bf16 v[140:143], a[160:163], a[108:111], v[140:143]
s_waitcnt vmcnt(22) lgkmcnt(10)
v_mfma_f32_16x16x32_bf16 v[0:3], a[128:131], a[112:115], v[0:3]
ds_read_b64_tr_b16 a[152:153], v233 offset:27840
v_mfma_f32_16x16x32_bf16 v[4:7], a[128:131], a[116:119], v[4:7]
ds_read_b64_tr_b16 a[154:155], v233 offset:32448
v_mfma_f32_16x16x32_bf16 v[8:11], a[128:131], a[120:123], v[8:11]
v_mfma_f32_16x16x32_bf16 v[12:15], a[128:131], a[124:127], v[12:15]
s_add_u32 s64, s64, s65
s_add_u32 s69, s69, 36864
s_cmp_eq_u32 s69, 147456
s_cselect_b32 s69, 0, s69
buffer_load_dwordx4 a[48:51], v235, s[44:47], s71 offen offset:0 sc1
s_waitcnt lgkmcnt(10)
v_mfma_f32_16x16x32_bf16 v[16:19], a[132:135], a[112:115], v[16:19]
ds_read_b64_tr_b16 a[156:157], v233 offset:27872
v_mfma_f32_16x16x32_bf16 v[20:23], a[132:135], a[116:119], v[20:23]
ds_read_b64_tr_b16 a[158:159], v233 offset:32480
v_mfma_f32_16x16x32_bf16 v[24:27], a[132:135], a[120:123], v[24:27]
v_mfma_f32_16x16x32_bf16 v[28:31], a[132:135], a[124:127], v[28:31]
buffer_load_dwordx4 a[52:55], v235, s[44:47], s71 offen offset:1024 sc1
s_waitcnt lgkmcnt(10)
v_mfma_f32_16x16x32_bf16 v[32:35], a[136:139], a[112:115], v[32:35]
ds_read_b64_tr_b16 a[160:161], v233 offset:27904
v_mfma_f32_16x16x32_bf16 v[36:39], a[136:139], a[116:119], v[36:39]
ds_read_b64_tr_b16 a[162:163], v233 offset:32512
v_mfma_f32_16x16x32_bf16 v[40:43], a[136:139], a[120:123], v[40:43]
v_mfma_f32_16x16x32_bf16 v[44:47], a[136:139], a[124:127], v[44:47]
buffer_load_dwordx4 a[56:59], v235, s[44:47], s71 offen offset:2048 sc1
s_waitcnt lgkmcnt(10)
v_mfma_f32_16x16x32_bf16 v[48:51], a[140:143], a[112:115], v[48:51]
s_add_u32 s67, s67, 36864
s_cmp_eq_u32 s67, 147456
s_cselect_b32 s66, s76, 36864
s_cselect_b32 s67, 0, s67
v_mfma_f32_16x16x32_bf16 v[52:55], a[140:143], a[116:119], v[52:55]
v_mfma_f32_16x16x32_bf16 v[56:59], a[140:143], a[120:123], v[56:59]
v_mfma_f32_16x16x32_bf16 v[60:63], a[140:143], a[124:127], v[60:63]
buffer_load_dwordx4 a[60:63], v235, s[44:47], s71 offen offset:3072 sc1
s_add_u32 s71, s71, 4096
s_waitcnt lgkmcnt(8)
v_mfma_f32_16x16x32_bf16 v[64:67], a[144:147], a[112:115], v[64:67]
v_add_u32 v233, s66, v233
v_mfma_f32_16x16x32_bf16 v[68:71], a[144:147], a[116:119], v[68:71]
v_mfma_f32_16x16x32_bf16 v[72:75], a[144:147], a[120:123], v[72:75]
v_mfma_f32_16x16x32_bf16 v[76:79], a[144:147], a[124:127], v[76:79]
buffer_load_dwordx4 a[64:67], v235, s[44:47], s71 offen offset:0 sc1
s_waitcnt vmcnt(61) lgkmcnt(0)
s_barrier
v_mfma_f32_16x16x32_bf16 v[80:83], a[148:151], a[112:115], v[80:83]
ds_read_b64_tr_b16 a[128:129], v233 offset:0
ds_read_b64_tr_b16 a[130:131], v233 offset:4608
v_mfma_f32_16x16x32_bf16 v[84:87], a[148:151], a[116:119], v[84:87]
ds_read_b64_tr_b16 a[132:133], v233 offset:32
ds_read_b64_tr_b16 a[134:135], v233 offset:4640
v_mfma_f32_16x16x32_bf16 v[88:91], a[148:151], a[120:123], v[88:91]
v_mfma_f32_16x16x32_bf16 v[92:95], a[148:151], a[124:127], v[92:95]
buffer_load_dwordx4 a[68:71], v235, s[44:47], s71 offen offset:1024 sc1
s_waitcnt lgkmcnt(8)
v_mfma_f32_16x16x32_bf16 v[96:99], a[152:155], a[112:115], v[96:99]
ds_read_b64_tr_b16 a[136:137], v233 offset:64
ds_read_b64_tr_b16 a[138:139], v233 offset:4672
v_mfma_f32_16x16x32_bf16 v[100:103], a[152:155], a[116:119], v[100:103]
ds_read_b64_tr_b16 a[140:141], v233 offset:96
ds_read_b64_tr_b16 a[142:143], v233 offset:4704
v_mfma_f32_16x16x32_bf16 v[104:107], a[152:155], a[120:123], v[104:107]
v_mfma_f32_16x16x32_bf16 v[108:111], a[152:155], a[124:127], v[108:111]
buffer_load_dwordx4 a[72:75], v235, s[44:47], s71 offen offset:2048 sc1
s_waitcnt lgkmcnt(10)
v_mfma_f32_16x16x32_bf16 v[112:115], a[156:159], a[112:115], v[112:115]
ds_read_b64_tr_b16 a[144:145], v233 offset:128
v_mfma_f32_16x16x32_bf16 v[116:119], a[156:159], a[116:119], v[116:119]
ds_read_b64_tr_b16 a[146:147], v233 offset:4736
v_mfma_f32_16x16x32_bf16 v[120:123], a[156:159], a[120:123], v[120:123]
v_mfma_f32_16x16x32_bf16 v[124:127], a[156:159], a[124:127], v[124:127]
buffer_load_dwordx4 a[76:79], v235, s[44:47], s71 offen offset:3072 sc1
s_add_u32 s71, s71, 4096
s_waitcnt lgkmcnt(10)
v_mfma_f32_16x16x32_bf16 v[128:131], a[160:163], a[112:115], v[128:131]
ds_read_b64_tr_b16 a[148:149], v233 offset:160
v_mfma_f32_16x16x32_bf16 v[132:135], a[160:163], a[116:119], v[132:135]
ds_read_b64_tr_b16 a[150:151], v233 offset:4768
v_mfma_f32_16x16x32_bf16 v[136:139], a[160:163], a[120:123], v[136:139]
v_mfma_f32_16x16x32_bf16 v[140:143], a[160:163], a[124:127], v[140:143]
s_waitcnt vmcnt(24) lgkmcnt(10)
v_mfma_f32_16x16x32_bf16 v[0:3], a[128:131], a[0:3], v[0:3]
ds_read_b64_tr_b16 a[152:153], v233 offset:192
v_mfma_f32_16x16x32_bf16 v[4:7], a[128:131], a[4:7], v[4:7]
ds_read_b64_tr_b16 a[154:155], v233 offset:4800
v_mfma_f32_16x16x32_bf16 v[8:11], a[128:131], a[8:11], v[8:11]
v_mfma_f32_16x16x32_bf16 v[12:15], a[128:131], a[12:15], v[12:15]
buffer_load_dwordx4 a[80:83], v235, s[44:47], s71 offen offset:0 sc1
s_waitcnt lgkmcnt(10)
v_mfma_f32_16x16x32_bf16 v[16:19], a[132:135], a[0:3], v[16:19]
ds_read_b64_tr_b16 a[156:157], v233 offset:224
v_mfma_f32_16x16x32_bf16 v[20:23], a[132:135], a[4:7], v[20:23]
ds_read_b64_tr_b16 a[158:159], v233 offset:4832
s_add_u32 m0, s70, s69
v_mfma_f32_16x16x32_bf16 v[24:27], a[132:135], a[8:11], v[24:27]
buffer_load_dwordx4 v240, s[40:43], s64 offen lds
v_mfma_f32_16x16x32_bf16 v[28:31], a[132:135], a[12:15], v[28:31]
s_waitcnt lgkmcnt(10)
v_mfma_f32_16x16x32_bf16 v[32:35], a[136:139], a[0:3], v[32:35]
ds_read_b64_tr_b16 a[160:161], v233 offset:256
v_mfma_f32_16x16x32_bf16 v[36:39], a[136:139], a[4:7], v[36:39]
ds_read_b64_tr_b16 a[162:163], v233 offset:4864
v_mfma_f32_16x16x32_bf16 v[40:43], a[136:139], a[8:11], v[40:43]
v_mfma_f32_16x16x32_bf16 v[44:47], a[136:139], a[12:15], v[44:47]
buffer_load_dwordx4 a[84:87], v235, s[44:47], s71 offen offset:1024 sc1
s_waitcnt lgkmcnt(10)
v_mfma_f32_16x16x32_bf16 v[48:51], a[140:143], a[0:3], v[48:51]
ds_read_b64_tr_b16 a[128:129], v233 offset:9216
v_mfma_f32_16x16x32_bf16 v[52:55], a[140:143], a[4:7], v[52:55]
ds_read_b64_tr_b16 a[130:131], v233 offset:13824
v_mfma_f32_16x16x32_bf16 v[56:59], a[140:143], a[8:11], v[56:59]
v_mfma_f32_16x16x32_bf16 v[60:63], a[140:143], a[12:15], v[60:63]
s_waitcnt lgkmcnt(10)
v_mfma_f32_16x16x32_bf16 v[64:67], a[144:147], a[0:3], v[64:67]
ds_read_b64_tr_b16 a[132:133], v233 offset:9248
v_mfma_f32_16x16x32_bf16 v[68:71], a[144:147], a[4:7], v[68:71]
ds_read_b64_tr_b16 a[134:135], v233 offset:13856
s_add_u32 m0, m0, 1024
v_mfma_f32_16x16x32_bf16 v[72:75], a[144:147], a[8:11], v[72:75]
buffer_load_dwordx4 v241, s[40:43], s64 offen lds
v_mfma_f32_16x16x32_bf16 v[76:79], a[144:147], a[12:15], v[76:79]
buffer_load_dwordx4 a[88:91], v235, s[44:47], s71 offen offset:2048 sc1
s_waitcnt lgkmcnt(10)
v_mfma_f32_16x16x32_bf16 v[80:83], a[148:151], a[0:3], v[80:83]
ds_read_b64_tr_b16 a[136:137], v233 offset:9280
v_mfma_f32_16x16x32_bf16 v[84:87], a[148:151], a[4:7], v[84:87]
ds_read_b64_tr_b16 a[138:139], v233 offset:13888
v_mfma_f32_16x16x32_bf16 v[88:91], a[148:151], a[8:11], v[88:91]
v_mfma_f32_16x16x32_bf16 v[92:95], a[148:151], a[12:15], v[92:95]
s_waitcnt lgkmcnt(10)
v_mfma_f32_16x16x32_bf16 v[96:99], a[152:155], a[0:3], v[96:99]
ds_read_b64_tr_b16 a[140:141], v233 offset:9312
v_mfma_f32_16x16x32_bf16 v[100:103], a[152:155], a[4:7], v[100:103]
ds_read_b64_tr_b16 a[142:143], v233 offset:13920
v_mfma_f32_16x16x32_bf16 v[104:107], a[152:155], a[8:11], v[104:107]
v_mfma_f32_16x16x32_bf16 v[108:111], a[152:155], a[12:15], v[108:111]
buffer_load_dwordx4 a[92:95], v235, s[44:47], s71 offen offset:3072 sc1
s_add_u32 s71, s71, 4096
s_waitcnt lgkmcnt(10)
v_mfma_f32_16x16x32_bf16 v[112:115], a[156:159], a[0:3], v[112:115]
ds_read_b64_tr_b16 a[144:145], v233 offset:9344
v_mfma_f32_16x16x32_bf16 v[116:119], a[156:159], a[4:7], v[116:119]
ds_read_b64_tr_b16 a[146:147], v233 offset:13952
s_add_u32 m0, m0, 1024
v_mfma_f32_16x16x32_bf16 v[120:123], a[156:159], a[8:11], v[120:123]
buffer_load_dwordx4 v242, s[40:43], s64 offen lds
v_mfma_f32_16x16x32_bf16 v[124:127], a[156:159], a[12:15], v[124:127]
s_waitcnt lgkmcnt(10)
v_mfma_f32_16x16x32_bf16 v[128:131], a[160:163], a[0:3], v[128:131]
ds_read_b64_tr_b16 a[148:149], v233 offset:9376
v_mfma_f32_16x16x32_bf16 v[132:135], a[160:163], a[4:7], v[132:135]
ds_read_b64_tr_b16 a[150:151], v233 offset:13984
v_mfma_f32_16x16x32_bf16 v[136:139], a[160:163], a[8:11], v[136:139]
v_mfma_f32_16x16x32_bf16 v[140:143], a[160:163], a[12:15], v[140:143]
s_waitcnt vmcnt(23) lgkmcnt(10)
v_mfma_f32_16x16x32_bf16 v[0:3], a[128:131], a[16:19], v[0:3]
ds_read_b64_tr_b16 a[152:153], v233 offset:9408
v_mfma_f32_16x16x32_bf16 v[4:7], a[128:131], a[20:23], v[4:7]
ds_read_b64_tr_b16 a[154:155], v233 offset:14016
v_mfma_f32_16x16x32_bf16 v[8:11], a[128:131], a[24:27], v[8:11]
v_mfma_f32_16x16x32_bf16 v[12:15], a[128:131], a[28:31], v[12:15]
buffer_load_dwordx4 a[96:99], v235, s[44:47], s71 offen offset:0 sc1
s_waitcnt lgkmcnt(10)
v_mfma_f32_16x16x32_bf16 v[16:19], a[132:135], a[16:19], v[16:19]
ds_read_b64_tr_b16 a[156:157], v233 offset:9440
v_mfma_f32_16x16x32_bf16 v[20:23], a[132:135], a[20:23], v[20:23]
ds_read_b64_tr_b16 a[158:159], v233 offset:14048
s_add_u32 m0, m0, 1024
v_mfma_f32_16x16x32_bf16 v[24:27], a[132:135], a[24:27], v[24:27]
buffer_load_dwordx4 v243, s[40:43], s64 offen lds
v_mfma_f32_16x16x32_bf16 v[28:31], a[132:135], a[28:31], v[28:31]
s_waitcnt lgkmcnt(10)
v_mfma_f32_16x16x32_bf16 v[32:35], a[136:139], a[16:19], v[32:35]
ds_read_b64_tr_b16 a[160:161], v233 offset:9472
v_mfma_f32_16x16x32_bf16 v[36:39], a[136:139], a[20:23], v[36:39]
ds_read_b64_tr_b16 a[162:163], v233 offset:14080
v_mfma_f32_16x16x32_bf16 v[40:43], a[136:139], a[24:27], v[40:43]
v_mfma_f32_16x16x32_bf16 v[44:47], a[136:139], a[28:31], v[44:47]
buffer_load_dwordx4 a[100:103], v235, s[44:47], s71 offen offset:1024 sc1
s_waitcnt lgkmcnt(10)
v_mfma_f32_16x16x32_bf16 v[48:51], a[140:143], a[16:19], v[48:51]
ds_read_b64_tr_b16 a[128:129], v233 offset:18432
v_mfma_f32_16x16x32_bf16 v[52:55], a[140:143], a[20:23], v[52:55]
ds_read_b64_tr_b16 a[130:131], v233 offset:23040
v_mfma_f32_16x16x32_bf16 v[56:59], a[140:143], a[24:27], v[56:59]
v_mfma_f32_16x16x32_bf16 v[60:63], a[140:143], a[28:31], v[60:63]
s_waitcnt lgkmcnt(10)
v_mfma_f32_16x16x32_bf16 v[64:67], a[144:147], a[16:19], v[64:67]
ds_read_b64_tr_b16 a[132:133], v233 offset:18464
v_mfma_f32_16x16x32_bf16 v[68:71], a[144:147], a[20:23], v[68:71]
ds_read_b64_tr_b16 a[134:135], v233 offset:23072
s_add_u32 m0, m0, 1024
v_mfma_f32_16x16x32_bf16 v[72:75], a[144:147], a[24:27], v[72:75]
buffer_load_dwordx4 v244, s[40:43], s64 offen lds
v_mfma_f32_16x16x32_bf16 v[76:79], a[144:147], a[28:31], v[76:79]
buffer_load_dwordx4 a[104:107], v235, s[44:47], s71 offen offset:2048 sc1
s_waitcnt lgkmcnt(10)
v_mfma_f32_16x16x32_bf16 v[80:83], a[148:151], a[16:19], v[80:83]
ds_read_b64_tr_b16 a[136:137], v233 offset:18496
v_mfma_f32_16x16x32_bf16 v[84:87], a[148:151], a[20:23], v[84:87]
ds_read_b64_tr_b16 a[138:139], v233 offset:23104
v_mfma_f32_16x16x32_bf16 v[88:91], a[148:151], a[24:27], v[88:91]
v_mfma_f32_16x16x32_bf16 v[92:95], a[148:151], a[28:31], v[92:95]
s_waitcnt lgkmcnt(10)
v_mfma_f32_16x16x32_bf16 v[96:99], a[152:155], a[16:19], v[96:99]
ds_read_b64_tr_b16 a[140:141], v233 offset:18528
v_mfma_f32_16x16x32_bf16 v[100:103], a[152:155], a[20:23], v[100:103]
ds_read_b64_tr_b16 a[142:143], v233 offset:23136
v_mfma_f32_16x16x32_bf16 v[104:107], a[152:155], a[24:27], v[104:107]
v_mfma_f32_16x16x32_bf16 v[108:111], a[152:155], a[28:31], v[108:111]
buffer_load_dwordx4 a[108:111], v235, s[44:47], s71 offen offset:3072 sc1
s_add_u32 s71, s71, 4096
s_waitcnt lgkmcnt(10)
v_mfma_f32_16x16x32_bf16 v[112:115], a[156:159], a[16:19], v[112:115]
ds_read_b64_tr_b16 a[144:145], v233 offset:18560
v_mfma_f32_16x16x32_bf16 v[116:119], a[156:159], a[20:23], v[116:119]
ds_read_b64_tr_b16 a[146:147], v233 offset:23168
s_add_u32 m0, m0, 1024
v_mfma_f32_16x16x32_bf16 v[120:123], a[156:159], a[24:27], v[120:123]
buffer_load_dwordx4 v245, s[40:43], s64 offen lds
v_mfma_f32_16x16x32_bf16 v[124:127], a[156:159], a[28:31], v[124:127]
s_waitcnt lgkmcnt(10)
v_mfma_f32_16x16x32_bf16 v[128:131], a[160:163], a[16:19], v[128:131]
ds_read_b64_tr_b16 a[148:149], v233 offset:18592
v_mfma_f32_16x16x32_bf16 v[132:135], a[160:163], a[20:23], v[132:135]
ds_read_b64_tr_b16 a[150:151], v233 offset:23200
v_mfma_f32_16x16x32_bf16 v[136:139], a[160:163], a[24:27], v[136:139]
v_mfma_f32_16x16x32_bf16 v[140:143], a[160:163], a[28:31], v[140:143]
s_waitcnt vmcnt(23) lgkmcnt(10)
v_mfma_f32_16x16x32_bf16 v[0:3], a[128:131], a[32:35], v[0:3]
ds_read_b64_tr_b16 a[152:153], v233 offset:18624
v_mfma_f32_16x16x32_bf16 v[4:7], a[128:131], a[36:39], v[4:7]
ds_read_b64_tr_b16 a[154:155], v233 offset:23232
v_mfma_f32_16x16x32_bf16 v[8:11], a[128:131], a[40:43], v[8:11]
v_mfma_f32_16x16x32_bf16 v[12:15], a[128:131], a[44:47], v[12:15]
buffer_load_dwordx4 a[112:115], v235, s[44:47], s71 offen offset:0 sc1
s_waitcnt lgkmcnt(10)
v_mfma_f32_16x16x32_bf16 v[16:19], a[132:135], a[32:35], v[16:19]
ds_read_b64_tr_b16 a[156:157], v233 offset:18656
v_mfma_f32_16x16x32_bf16 v[20:23], a[132:135], a[36:39], v[20:23]
ds_read_b64_tr_b16 a[158:159], v233 offset:23264
s_add_u32 m0, m0, 1024
v_mfma_f32_16x16x32_bf16 v[24:27], a[132:135], a[40:43], v[24:27]
buffer_load_dwordx4 v246, s[40:43], s64 offen lds
v_mfma_f32_16x16x32_bf16 v[28:31], a[132:135], a[44:47], v[28:31]
s_waitcnt lgkmcnt(10)
v_mfma_f32_16x16x32_bf16 v[32:35], a[136:139], a[32:35], v[32:35]
ds_read_b64_tr_b16 a[160:161], v233 offset:18688
v_mfma_f32_16x16x32_bf16 v[36:39], a[136:139], a[36:39], v[36:39]
ds_read_b64_tr_b16 a[162:163], v233 offset:23296
v_mfma_f32_16x16x32_bf16 v[40:43], a[136:139], a[40:43], v[40:43]
v_mfma_f32_16x16x32_bf16 v[44:47], a[136:139], a[44:47], v[44:47]
buffer_load_dwordx4 a[116:119], v235, s[44:47], s71 offen offset:1024 sc1
s_waitcnt lgkmcnt(10)
v_mfma_f32_16x16x32_bf16 v[48:51], a[140:143], a[32:35], v[48:51]
ds_read_b64_tr_b16 a[128:129], v233 offset:27648
v_mfma_f32_16x16x32_bf16 v[52:55], a[140:143], a[36:39], v[52:55]
ds_read_b64_tr_b16 a[130:131], v233 offset:32256
v_mfma_f32_16x16x32_bf16 v[56:59], a[140:143], a[40:43], v[56:59]
v_mfma_f32_16x16x32_bf16 v[60:63], a[140:143], a[44:47], v[60:63]
s_waitcnt lgkmcnt(10)
v_mfma_f32_16x16x32_bf16 v[64:67], a[144:147], a[32:35], v[64:67]
ds_read_b64_tr_b16 a[132:133], v233 offset:27680
v_mfma_f32_16x16x32_bf16 v[68:71], a[144:147], a[36:39], v[68:71]
ds_read_b64_tr_b16 a[134:135], v233 offset:32288
s_add_u32 m0, m0, 1024
v_mfma_f32_16x16x32_bf16 v[72:75], a[144:147], a[40:43], v[72:75]
buffer_load_dwordx4 v247, s[40:43], s64 offen lds
v_mfma_f32_16x16x32_bf16 v[76:79], a[144:147], a[44:47], v[76:79]
buffer_load_dwordx4 a[120:123], v235, s[44:47], s71 offen offset:2048 sc1
s_waitcnt lgkmcnt(10)
v_mfma_f32_16x16x32_bf16 v[80:83], a[148:151], a[32:35], v[80:83]
ds_read_b64_tr_b16 a[136:137], v233 offset:27712
v_mfma_f32_16x16x32_bf16 v[84:87], a[148:151], a[36:39], v[84:87]
ds_read_b64_tr_b16 a[138:139], v233 offset:32320
v_mfma_f32_16x16x32_bf16 v[88:91], a[148:151], a[40:43], v[88:91]
v_mfma_f32_16x16x32_bf16 v[92:95], a[148:151], a[44:47], v[92:95]
s_waitcnt lgkmcnt(10)
v_mfma_f32_16x16x32_bf16 v[96:99], a[152:155], a[32:35], v[96:99]
ds_read_b64_tr_b16 a[140:141], v233 offset:27744
v_mfma_f32_16x16x32_bf16 v[100:103], a[152:155], a[36:39], v[100:103]
ds_read_b64_tr_b16 a[142:143], v233 offset:32352
v_mfma_f32_16x16x32_bf16 v[104:107], a[152:155], a[40:43], v[104:107]
v_mfma_f32_16x16x32_bf16 v[108:111], a[152:155], a[44:47], v[108:111]
buffer_load_dwordx4 a[124:127], v235, s[44:47], s71 offen offset:3072 sc1
s_add_u32 s71, s71, 4096
s_waitcnt lgkmcnt(10)
v_mfma_f32_16x16x32_bf16 v[112:115], a[156:159], a[32:35], v[112:115]
ds_read_b64_tr_b16 a[144:145], v233 offset:27776
v_mfma_f32_16x16x32_bf16 v[116:119], a[156:159], a[36:39], v[116:119]
ds_read_b64_tr_b16 a[146:147], v233 offset:32384
s_add_u32 m0, m0, 1024
v_mfma_f32_16x16x32_bf16 v[120:123], a[156:159], a[40:43], v[120:123]
buffer_load_dwordx4 v248, s[40:43], s64 offen lds
v_mfma_f32_16x16x32_bf16 v[124:127], a[156:159], a[44:47], v[124:127]
s_waitcnt lgkmcnt(10)
v_mfma_f32_16x16x32_bf16 v[128:131], a[160:163], a[32:35], v[128:131]
ds_read_b64_tr_b16 a[148:149], v233 offset:27808
v_mfma_f32_16x16x32_bf16 v[132:135], a[160:163], a[36:39], v[132:135]
ds_read_b64_tr_b16 a[150:151], v233 offset:32416
v_mfma_f32_16x16x32_bf16 v[136:139], a[160:163], a[40:43], v[136:139]
v_mfma_f32_16x16x32_bf16 v[140:143], a[160:163], a[44:47], v[140:143]
s_waitcnt vmcnt(25) lgkmcnt(10)
v_mfma_f32_16x16x32_bf16 v[0:3], a[128:131], a[48:51], v[0:3]
ds_read_b64_tr_b16 a[152:153], v233 offset:27840
v_mfma_f32_16x16x32_bf16 v[4:7], a[128:131], a[52:55], v[4:7]
ds_read_b64_tr_b16 a[154:155], v233 offset:32448
v_mfma_f32_16x16x32_bf16 v[8:11], a[128:131], a[56:59], v[8:11]
v_mfma_f32_16x16x32_bf16 v[12:15], a[128:131], a[60:63], v[12:15]
s_add_u32 s64, s64, s65
s_add_u32 s69, s69, 36864
s_cmp_eq_u32 s69, 147456
s_cselect_b32 s69, 0, s69
buffer_load_dwordx4 a[0:3], v235, s[44:47], s71 offen offset:0 sc1
s_waitcnt lgkmcnt(10)
v_mfma_f32_16x16x32_bf16 v[16:19], a[132:135], a[48:51], v[16:19]
ds_read_b64_tr_b16 a[156:157], v233 offset:27872
v_mfma_f32_16x16x32_bf16 v[20:23], a[132:135], a[52:55], v[20:23]
ds_read_b64_tr_b16 a[158:159], v233 offset:32480
v_mfma_f32_16x16x32_bf16 v[24:27], a[132:135], a[56:59], v[24:27]
v_mfma_f32_16x16x32_bf16 v[28:31], a[132:135], a[60:63], v[28:31]
s_waitcnt lgkmcnt(10)
v_mfma_f32_16x16x32_bf16 v[32:35], a[136:139], a[48:51], v[32:35]
ds_read_b64_tr_b16 a[160:161], v233 offset:27904
v_mfma_f32_16x16x32_bf16 v[36:39], a[136:139], a[52:55], v[36:39]
ds_read_b64_tr_b16 a[162:163], v233 offset:32512
v_mfma_f32_16x16x32_bf16 v[40:43], a[136:139], a[56:59], v[40:43]
v_mfma_f32_16x16x32_bf16 v[44:47], a[136:139], a[60:63], v[44:47]
buffer_load_dwordx4 a[4:7], v235, s[44:47], s71 offen offset:1024 sc1
s_waitcnt lgkmcnt(10)
v_mfma_f32_16x16x32_bf16 v[48:51], a[140:143], a[48:51], v[48:51]
s_add_u32 s67, s67, 36864
s_cmp_eq_u32 s67, 147456
s_cselect_b32 s66, s76, 36864
s_cselect_b32 s67, 0, s67
v_mfma_f32_16x16x32_bf16 v[52:55], a[140:143], a[52:55], v[52:55]
v_mfma_f32_16x16x32_bf16 v[56:59], a[140:143], a[56:59], v[56:59]
v_mfma_f32_16x16x32_bf16 v[60:63], a[140:143], a[60:63], v[60:63]
s_waitcnt lgkmcnt(8)
v_mfma_f32_16x16x32_bf16 v[64:67], a[144:147], a[48:51], v[64:67]
v_add_u32 v233, s66, v233
v_mfma_f32_16x16x32_bf16 v[68:71], a[144:147], a[52:55], v[68:71]
v_mfma_f32_16x16x32_bf16 v[72:75], a[144:147], a[56:59], v[72:75]
v_mfma_f32_16x16x32_bf16 v[76:79], a[144:147], a[60:63], v[76:79]
buffer_load_dwordx4 a[8:11], v235, s[44:47], s71 offen offset:2048 sc1
s_waitcnt vmcnt(59) lgkmcnt(0)
s_barrier
v_mfma_f32_16x16x32_bf16 v[80:83], a[148:151], a[48:51], v[80:83]
ds_read_b64_tr_b16 a[128:129], v233 offset:0
ds_read_b64_tr_b16 a[130:131], v233 offset:4608
v_mfma_f32_16x16x32_bf16 v[84:87], a[148:151], a[52:55], v[84:87]
ds_read_b64_tr_b16 a[132:133], v233 offset:32
ds_read_b64_tr_b16 a[134:135], v233 offset:4640
v_mfma_f32_16x16x32_bf16 v[88:91], a[148:151], a[56:59], v[88:91]
v_mfma_f32_16x16x32_bf16 v[92:95], a[148:151], a[60:63], v[92:95]
s_waitcnt lgkmcnt(8)
v_mfma_f32_16x16x32_bf16 v[96:99], a[152:155], a[48:51], v[96:99]
ds_read_b64_tr_b16 a[136:137], v233 offset:64
ds_read_b64_tr_b16 a[138:139], v233 offset:4672
v_mfma_f32_16x16x32_bf16 v[100:103], a[152:155], a[52:55], v[100:103]
ds_read_b64_tr_b16 a[140:141], v233 offset:96
ds_read_b64_tr_b16 a[142:143], v233 offset:4704
v_mfma_f32_16x16x32_bf16 v[104:107], a[152:155], a[56:59], v[104:107]
v_mfma_f32_16x16x32_bf16 v[108:111], a[152:155], a[60:63], v[108:111]
buffer_load_dwordx4 a[12:15], v235, s[44:47], s71 offen offset:3072 sc1
s_add_u32 s71, s71, 4096
s_waitcnt lgkmcnt(10)
v_mfma_f32_16x16x32_bf16 v[112:115], a[156:159], a[48:51], v[112:115]
ds_read_b64_tr_b16 a[144:145], v233 offset:128
v_mfma_f32_16x16x32_bf16 v[116:119], a[156:159], a[52:55], v[116:119]
ds_read_b64_tr_b16 a[146:147], v233 offset:4736
v_mfma_f32_16x16x32_bf16 v[120:123], a[156:159], a[56:59], v[120:123]
v_mfma_f32_16x16x32_bf16 v[124:127], a[156:159], a[60:63], v[124:127]
s_waitcnt lgkmcnt(10)
v_mfma_f32_16x16x32_bf16 v[128:131], a[160:163], a[48:51], v[128:131]
ds_read_b64_tr_b16 a[148:149], v233 offset:160
v_mfma_f32_16x16x32_bf16 v[132:135], a[160:163], a[52:55], v[132:135]
ds_read_b64_tr_b16 a[150:151], v233 offset:4768
)"
R"(
v_mfma_f32_16x16x32_bf16 v[136:139], a[160:163], a[56:59], v[136:139]
v_mfma_f32_16x16x32_bf16 v[140:143], a[160:163], a[60:63], v[140:143]
s_waitcnt vmcnt(25) lgkmcnt(10)
v_mfma_f32_16x16x32_bf16 v[0:3], a[128:131], a[64:67], v[0:3]
ds_read_b64_tr_b16 a[152:153], v233 offset:192
v_mfma_f32_16x16x32_bf16 v[4:7], a[128:131], a[68:71], v[4:7]
ds_read_b64_tr_b16 a[154:155], v233 offset:4800
v_mfma_f32_16x16x32_bf16 v[8:11], a[128:131], a[72:75], v[8:11]
v_mfma_f32_16x16x32_bf16 v[12:15], a[128:131], a[76:79], v[12:15]
buffer_load_dwordx4 a[16:19], v235, s[44:47], s71 offen offset:0 sc1
s_waitcnt lgkmcnt(10)
v_mfma_f32_16x16x32_bf16 v[16:19], a[132:135], a[64:67], v[16:19]
ds_read_b64_tr_b16 a[156:157], v233 offset:224
v_mfma_f32_16x16x32_bf16 v[20:23], a[132:135], a[68:71], v[20:23]
ds_read_b64_tr_b16 a[158:159], v233 offset:4832
s_add_u32 m0, s70, s69
v_mfma_f32_16x16x32_bf16 v[24:27], a[132:135], a[72:75], v[24:27]
buffer_load_dwordx4 v240, s[40:43], s64 offen lds
v_mfma_f32_16x16x32_bf16 v[28:31], a[132:135], a[76:79], v[28:31]
buffer_load_dwordx4 a[20:23], v235, s[44:47], s71 offen offset:1024 sc1
s_waitcnt lgkmcnt(10)
v_mfma_f32_16x16x32_bf16 v[32:35], a[136:139], a[64:67], v[32:35]
ds_read_b64_tr_b16 a[160:161], v233 offset:256
v_mfma_f32_16x16x32_bf16 v[36:39], a[136:139], a[68:71], v[36:39]
ds_read_b64_tr_b16 a[162:163], v233 offset:4864
v_mfma_f32_16x16x32_bf16 v[40:43], a[136:139], a[72:75], v[40:43]
v_mfma_f32_16x16x32_bf16 v[44:47], a[136:139], a[76:79], v[44:47]
buffer_load_dwordx4 a[24:27], v235, s[44:47], s71 offen offset:2048 sc1
s_waitcnt lgkmcnt(10)
v_mfma_f32_16x16x32_bf16 v[48:51], a[140:143], a[64:67], v[48:51]
ds_read_b64_tr_b16 a[128:129], v233 offset:9216
v_mfma_f32_16x16x32_bf16 v[52:55], a[140:143], a[68:71], v[52:55]
ds_read_b64_tr_b16 a[130:131], v233 offset:13824
v_mfma_f32_16x16x32_bf16 v[56:59], a[140:143], a[72:75], v[56:59]
v_mfma_f32_16x16x32_bf16 v[60:63], a[140:143], a[76:79], v[60:63]
buffer_load_dwordx4 a[28:31], v235, s[44:47], s71 offen offset:3072 sc1
s_add_u32 s71, s71, 4096
s_waitcnt lgkmcnt(10)
v_mfma_f32_16x16x32_bf16 v[64:67], a[144:147], a[64:67], v[64:67]
ds_read_b64_tr_b16 a[132:133], v233 offset:9248
v_mfma_f32_16x16x32_bf16 v[68:71], a[144:147], a[68:71], v[68:71]
ds_read_b64_tr_b16 a[134:135], v233 offset:13856
s_add_u32 m0, m0, 1024
v_mfma_f32_16x16x32_bf16 v[72:75], a[144:147], a[72:75], v[72:75]
buffer_load_dwordx4 v241, s[40:43], s64 offen lds
v_mfma_f32_16x16x32_bf16 v[76:79], a[144:147], a[76:79], v[76:79]
buffer_load_dwordx4 a[32:35], v235, s[44:47], s71 offen offset:0 sc1
s_waitcnt lgkmcnt(10)
v_mfma_f32_16x16x32_bf16 v[80:83], a[148:151], a[64:67], v[80:83]
ds_read_b64_tr_b16 a[136:137], v233 offset:9280
v_mfma_f32_16x16x32_bf16 v[84:87], a[148:151], a[68:71], v[84:87]
ds_read_b64_tr_b16 a[138:139], v233 offset:13888
v_mfma_f32_16x16x32_bf16 v[88:91], a[148:151], a[72:75], v[88:91]
v_mfma_f32_16x16x32_bf16 v[92:95], a[148:151], a[76:79], v[92:95]
buffer_load_dwordx4 a[36:39], v235, s[44:47], s71 offen offset:1024 sc1
s_waitcnt lgkmcnt(10)
v_mfma_f32_16x16x32_bf16 v[96:99], a[152:155], a[64:67], v[96:99]
ds_read_b64_tr_b16 a[140:141], v233 offset:9312
v_mfma_f32_16x16x32_bf16 v[100:103], a[152:155], a[68:71], v[100:103]
ds_read_b64_tr_b16 a[142:143], v233 offset:13920
v_mfma_f32_16x16x32_bf16 v[104:107], a[152:155], a[72:75], v[104:107]
v_mfma_f32_16x16x32_bf16 v[108:111], a[152:155], a[76:79], v[108:111]
buffer_load_dwordx4 a[40:43], v235, s[44:47], s71 offen offset:2048 sc1
s_waitcnt lgkmcnt(10)
v_mfma_f32_16x16x32_bf16 v[112:115], a[156:159], a[64:67], v[112:115]
ds_read_b64_tr_b16 a[144:145], v233 offset:9344
v_mfma_f32_16x16x32_bf16 v[116:119], a[156:159], a[68:71], v[116:119]
ds_read_b64_tr_b16 a[146:147], v233 offset:13952
s_add_u32 m0, m0, 1024
v_mfma_f32_16x16x32_bf16 v[120:123], a[156:159], a[72:75], v[120:123]
buffer_load_dwordx4 v242, s[40:43], s64 offen lds
v_mfma_f32_16x16x32_bf16 v[124:127], a[156:159], a[76:79], v[124:127]
buffer_load_dwordx4 a[44:47], v235, s[44:47], s71 offen offset:3072 sc1
s_add_u32 s71, s71, 4096
s_waitcnt lgkmcnt(10)
v_mfma_f32_16x16x32_bf16 v[128:131], a[160:163], a[64:67], v[128:131]
ds_read_b64_tr_b16 a[148:149], v233 offset:9376
v_mfma_f32_16x16x32_bf16 v[132:135], a[160:163], a[68:71], v[132:135]
ds_read_b64_tr_b16 a[150:151], v233 offset:13984
v_mfma_f32_16x16x32_bf16 v[136:139], a[160:163], a[72:75], v[136:139]
v_mfma_f32_16x16x32_bf16 v[140:143], a[160:163], a[76:79], v[140:143]
s_waitcnt vmcnt(30) lgkmcnt(10)
v_mfma_f32_16x16x32_bf16 v[0:3], a[128:131], a[80:83], v[0:3]
ds_read_b64_tr_b16 a[152:153], v233 offset:9408
v_mfma_f32_16x16x32_bf16 v[4:7], a[128:131], a[84:87], v[4:7]
ds_read_b64_tr_b16 a[154:155], v233 offset:14016
v_mfma_f32_16x16x32_bf16 v[8:11], a[128:131], a[88:91], v[8:11]
v_mfma_f32_16x16x32_bf16 v[12:15], a[128:131], a[92:95], v[12:15]
buffer_load_dwordx4 a[48:51], v235, s[44:47], s71 offen offset:0 sc1
s_waitcnt lgkmcnt(10)
v_mfma_f32_16x16x32_bf16 v[16:19], a[132:135], a[80:83], v[16:19]
ds_read_b64_tr_b16 a[156:157], v233 offset:9440
v_mfma_f32_16x16x32_bf16 v[20:23], a[132:135], a[84:87], v[20:23]
ds_read_b64_tr_b16 a[158:159], v233 offset:14048
s_add_u32 m0, m0, 1024
v_mfma_f32_16x16x32_bf16 v[24:27], a[132:135], a[88:91], v[24:27]
buffer_load_dwordx4 v243, s[40:43], s64 offen lds
v_mfma_f32_16x16x32_bf16 v[28:31], a[132:135], a[92:95], v[28:31]
s_waitcnt lgkmcnt(10)
v_mfma_f32_16x16x32_bf16 v[32:35], a[136:139], a[80:83], v[32:35]
ds_read_b64_tr_b16 a[160:161], v233 offset:9472
v_mfma_f32_16x16x32_bf16 v[36:39], a[136:139], a[84:87], v[36:39]
ds_read_b64_tr_b16 a[162:163], v233 offset:14080
v_mfma_f32_16x16x32_bf16 v[40:43], a[136:139], a[88:91], v[40:43]
v_mfma_f32_16x16x32_bf16 v[44:47], a[136:139], a[92:95], v[44:47]
buffer_load_dwordx4 a[52:55], v235, s[44:47], s71 offen offset:1024 sc1
s_waitcnt lgkmcnt(10)
v_mfma_f32_16x16x32_bf16 v[48:51], a[140:143], a[80:83], v[48:51]
ds_read_b64_tr_b16 a[128:129], v233 offset:18432
v_mfma_f32_16x16x32_bf16 v[52:55], a[140:143], a[84:87], v[52:55]
ds_read_b64_tr_b16 a[130:131], v233 offset:23040
v_mfma_f32_16x16x32_bf16 v[56:59], a[140:143], a[88:91], v[56:59]
v_mfma_f32_16x16x32_bf16 v[60:63], a[140:143], a[92:95], v[60:63]
s_waitcnt lgkmcnt(10)
v_mfma_f32_16x16x32_bf16 v[64:67], a[144:147], a[80:83], v[64:67]
ds_read_b64_tr_b16 a[132:133], v233 offset:18464
v_mfma_f32_16x16x32_bf16 v[68:71], a[144:147], a[84:87], v[68:71]
ds_read_b64_tr_b16 a[134:135], v233 offset:23072
s_add_u32 m0, m0, 1024
v_mfma_f32_16x16x32_bf16 v[72:75], a[144:147], a[88:91], v[72:75]
buffer_load_dwordx4 v244, s[40:43], s64 offen lds
v_mfma_f32_16x16x32_bf16 v[76:79], a[144:147], a[92:95], v[76:79]
buffer_load_dwordx4 a[56:59], v235, s[44:47], s71 offen offset:2048 sc1
s_waitcnt lgkmcnt(10)
v_mfma_f32_16x16x32_bf16 v[80:83], a[148:151], a[80:83], v[80:83]
ds_read_b64_tr_b16 a[136:137], v233 offset:18496
v_mfma_f32_16x16x32_bf16 v[84:87], a[148:151], a[84:87], v[84:87]
ds_read_b64_tr_b16 a[138:139], v233 offset:23104
v_mfma_f32_16x16x32_bf16 v[88:91], a[148:151], a[88:91], v[88:91]
v_mfma_f32_16x16x32_bf16 v[92:95], a[148:151], a[92:95], v[92:95]
s_waitcnt lgkmcnt(10)
v_mfma_f32_16x16x32_bf16 v[96:99], a[152:155], a[80:83], v[96:99]
ds_read_b64_tr_b16 a[140:141], v233 offset:18528
v_mfma_f32_16x16x32_bf16 v[100:103], a[152:155], a[84:87], v[100:103]
ds_read_b64_tr_b16 a[142:143], v233 offset:23136
v_mfma_f32_16x16x32_bf16 v[104:107], a[152:155], a[88:91], v[104:107]
v_mfma_f32_16x16x32_bf16 v[108:111], a[152:155], a[92:95], v[108:111]
buffer_load_dwordx4 a[60:63], v235, s[44:47], s71 offen offset:3072 sc1
s_add_u32 s71, s71, 4096
s_waitcnt lgkmcnt(10)
v_mfma_f32_16x16x32_bf16 v[112:115], a[156:159], a[80:83], v[112:115]
ds_read_b64_tr_b16 a[144:145], v233 offset:18560
v_mfma_f32_16x16x32_bf16 v[116:119], a[156:159], a[84:87], v[116:119]
ds_read_b64_tr_b16 a[146:147], v233 offset:23168
s_add_u32 m0, m0, 1024
v_mfma_f32_16x16x32_bf16 v[120:123], a[156:159], a[88:91], v[120:123]
buffer_load_dwordx4 v245, s[40:43], s64 offen lds
v_mfma_f32_16x16x32_bf16 v[124:127], a[156:159], a[92:95], v[124:127]
s_waitcnt lgkmcnt(10)
v_mfma_f32_16x16x32_bf16 v[128:131], a[160:163], a[80:83], v[128:131]
ds_read_b64_tr_b16 a[148:149], v233 offset:18592
v_mfma_f32_16x16x32_bf16 v[132:135], a[160:163], a[84:87], v[132:135]
ds_read_b64_tr_b16 a[150:151], v233 offset:23200
v_mfma_f32_16x16x32_bf16 v[136:139], a[160:163], a[88:91], v[136:139]
v_mfma_f32_16x16x32_bf16 v[140:143], a[160:163], a[92:95], v[140:143]
s_waitcnt vmcnt(30) lgkmcnt(10)
v_mfma_f32_16x16x32_bf16 v[0:3], a[128:131], a[96:99], v[0:3]
ds_read_b64_tr_b16 a[152:153], v233 offset:18624
v_mfma_f32_16x16x32_bf16 v[4:7], a[128:131], a[100:103], v[4:7]
ds_read_b64_tr_b16 a[154:155], v233 offset:23232
v_mfma_f32_16x16x32_bf16 v[8:11], a[128:131], a[104:107], v[8:11]
v_mfma_f32_16x16x32_bf16 v[12:15], a[128:131], a[108:111], v[12:15]
buffer_load_dwordx4 a[64:67], v235, s[44:47], s71 offen offset:0 sc1
s_waitcnt lgkmcnt(10)
v_mfma_f32_16x16x32_bf16 v[16:19], a[132:135], a[96:99], v[16:19]
ds_read_b64_tr_b16 a[156:157], v233 offset:18656
v_mfma_f32_16x16x32_bf16 v[20:23], a[132:135], a[100:103], v[20:23]
ds_read_b64_tr_b16 a[158:159], v233 offset:23264
s_add_u32 m0, m0, 1024
v_mfma_f32_16x16x32_bf16 v[24:27], a[132:135], a[104:107], v[24:27]
buffer_load_dwordx4 v246, s[40:43], s64 offen lds
v_mfma_f32_16x16x32_bf16 v[28:31], a[132:135], a[108:111], v[28:31]
s_waitcnt lgkmcnt(10)
v_mfma_f32_16x16x32_bf16 v[32:35], a[136:139], a[96:99], v[32:35]
ds_read_b64_tr_b16 a[160:161], v233 offset:18688
v_mfma_f32_16x16x32_bf16 v[36:39], a[136:139], a[100:103], v[36:39]
ds_read_b64_tr_b16 a[162:163], v233 offset:23296
v_mfma_f32_16x16x32_bf16 v[40:43], a[136:139], a[104:107], v[40:43]
v_mfma_f32_16x16x32_bf16 v[44:47], a[136:139], a[108:111], v[44:47]
buffer_load_dwordx4 a[68:71], v235, s[44:47], s71 offen offset:1024 sc1
s_waitcnt lgkmcnt(10)
v_mfma_f32_16x16x32_bf16 v[48:51], a[140:143], a[96:99], v[48:51]
ds_read_b64_tr_b16 a[128:129], v233 offset:27648
v_mfma_f32_16x16x32_bf16 v[52:55], a[140:143], a[100:103], v[52:55]
ds_read_b64_tr_b16 a[130:131], v233 offset:32256
v_mfma_f32_16x16x32_bf16 v[56:59], a[140:143], a[104:107], v[56:59]
v_mfma_f32_16x16x32_bf16 v[60:63], a[140:143], a[108:111], v[60:63]
s_waitcnt lgkmcnt(10)
v_mfma_f32_16x16x32_bf16 v[64:67], a[144:147], a[96:99], v[64:67]
ds_read_b64_tr_b16 a[132:133], v233 offset:27680
v_mfma_f32_16x16x32_bf16 v[68:71], a[144:147], a[100:103], v[68:71]
ds_read_b64_tr_b16 a[134:135], v233 offset:32288
s_add_u32 m0, m0, 1024
v_mfma_f32_16x16x32_bf16 v[72:75], a[144:147], a[104:107], v[72:75]
buffer_load_dwordx4 v247, s[40:43], s64 offen lds
v_mfma_f32_16x16x32_bf16 v[76:79], a[144:147], a[108:111], v[76:79]
buffer_load_dwordx4 a[72:75], v235, s[44:47], s71 offen offset:2048 sc1
s_waitcnt lgkmcnt(10)
v_mfma_f32_16x16x32_bf16 v[80:83], a[148:151], a[96:99], v[80:83]
ds_read_b64_tr_b16 a[136:137], v233 offset:27712
v_mfma_f32_16x16x32_bf16 v[84:87], a[148:151], a[100:103], v[84:87]
ds_read_b64_tr_b16 a[138:139], v233 offset:32320
v_mfma_f32_16x16x32_bf16 v[88:91], a[148:151], a[104:107], v[88:91]
v_mfma_f32_16x16x32_bf16 v[92:95], a[148:151], a[108:111], v[92:95]
s_waitcnt lgkmcnt(10)
v_mfma_f32_16x16x32_bf16 v[96:99], a[152:155], a[96:99], v[96:99]
ds_read_b64_tr_b16 a[140:141], v233 offset:27744
v_mfma_f32_16x16x32_bf16 v[100:103], a[152:155], a[100:103], v[100:103]
ds_read_b64_tr_b16 a[142:143], v233 offset:32352
v_mfma_f32_16x16x32_bf16 v[104:107], a[152:155], a[104:107], v[104:107]
v_mfma_f32_16x16x32_bf16 v[108:111], a[152:155], a[108:111], v[108:111]
buffer_load_dwordx4 a[76:79], v235, s[44:47], s71 offen offset:3072 sc1
s_add_u32 s71, s71, 4096
s_waitcnt lgkmcnt(10)
v_mfma_f32_16x16x32_bf16 v[112:115], a[156:159], a[96:99], v[112:115]
ds_read_b64_tr_b16 a[144:145], v233 offset:27776
v_mfma_f32_16x16x32_bf16 v[116:119], a[156:159], a[100:103], v[116:119]
ds_read_b64_tr_b16 a[146:147], v233 offset:32384
s_add_u32 m0, m0, 1024
v_mfma_f32_16x16x32_bf16 v[120:123], a[156:159], a[104:107], v[120:123]
buffer_load_dwordx4 v248, s[40:43], s64 offen lds
v_mfma_f32_16x16x32_bf16 v[124:127], a[156:159], a[108:111], v[124:127]
s_waitcnt lgkmcnt(10)
v_mfma_f32_16x16x32_bf16 v[128:131], a[160:163], a[96:99], v[128:131]
ds_read_b64_tr_b16 a[148:149], v233 offset:27808
v_mfma_f32_16x16x32_bf16 v[132:135], a[160:163], a[100:103], v[132:135]
ds_read_b64_tr_b16 a[150:151], v233 offset:32416
v_mfma_f32_16x16x32_bf16 v[136:139], a[160:163], a[104:107], v[136:139]
v_mfma_f32_16x16x32_bf16 v[140:143], a[160:163], a[108:111], v[140:143]
s_waitcnt vmcnt(30) lgkmcnt(10)
v_mfma_f32_16x16x32_bf16 v[0:3], a[128:131], a[112:115], v[0:3]
ds_read_b64_tr_b16 a[152:153], v233 offset:27840
v_mfma_f32_16x16x32_bf16 v[4:7], a[128:131], a[116:119], v[4:7]
ds_read_b64_tr_b16 a[154:155], v233 offset:32448
v_mfma_f32_16x16x32_bf16 v[8:11], a[128:131], a[120:123], v[8:11]
v_mfma_f32_16x16x32_bf16 v[12:15], a[128:131], a[124:127], v[12:15]
s_add_u32 s64, s64, s65
s_add_u32 s69, s69, 36864
s_cmp_eq_u32 s69, 147456
s_cselect_b32 s69, 0, s69
buffer_load_dwordx4 a[80:83], v235, s[44:47], s71 offen offset:0 sc1
s_waitcnt lgkmcnt(10)
v_mfma_f32_16x16x32_bf16 v[16:19], a[132:135], a[112:115], v[16:19]
ds_read_b64_tr_b16 a[156:157], v233 offset:27872
v_mfma_f32_16x16x32_bf16 v[20:23], a[132:135], a[116:119], v[20:23]
ds_read_b64_tr_b16 a[158:159], v233 offset:32480
v_mfma_f32_16x16x32_bf16 v[24:27], a[132:135], a[120:123], v[24:27]
v_mfma_f32_16x16x32_bf16 v[28:31], a[132:135], a[124:127], v[28:31]
s_waitcnt lgkmcnt(10)
v_mfma_f32_16x16x32_bf16 v[32:35], a[136:139], a[112:115], v[32:35]
ds_read_b64_tr_b16 a[160:161], v233 offset:27904
v_mfma_f32_16x16x32_bf16 v[36:39], a[136:139], a[116:119], v[36:39]
ds_read_b64_tr_b16 a[162:163], v233 offset:32512
v_mfma_f32_16x16x32_bf16 v[40:43], a[136:139], a[120:123], v[40:43]
v_mfma_f32_16x16x32_bf16 v[44:47], a[136:139], a[124:127], v[44:47]
buffer_load_dwordx4 a[84:87], v235, s[44:47], s71 offen offset:1024 sc1
s_waitcnt lgkmcnt(10)
v_mfma_f32_16x16x32_bf16 v[48:51], a[140:143], a[112:115], v[48:51]
s_add_u32 s67, s67, 36864
s_cmp_eq_u32 s67, 147456
s_cselect_b32 s66, s76, 36864
s_cselect_b32 s67, 0, s67
v_mfma_f32_16x16x32_bf16 v[52:55], a[140:143], a[116:119], v[52:55]
v_mfma_f32_16x16x32_bf16 v[56:59], a[140:143], a[120:123], v[56:59]
v_mfma_f32_16x16x32_bf16 v[60:63], a[140:143], a[124:127], v[60:63]
s_waitcnt lgkmcnt(8)
v_mfma_f32_16x16x32_bf16 v[64:67], a[144:147], a[112:115], v[64:67]
v_add_u32 v233, s66, v233
v_mfma_f32_16x16x32_bf16 v[68:71], a[144:147], a[116:119], v[68:71]
v_mfma_f32_16x16x32_bf16 v[72:75], a[144:147], a[120:123], v[72:75]
v_mfma_f32_16x16x32_bf16 v[76:79], a[144:147], a[124:127], v[76:79]
buffer_load_dwordx4 a[88:91], v235, s[44:47], s71 offen offset:2048 sc1
s_waitcnt vmcnt(61) lgkmcnt(0)
s_barrier
v_mfma_f32_16x16x32_bf16 v[80:83], a[148:151], a[112:115], v[80:83]
ds_read_b64_tr_b16 a[128:129], v233 offset:0
ds_read_b64_tr_b16 a[130:131], v233 offset:4608
v_mfma_f32_16x16x32_bf16 v[84:87], a[148:151], a[116:119], v[84:87]
ds_read_b64_tr_b16 a[132:133], v233 offset:32
ds_read_b64_tr_b16 a[134:135], v233 offset:4640
v_mfma_f32_16x16x32_bf16 v[88:91], a[148:151], a[120:123], v[88:91]
v_mfma_f32_16x16x32_bf16 v[92:95], a[148:151], a[124:127], v[92:95]
s_waitcnt lgkmcnt(8)
v_mfma_f32_16x16x32_bf16 v[96:99], a[152:155], a[112:115], v[96:99]
ds_read_b64_tr_b16 a[136:137], v233 offset:64
ds_read_b64_tr_b16 a[138:139], v233 offset:4672
v_mfma_f32_16x16x32_bf16 v[100:103], a[152:155], a[116:119], v[100:103]
ds_read_b64_tr_b16 a[140:141], v233 offset:96
ds_read_b64_tr_b16 a[142:143], v233 offset:4704
v_mfma_f32_16x16x32_bf16 v[104:107], a[152:155], a[120:123], v[104:107]
v_mfma_f32_16x16x32_bf16 v[108:111], a[152:155], a[124:127], v[108:111]
buffer_load_dwordx4 a[92:95], v235, s[44:47], s71 offen offset:3072 sc1
s_add_u32 s71, s71, 4096
s_waitcnt lgkmcnt(10)
v_mfma_f32_16x16x32_bf16 v[112:115], a[156:159], a[112:115], v[112:115]
ds_read_b64_tr_b16 a[144:145], v233 offset:128
v_mfma_f32_16x16x32_bf16 v[116:119], a[156:159], a[116:119], v[116:119]
ds_read_b64_tr_b16 a[146:147], v233 offset:4736
v_mfma_f32_16x16x32_bf16 v[120:123], a[156:159], a[120:123], v[120:123]
v_mfma_f32_16x16x32_bf16 v[124:127], a[156:159], a[124:127], v[124:127]
s_waitcnt lgkmcnt(10)
v_mfma_f32_16x16x32_bf16 v[128:131], a[160:163], a[112:115], v[128:131]
ds_read_b64_tr_b16 a[148:149], v233 offset:160
v_mfma_f32_16x16x32_bf16 v[132:135], a[160:163], a[116:119], v[132:135]
ds_read_b64_tr_b16 a[150:151], v233 offset:4768
v_mfma_f32_16x16x32_bf16 v[136:139], a[160:163], a[120:123], v[136:139]
v_mfma_f32_16x16x32_bf16 v[140:143], a[160:163], a[124:127], v[140:143]
s_waitcnt vmcnt(29) lgkmcnt(10)
v_mfma_f32_16x16x32_bf16 v[0:3], a[128:131], a[0:3], v[0:3]
ds_read_b64_tr_b16 a[152:153], v233 offset:192
v_mfma_f32_16x16x32_bf16 v[4:7], a[128:131], a[4:7], v[4:7]
ds_read_b64_tr_b16 a[154:155], v233 offset:4800
v_mfma_f32_16x16x32_bf16 v[8:11], a[128:131], a[8:11], v[8:11]
v_mfma_f32_16x16x32_bf16 v[12:15], a[128:131], a[12:15], v[12:15]
buffer_load_dwordx4 a[96:99], v235, s[44:47], s71 offen offset:0 sc1
s_waitcnt lgkmcnt(10)
v_mfma_f32_16x16x32_bf16 v[16:19], a[132:135], a[0:3], v[16:19]
ds_read_b64_tr_b16 a[156:157], v233 offset:224
v_mfma_f32_16x16x32_bf16 v[20:23], a[132:135], a[4:7], v[20:23]
ds_read_b64_tr_b16 a[158:159], v233 offset:4832
v_mfma_f32_16x16x32_bf16 v[24:27], a[132:135], a[8:11], v[24:27]
v_mfma_f32_16x16x32_bf16 v[28:31], a[132:135], a[12:15], v[28:31]
buffer_load_dwordx4 a[100:103], v235, s[44:47], s71 offen offset:1024 sc1
s_waitcnt lgkmcnt(10)
v_mfma_f32_16x16x32_bf16 v[32:35], a[136:139], a[0:3], v[32:35]
ds_read_b64_tr_b16 a[160:161], v233 offset:256
v_mfma_f32_16x16x32_bf16 v[36:39], a[136:139], a[4:7], v[36:39]
ds_read_b64_tr_b16 a[162:163], v233 offset:4864
v_mfma_f32_16x16x32_bf16 v[40:43], a[136:139], a[8:11], v[40:43]
v_mfma_f32_16x16x32_bf16 v[44:47], a[136:139], a[12:15], v[44:47]
buffer_load_dwordx4 a[104:107], v235, s[44:47], s71 offen offset:2048 sc1
s_waitcnt lgkmcnt(10)
v_mfma_f32_16x16x32_bf16 v[48:51], a[140:143], a[0:3], v[48:51]
ds_read_b64_tr_b16 a[128:129], v233 offset:9216
v_mfma_f32_16x16x32_bf16 v[52:55], a[140:143], a[4:7], v[52:55]
ds_read_b64_tr_b16 a[130:131], v233 offset:13824
v_mfma_f32_16x16x32_bf16 v[56:59], a[140:143], a[8:11], v[56:59]
v_mfma_f32_16x16x32_bf16 v[60:63], a[140:143], a[12:15], v[60:63]
buffer_load_dwordx4 a[108:111], v235, s[44:47], s71 offen offset:3072 sc1
s_add_u32 s71, s71, 4096
s_waitcnt lgkmcnt(10)
v_mfma_f32_16x16x32_bf16 v[64:67], a[144:147], a[0:3], v[64:67]
ds_read_b64_tr_b16 a[132:133], v233 offset:9248
v_mfma_f32_16x16x32_bf16 v[68:71], a[144:147], a[4:7], v[68:71]
ds_read_b64_tr_b16 a[134:135], v233 offset:13856
v_mfma_f32_16x16x32_bf16 v[72:75], a[144:147], a[8:11], v[72:75]
v_mfma_f32_16x16x32_bf16 v[76:79], a[144:147], a[12:15], v[76:79]
buffer_load_dwordx4 a[112:115], v235, s[44:47], s71 offen offset:0 sc1
s_waitcnt lgkmcnt(10)
v_mfma_f32_16x16x32_bf16 v[80:83], a[148:151], a[0:3], v[80:83]
ds_read_b64_tr_b16 a[136:137], v233 offset:9280
v_mfma_f32_16x16x32_bf16 v[84:87], a[148:151], a[4:7], v[84:87]
ds_read_b64_tr_b16 a[138:139], v233 offset:13888
v_mfma_f32_16x16x32_bf16 v[88:91], a[148:151], a[8:11], v[88:91]
v_mfma_f32_16x16x32_bf16 v[92:95], a[148:151], a[12:15], v[92:95]
buffer_load_dwordx4 a[116:119], v235, s[44:47], s71 offen offset:1024 sc1
s_waitcnt lgkmcnt(10)
v_mfma_f32_16x16x32_bf16 v[96:99], a[152:155], a[0:3], v[96:99]
ds_read_b64_tr_b16 a[140:141], v233 offset:9312
v_mfma_f32_16x16x32_bf16 v[100:103], a[152:155], a[4:7], v[100:103]
ds_read_b64_tr_b16 a[142:143], v233 offset:13920
v_mfma_f32_16x16x32_bf16 v[104:107], a[152:155], a[8:11], v[104:107]
v_mfma_f32_16x16x32_bf16 v[108:111], a[152:155], a[12:15], v[108:111]
buffer_load_dwordx4 a[120:123], v235, s[44:47], s71 offen offset:2048 sc1
s_waitcnt lgkmcnt(10)
v_mfma_f32_16x16x32_bf16 v[112:115], a[156:159], a[0:3], v[112:115]
ds_read_b64_tr_b16 a[144:145], v233 offset:9344
v_mfma_f32_16x16x32_bf16 v[116:119], a[156:159], a[4:7], v[116:119]
ds_read_b64_tr_b16 a[146:147], v233 offset:13952
v_mfma_f32_16x16x32_bf16 v[120:123], a[156:159], a[8:11], v[120:123]
v_mfma_f32_16x16x32_bf16 v[124:127], a[156:159], a[12:15], v[124:127]
buffer_load_dwordx4 a[124:127], v235, s[44:47], s71 offen offset:3072 sc1
s_add_u32 s71, s71, 4096
s_waitcnt lgkmcnt(10)
v_mfma_f32_16x16x32_bf16 v[128:131], a[160:163], a[0:3], v[128:131]
ds_read_b64_tr_b16 a[148:149], v233 offset:9376
v_mfma_f32_16x16x32_bf16 v[132:135], a[160:163], a[4:7], v[132:135]
ds_read_b64_tr_b16 a[150:151], v233 offset:13984
v_mfma_f32_16x16x32_bf16 v[136:139], a[160:163], a[8:11], v[136:139]
v_mfma_f32_16x16x32_bf16 v[140:143], a[160:163], a[12:15], v[140:143]
s_waitcnt vmcnt(32) lgkmcnt(10)
v_mfma_f32_16x16x32_bf16 v[0:3], a[128:131], a[16:19], v[0:3]
ds_read_b64_tr_b16 a[152:153], v233 offset:9408
v_mfma_f32_16x16x32_bf16 v[4:7], a[128:131], a[20:23], v[4:7]
ds_read_b64_tr_b16 a[154:155], v233 offset:14016
v_mfma_f32_16x16x32_bf16 v[8:11], a[128:131], a[24:27], v[8:11]
v_mfma_f32_16x16x32_bf16 v[12:15], a[128:131], a[28:31], v[12:15]
buffer_load_dwordx4 a[0:3], v235, s[44:47], s71 offen offset:0 sc1
s_waitcnt lgkmcnt(10)
v_mfma_f32_16x16x32_bf16 v[16:19], a[132:135], a[16:19], v[16:19]
ds_read_b64_tr_b16 a[156:157], v233 offset:9440
v_mfma_f32_16x16x32_bf16 v[20:23], a[132:135], a[20:23], v[20:23]
ds_read_b64_tr_b16 a[158:159], v233 offset:14048
v_mfma_f32_16x16x32_bf16 v[24:27], a[132:135], a[24:27], v[24:27]
v_mfma_f32_16x16x32_bf16 v[28:31], a[132:135], a[28:31], v[28:31]
s_waitcnt lgkmcnt(10)
v_mfma_f32_16x16x32_bf16 v[32:35], a[136:139], a[16:19], v[32:35]
ds_read_b64_tr_b16 a[160:161], v233 offset:9472
v_mfma_f32_16x16x32_bf16 v[36:39], a[136:139], a[20:23], v[36:39]
ds_read_b64_tr_b16 a[162:163], v233 offset:14080
)"
R"(
v_mfma_f32_16x16x32_bf16 v[40:43], a[136:139], a[24:27], v[40:43]
v_mfma_f32_16x16x32_bf16 v[44:47], a[136:139], a[28:31], v[44:47]
buffer_load_dwordx4 a[4:7], v235, s[44:47], s71 offen offset:1024 sc1
s_waitcnt lgkmcnt(10)
v_mfma_f32_16x16x32_bf16 v[48:51], a[140:143], a[16:19], v[48:51]
ds_read_b64_tr_b16 a[128:129], v233 offset:18432
v_mfma_f32_16x16x32_bf16 v[52:55], a[140:143], a[20:23], v[52:55]
ds_read_b64_tr_b16 a[130:131], v233 offset:23040
v_mfma_f32_16x16x32_bf16 v[56:59], a[140:143], a[24:27], v[56:59]
v_mfma_f32_16x16x32_bf16 v[60:63], a[140:143], a[28:31], v[60:63]
s_waitcnt lgkmcnt(10)
v_mfma_f32_16x16x32_bf16 v[64:67], a[144:147], a[16:19], v[64:67]
ds_read_b64_tr_b16 a[132:133], v233 offset:18464
v_mfma_f32_16x16x32_bf16 v[68:71], a[144:147], a[20:23], v[68:71]
ds_read_b64_tr_b16 a[134:135], v233 offset:23072
v_mfma_f32_16x16x32_bf16 v[72:75], a[144:147], a[24:27], v[72:75]
v_mfma_f32_16x16x32_bf16 v[76:79], a[144:147], a[28:31], v[76:79]
buffer_load_dwordx4 a[8:11], v235, s[44:47], s71 offen offset:2048 sc1
s_waitcnt lgkmcnt(10)
v_mfma_f32_16x16x32_bf16 v[80:83], a[148:151], a[16:19], v[80:83]
ds_read_b64_tr_b16 a[136:137], v233 offset:18496
v_mfma_f32_16x16x32_bf16 v[84:87], a[148:151], a[20:23], v[84:87]
ds_read_b64_tr_b16 a[138:139], v233 offset:23104
v_mfma_f32_16x16x32_bf16 v[88:91], a[148:151], a[24:27], v[88:91]
v_mfma_f32_16x16x32_bf16 v[92:95], a[148:151], a[28:31], v[92:95]
s_waitcnt lgkmcnt(10)
v_mfma_f32_16x16x32_bf16 v[96:99], a[152:155], a[16:19], v[96:99]
ds_read_b64_tr_b16 a[140:141], v233 offset:18528
v_mfma_f32_16x16x32_bf16 v[100:103], a[152:155], a[20:23], v[100:103]
ds_read_b64_tr_b16 a[142:143], v233 offset:23136
v_mfma_f32_16x16x32_bf16 v[104:107], a[152:155], a[24:27], v[104:107]
v_mfma_f32_16x16x32_bf16 v[108:111], a[152:155], a[28:31], v[108:111]
buffer_load_dwordx4 a[12:15], v235, s[44:47], s71 offen offset:3072 sc1
s_add_u32 s71, s71, 4096
s_waitcnt lgkmcnt(10)
v_mfma_f32_16x16x32_bf16 v[112:115], a[156:159], a[16:19], v[112:115]
ds_read_b64_tr_b16 a[144:145], v233 offset:18560
v_mfma_f32_16x16x32_bf16 v[116:119], a[156:159], a[20:23], v[116:119]
ds_read_b64_tr_b16 a[146:147], v233 offset:23168
v_mfma_f32_16x16x32_bf16 v[120:123], a[156:159], a[24:27], v[120:123]
v_mfma_f32_16x16x32_bf16 v[124:127], a[156:159], a[28:31], v[124:127]
s_waitcnt lgkmcnt(10)
v_mfma_f32_16x16x32_bf16 v[128:131], a[160:163], a[16:19], v[128:131]
ds_read_b64_tr_b16 a[148:149], v233 offset:18592
v_mfma_f32_16x16x32_bf16 v[132:135], a[160:163], a[20:23], v[132:135]
ds_read_b64_tr_b16 a[150:151], v233 offset:23200
v_mfma_f32_16x16x32_bf16 v[136:139], a[160:163], a[24:27], v[136:139]
v_mfma_f32_16x16x32_bf16 v[140:143], a[160:163], a[28:31], v[140:143]
s_waitcnt vmcnt(30) lgkmcnt(10)
v_mfma_f32_16x16x32_bf16 v[0:3], a[128:131], a[32:35], v[0:3]
ds_read_b64_tr_b16 a[152:153], v233 offset:18624
v_mfma_f32_16x16x32_bf16 v[4:7], a[128:131], a[36:39], v[4:7]
ds_read_b64_tr_b16 a[154:155], v233 offset:23232
v_mfma_f32_16x16x32_bf16 v[8:11], a[128:131], a[40:43], v[8:11]
v_mfma_f32_16x16x32_bf16 v[12:15], a[128:131], a[44:47], v[12:15]
buffer_load_dwordx4 a[16:19], v235, s[44:47], s71 offen offset:0 sc1
s_waitcnt lgkmcnt(10)
v_mfma_f32_16x16x32_bf16 v[16:19], a[132:135], a[32:35], v[16:19]
ds_read_b64_tr_b16 a[156:157], v233 offset:18656
v_mfma_f32_16x16x32_bf16 v[20:23], a[132:135], a[36:39], v[20:23]
ds_read_b64_tr_b16 a[158:159], v233 offset:23264
v_mfma_f32_16x16x32_bf16 v[24:27], a[132:135], a[40:43], v[24:27]
v_mfma_f32_16x16x32_bf16 v[28:31], a[132:135], a[44:47], v[28:31]
s_waitcnt lgkmcnt(10)
v_mfma_f32_16x16x32_bf16 v[32:35], a[136:139], a[32:35], v[32:35]
ds_read_b64_tr_b16 a[160:161], v233 offset:18688
v_mfma_f32_16x16x32_bf16 v[36:39], a[136:139], a[36:39], v[36:39]
ds_read_b64_tr_b16 a[162:163], v233 offset:23296
v_mfma_f32_16x16x32_bf16 v[40:43], a[136:139], a[40:43], v[40:43]
v_mfma_f32_16x16x32_bf16 v[44:47], a[136:139], a[44:47], v[44:47]
buffer_load_dwordx4 a[20:23], v235, s[44:47], s71 offen offset:1024 sc1
s_waitcnt lgkmcnt(10)
v_mfma_f32_16x16x32_bf16 v[48:51], a[140:143], a[32:35], v[48:51]
ds_read_b64_tr_b16 a[128:129], v233 offset:27648
v_mfma_f32_16x16x32_bf16 v[52:55], a[140:143], a[36:39], v[52:55]
ds_read_b64_tr_b16 a[130:131], v233 offset:32256
v_mfma_f32_16x16x32_bf16 v[56:59], a[140:143], a[40:43], v[56:59]
v_mfma_f32_16x16x32_bf16 v[60:63], a[140:143], a[44:47], v[60:63]
s_waitcnt lgkmcnt(10)
v_mfma_f32_16x16x32_bf16 v[64:67], a[144:147], a[32:35], v[64:67]
ds_read_b64_tr_b16 a[132:133], v233 offset:27680
v_mfma_f32_16x16x32_bf16 v[68:71], a[144:147], a[36:39], v[68:71]
ds_read_b64_tr_b16 a[134:135], v233 offset:32288
v_mfma_f32_16x16x32_bf16 v[72:75], a[144:147], a[40:43], v[72:75]
v_mfma_f32_16x16x32_bf16 v[76:79], a[144:147], a[44:47], v[76:79]
buffer_load_dwordx4 a[24:27], v235, s[44:47], s71 offen offset:2048 sc1
s_waitcnt lgkmcnt(10)
v_mfma_f32_16x16x32_bf16 v[80:83], a[148:151], a[32:35], v[80:83]
ds_read_b64_tr_b16 a[136:137], v233 offset:27712
v_mfma_f32_16x16x32_bf16 v[84:87], a[148:151], a[36:39], v[84:87]
ds_read_b64_tr_b16 a[138:139], v233 offset:32320
v_mfma_f32_16x16x32_bf16 v[88:91], a[148:151], a[40:43], v[88:91]
v_mfma_f32_16x16x32_bf16 v[92:95], a[148:151], a[44:47], v[92:95]
s_waitcnt lgkmcnt(10)
v_mfma_f32_16x16x32_bf16 v[96:99], a[152:155], a[32:35], v[96:99]
ds_read_b64_tr_b16 a[140:141], v233 offset:27744
v_mfma_f32_16x16x32_bf16 v[100:103], a[152:155], a[36:39], v[100:103]
ds_read_b64_tr_b16 a[142:143], v233 offset:32352
v_mfma_f32_16x16x32_bf16 v[104:107], a[152:155], a[40:43], v[104:107]
v_mfma_f32_16x16x32_bf16 v[108:111], a[152:155], a[44:47], v[108:111]
buffer_load_dwordx4 a[28:31], v235, s[44:47], s71 offen offset:3072 sc1
s_add_u32 s71, s71, 4096
s_waitcnt lgkmcnt(10)
v_mfma_f32_16x16x32_bf16 v[112:115], a[156:159], a[32:35], v[112:115]
ds_read_b64_tr_b16 a[144:145], v233 offset:27776
v_mfma_f32_16x16x32_bf16 v[116:119], a[156:159], a[36:39], v[116:119]
ds_read_b64_tr_b16 a[146:147], v233 offset:32384
v_mfma_f32_16x16x32_bf16 v[120:123], a[156:159], a[40:43], v[120:123]
v_mfma_f32_16x16x32_bf16 v[124:127], a[156:159], a[44:47], v[124:127]
s_waitcnt lgkmcnt(10)
v_mfma_f32_16x16x32_bf16 v[128:131], a[160:163], a[32:35], v[128:131]
ds_read_b64_tr_b16 a[148:149], v233 offset:27808
v_mfma_f32_16x16x32_bf16 v[132:135], a[160:163], a[36:39], v[132:135]
ds_read_b64_tr_b16 a[150:151], v233 offset:32416
v_mfma_f32_16x16x32_bf16 v[136:139], a[160:163], a[40:43], v[136:139]
v_mfma_f32_16x16x32_bf16 v[140:143], a[160:163], a[44:47], v[140:143]
s_waitcnt vmcnt(28) lgkmcnt(10)
v_mfma_f32_16x16x32_bf16 v[0:3], a[128:131], a[48:51], v[0:3]
ds_read_b64_tr_b16 a[152:153], v233 offset:27840
v_mfma_f32_16x16x32_bf16 v[4:7], a[128:131], a[52:55], v[4:7]
ds_read_b64_tr_b16 a[154:155], v233 offset:32448
v_mfma_f32_16x16x32_bf16 v[8:11], a[128:131], a[56:59], v[8:11]
v_mfma_f32_16x16x32_bf16 v[12:15], a[128:131], a[60:63], v[12:15]
buffer_load_dwordx4 a[32:35], v235, s[44:47], s71 offen offset:0 sc1
s_waitcnt lgkmcnt(10)
v_mfma_f32_16x16x32_bf16 v[16:19], a[132:135], a[48:51], v[16:19]
ds_read_b64_tr_b16 a[156:157], v233 offset:27872
v_mfma_f32_16x16x32_bf16 v[20:23], a[132:135], a[52:55], v[20:23]
ds_read_b64_tr_b16 a[158:159], v233 offset:32480
v_mfma_f32_16x16x32_bf16 v[24:27], a[132:135], a[56:59], v[24:27]
v_mfma_f32_16x16x32_bf16 v[28:31], a[132:135], a[60:63], v[28:31]
s_waitcnt lgkmcnt(10)
v_mfma_f32_16x16x32_bf16 v[32:35], a[136:139], a[48:51], v[32:35]
ds_read_b64_tr_b16 a[160:161], v233 offset:27904
v_mfma_f32_16x16x32_bf16 v[36:39], a[136:139], a[52:55], v[36:39]
ds_read_b64_tr_b16 a[162:163], v233 offset:32512
v_mfma_f32_16x16x32_bf16 v[40:43], a[136:139], a[56:59], v[40:43]
v_mfma_f32_16x16x32_bf16 v[44:47], a[136:139], a[60:63], v[44:47]
buffer_load_dwordx4 a[36:39], v235, s[44:47], s71 offen offset:1024 sc1
s_waitcnt lgkmcnt(10)
v_mfma_f32_16x16x32_bf16 v[48:51], a[140:143], a[48:51], v[48:51]
s_add_u32 s67, s67, 36864
s_cmp_eq_u32 s67, 147456
s_cselect_b32 s66, s76, 36864
s_cselect_b32 s67, 0, s67
v_mfma_f32_16x16x32_bf16 v[52:55], a[140:143], a[52:55], v[52:55]
v_mfma_f32_16x16x32_bf16 v[56:59], a[140:143], a[56:59], v[56:59]
v_mfma_f32_16x16x32_bf16 v[60:63], a[140:143], a[60:63], v[60:63]
s_waitcnt lgkmcnt(8)
v_mfma_f32_16x16x32_bf16 v[64:67], a[144:147], a[48:51], v[64:67]
v_add_u32 v233, s66, v233
v_mfma_f32_16x16x32_bf16 v[68:71], a[144:147], a[52:55], v[68:71]
v_mfma_f32_16x16x32_bf16 v[72:75], a[144:147], a[56:59], v[72:75]
v_mfma_f32_16x16x32_bf16 v[76:79], a[144:147], a[60:63], v[76:79]
buffer_load_dwordx4 a[40:43], v235, s[44:47], s71 offen offset:2048 sc1
s_waitcnt vmcnt(52) lgkmcnt(0)
s_barrier
v_mfma_f32_16x16x32_bf16 v[80:83], a[148:151], a[48:51], v[80:83]
ds_read_b64_tr_b16 a[128:129], v233 offset:0
ds_read_b64_tr_b16 a[130:131], v233 offset:4608
v_mfma_f32_16x16x32_bf16 v[84:87], a[148:151], a[52:55], v[84:87]
ds_read_b64_tr_b16 a[132:133], v233 offset:32
ds_read_b64_tr_b16 a[134:135], v233 offset:4640
v_mfma_f32_16x16x32_bf16 v[88:91], a[148:151], a[56:59], v[88:91]
v_mfma_f32_16x16x32_bf16 v[92:95], a[148:151], a[60:63], v[92:95]
s_waitcnt lgkmcnt(8)
v_mfma_f32_16x16x32_bf16 v[96:99], a[152:155], a[48:51], v[96:99]
ds_read_b64_tr_b16 a[136:137], v233 offset:64
ds_read_b64_tr_b16 a[138:139], v233 offset:4672
v_mfma_f32_16x16x32_bf16 v[100:103], a[152:155], a[52:55], v[100:103]
ds_read_b64_tr_b16 a[140:141], v233 offset:96
ds_read_b64_tr_b16 a[142:143], v233 offset:4704
v_mfma_f32_16x16x32_bf16 v[104:107], a[152:155], a[56:59], v[104:107]
v_mfma_f32_16x16x32_bf16 v[108:111], a[152:155], a[60:63], v[108:111]
buffer_load_dwordx4 a[44:47], v235, s[44:47], s71 offen offset:3072 sc1
s_add_u32 s71, s71, 4096
s_waitcnt lgkmcnt(10)
v_mfma_f32_16x16x32_bf16 v[112:115], a[156:159], a[48:51], v[112:115]
ds_read_b64_tr_b16 a[144:145], v233 offset:128
v_mfma_f32_16x16x32_bf16 v[116:119], a[156:159], a[52:55], v[116:119]
ds_read_b64_tr_b16 a[146:147], v233 offset:4736
v_mfma_f32_16x16x32_bf16 v[120:123], a[156:159], a[56:59], v[120:123]
v_mfma_f32_16x16x32_bf16 v[124:127], a[156:159], a[60:63], v[124:127]
s_waitcnt lgkmcnt(10)
v_mfma_f32_16x16x32_bf16 v[128:131], a[160:163], a[48:51], v[128:131]
ds_read_b64_tr_b16 a[148:149], v233 offset:160
v_mfma_f32_16x16x32_bf16 v[132:135], a[160:163], a[52:55], v[132:135]
ds_read_b64_tr_b16 a[150:151], v233 offset:4768
v_mfma_f32_16x16x32_bf16 v[136:139], a[160:163], a[56:59], v[136:139]
v_mfma_f32_16x16x32_bf16 v[140:143], a[160:163], a[60:63], v[140:143]
s_waitcnt vmcnt(25) lgkmcnt(10)
v_mfma_f32_16x16x32_bf16 v[0:3], a[128:131], a[64:67], v[0:3]
ds_read_b64_tr_b16 a[152:153], v233 offset:9216
v_mfma_f32_16x16x32_bf16 v[4:7], a[128:131], a[68:71], v[4:7]
ds_read_b64_tr_b16 a[154:155], v233 offset:13824
v_mfma_f32_16x16x32_bf16 v[8:11], a[128:131], a[72:75], v[8:11]
s_add_u32 s67, s67, 36864
s_cmp_eq_u32 s67, 147456
s_cselect_b32 s66, s76, 36864
s_cselect_b32 s67, 0, s67
v_mfma_f32_16x16x32_bf16 v[12:15], a[128:131], a[76:79], v[12:15]
buffer_load_dwordx4 a[48:51], v235, s[44:47], s71 offen offset:0 sc1
s_waitcnt lgkmcnt(10)
v_mfma_f32_16x16x32_bf16 v[16:19], a[132:135], a[64:67], v[16:19]
ds_read_b64_tr_b16 a[156:157], v233 offset:18432
v_mfma_f32_16x16x32_bf16 v[20:23], a[132:135], a[68:71], v[20:23]
ds_read_b64_tr_b16 a[158:159], v233 offset:23040
v_mfma_f32_16x16x32_bf16 v[24:27], a[132:135], a[72:75], v[24:27]
v_add_u32 v234, s66, v233
v_mfma_f32_16x16x32_bf16 v[28:31], a[132:135], a[76:79], v[28:31]
buffer_load_dwordx4 a[52:55], v235, s[44:47], s71 offen offset:1024 sc1
s_waitcnt lgkmcnt(10)
v_mfma_f32_16x16x32_bf16 v[32:35], a[136:139], a[64:67], v[32:35]
ds_read_b64_tr_b16 a[160:161], v233 offset:27648
v_mfma_f32_16x16x32_bf16 v[36:39], a[136:139], a[68:71], v[36:39]
ds_read_b64_tr_b16 a[162:163], v233 offset:32256
v_mfma_f32_16x16x32_bf16 v[40:43], a[136:139], a[72:75], v[40:43]
s_waitcnt vmcnt(26)
s_barrier
v_mfma_f32_16x16x32_bf16 v[44:47], a[136:139], a[76:79], v[44:47]
buffer_load_dwordx4 a[56:59], v235, s[44:47], s71 offen offset:2048 sc1
s_waitcnt lgkmcnt(10)
v_mfma_f32_16x16x32_bf16 v[48:51], a[140:143], a[64:67], v[48:51]
ds_read_b64_tr_b16 a[128:129], v234 offset:0
v_mfma_f32_16x16x32_bf16 v[52:55], a[140:143], a[68:71], v[52:55]
ds_read_b64_tr_b16 a[130:131], v234 offset:4608
v_mfma_f32_16x16x32_bf16 v[56:59], a[140:143], a[72:75], v[56:59]
v_mfma_f32_16x16x32_bf16 v[60:63], a[140:143], a[76:79], v[60:63]
buffer_load_dwordx4 a[60:63], v235, s[44:47], s71 offen offset:3072 sc1
s_waitcnt lgkmcnt(10)
v_mfma_f32_16x16x32_bf16 v[64:67], a[144:147], a[64:67], v[64:67]
ds_read_b64_tr_b16 a[132:133], v234 offset:9216
v_mfma_f32_16x16x32_bf16 v[68:71], a[144:147], a[68:71], v[68:71]
ds_read_b64_tr_b16 a[134:135], v234 offset:13824
v_mfma_f32_16x16x32_bf16 v[72:75], a[144:147], a[72:75], v[72:75]
s_lshl_b32 s88, s34, 3
s_add_u32 s88, s88, s7
s_lshl_b32 s88, s88, 2
s_add_u32 s88, s88, s35
s_lshl_b32 s88, s88, 2
s_add_u32 s88, s88, %[wid]
s_mul_i32 s88, s88, 18432
s_add_u32 s88, s88, 50331648
s_mov_b64 s[48:49], s[24:25]
s_add_u32 s48, s48, s88
s_addc_u32 s49, s49, 0
s_and_b32 s49, s49, 65535
s_mov_b32 s50, 18432
s_mov_b32 s51, 131072
v_mfma_f32_16x16x32_bf16 v[76:79], a[144:147], a[76:79], v[76:79]
s_waitcnt lgkmcnt(10)
v_mfma_f32_16x16x32_bf16 v[80:83], a[148:151], a[64:67], v[80:83]
ds_read_b64_tr_b16 a[136:137], v234 offset:18432
v_mfma_f32_16x16x32_bf16 v[84:87], a[148:151], a[68:71], v[84:87]
ds_read_b64_tr_b16 a[138:139], v234 offset:23040
v_mfma_f32_16x16x32_bf16 v[88:91], a[148:151], a[72:75], v[88:91]
s_lshl_b32 s88, s7, 3
s_add_u32 s88, s88, s34
s_mul_i32 s88, s88, 36864
s_mul_i32 s78, s35, 288
s_add_u32 s88, s88, s78
s_mov_b64 s[56:57], s[8:9]
s_add_u32 s56, s56, s88
s_addc_u32 s57, s57, 0
s_and_b32 s57, s57, 65535
s_mov_b32 s58, 36000
s_mov_b32 s59, 131072
s_mov_b64 s[60:61], s[22:23]
s_add_u32 s60, s60, s88
s_addc_u32 s61, s61, 0
s_and_b32 s61, s61, 65535
s_mov_b32 s62, 36000
s_mov_b32 s63, 131072
v_mfma_f32_16x16x32_bf16 v[92:95], a[148:151], a[76:79], v[92:95]
s_waitcnt vmcnt(24) lgkmcnt(10)
v_mfma_f32_16x16x32_bf16 v[0:3], a[152:155], a[80:83], v[0:3]
ds_read_b64_tr_b16 a[140:141], v233 offset:9248
v_mfma_f32_16x16x32_bf16 v[4:7], a[152:155], a[84:87], v[4:7]
ds_read_b64_tr_b16 a[142:143], v233 offset:13856
v_mfma_f32_16x16x32_bf16 v[8:11], a[152:155], a[88:91], v[8:11]
s_lshl_b32 s88, s7, 2
s_add_u32 s88, s88, s35
s_lshl_b32 s88, s88, 2
s_lshr_b32 s78, s34, 1
s_add_u32 s88, s88, s78
s_mul_i32 s88, s88, 18432
s_and_b32 s78, s34, 1
s_lshl_b32 s78, s78, 10
s_add_u32 s88, s88, s78
s_add_u32 s88, s88, 50331648
s_mov_b64 s[44:45], s[24:25]
s_add_u32 s44, s44, s88
s_addc_u32 s45, s45, 0
s_and_b32 s45, s45, 65535
s_mov_b32 s46, 16532480
s_mov_b32 s47, 131072
v_mfma_f32_16x16x32_bf16 v[12:15], a[152:155], a[92:95], v[12:15]
s_waitcnt vmcnt(20) lgkmcnt(10)
v_mfma_f32_16x16x32_bf16 v[0:3], a[156:159], a[96:99], v[0:3]
ds_read_b64_tr_b16 a[144:145], v233 offset:18464
v_mfma_f32_16x16x32_bf16 v[4:7], a[156:159], a[100:103], v[4:7]
ds_read_b64_tr_b16 a[146:147], v233 offset:23072
v_mfma_f32_16x16x32_bf16 v[8:11], a[156:159], a[104:107], v[8:11]
v_and_b32 v238, 15, v236
v_lshrrev_b32 v239, 4, v236
v_mfma_f32_16x16x32_bf16 v[12:15], a[156:159], a[108:111], v[12:15]
v_mul_u32_u24 v238, 1152, v238
s_and_b32 s78, %[wid], 1
s_mul_i32 s38, s78, 18432
s_lshr_b32 s77, %[wid], 1
s_lshl_b32 s75, s77, 5
s_add_u32 s38, s38, s75
s_lshl_b32 s77, s77, 11
s_lshl_b32 s78, s78, 3
s_add_u32 s77, s77, s78
s_waitcnt vmcnt(16) lgkmcnt(10)
v_mfma_f32_16x16x32_bf16 v[0:3], a[160:163], a[112:115], v[0:3]
ds_read_b64_tr_b16 a[148:149], v233 offset:27680
v_mfma_f32_16x16x32_bf16 v[4:7], a[160:163], a[116:119], v[4:7]
ds_read_b64_tr_b16 a[150:151], v233 offset:32288
v_mfma_f32_16x16x32_bf16 v[8:11], a[160:163], a[120:123], v[8:11]
v_lshl_add_u32 v238, v239, 3, v238
v_add_u32 v241, s77, v235
v_mfma_f32_16x16x32_bf16 v[12:15], a[160:163], a[124:127], v[12:15]
v_add_u32 v240, s38, v238
s_cmp_lt_u32 %[wid], 2
s_cselect_b32 s77, 0, 2130706432
s_waitcnt vmcnt(12) lgkmcnt(10)
v_mfma_f32_16x16x32_bf16 v[0:3], a[128:131], a[0:3], v[0:3]
ds_read_b64_tr_b16 a[152:153], v234 offset:32
v_mfma_f32_16x16x32_bf16 v[4:7], a[128:131], a[4:7], v[4:7]
ds_read_b64_tr_b16 a[154:155], v234 offset:4640
v_mfma_f32_16x16x32_bf16 v[8:11], a[128:131], a[8:11], v[8:11]
v_add_u32 v242, s77, v240
v_add_u32 v243, s77, v241
s_lshl_b32 s92, s35, 8
s_lshr_b32 s78, s34, 1
s_lshl_b32 s78, s78, 3
s_add_u32 s92, s92, s78
s_add_u32 s92, s92, 4096
s_lshl_b32 s39, s35, 3
s_add_u32 s39, s39, s34
s_lshl_b32 s39, s39, 2
s_add_u32 s39, s39, %[wid]
s_lshl_b32 s39, s39, 3
v_mfma_f32_16x16x32_bf16 v[12:15], a[128:131], a[12:15], v[12:15]
s_waitcnt vmcnt(8) lgkmcnt(10)
v_mfma_f32_16x16x32_bf16 v[0:3], a[132:135], a[16:19], v[0:3]
ds_read_b64_tr_b16 a[156:157], v234 offset:9248
v_mfma_f32_16x16x32_bf16 v[4:7], a[132:135], a[20:23], v[4:7]
ds_read_b64_tr_b16 a[158:159], v234 offset:13856
v_mfma_f32_16x16x32_bf16 v[8:11], a[132:135], a[24:27], v[8:11]
buffer_load_dwordx2 v[224:225], v240, s[56:59], 0 offen offset:0
v_mfma_f32_16x16x32_bf16 v[12:15], a[132:135], a[28:31], v[12:15]
buffer_load_dwordx2 v[226:227], v240, s[56:59], 0 offen offset:64
s_waitcnt vmcnt(6) lgkmcnt(10)
v_mfma_f32_16x16x32_bf16 v[0:3], a[136:139], a[32:35], v[0:3]
ds_read_b64_tr_b16 a[160:161], v234 offset:18464
v_mfma_f32_16x16x32_bf16 v[4:7], a[136:139], a[36:39], v[4:7]
ds_read_b64_tr_b16 a[162:163], v234 offset:23072
v_mfma_f32_16x16x32_bf16 v[8:11], a[136:139], a[40:43], v[8:11]
buffer_load_dwordx2 v[228:229], v240, s[56:59], 0 offen offset:128
v_mfma_f32_16x16x32_bf16 v[12:15], a[136:139], a[44:47], v[12:15]
buffer_load_dwordx2 v[230:231], v240, s[56:59], 0 offen offset:192
s_waitcnt lgkmcnt(10)
v_mfma_f32_16x16x32_bf16 v[16:19], a[140:143], a[80:83], v[16:19]
ds_read_b64_tr_b16 a[128:129], v234 offset:27648
v_mfma_f32_16x16x32_bf16 v[20:23], a[140:143], a[84:87], v[20:23]
ds_read_b64_tr_b16 a[130:131], v234 offset:32256
v_mfma_f32_16x16x32_bf16 v[24:27], a[140:143], a[88:91], v[24:27]
buffer_load_dwordx2 v[244:245], v242, s[56:59], 0 offen offset:256
v_mfma_f32_16x16x32_bf16 v[28:31], a[140:143], a[92:95], v[28:31]
s_waitcnt lgkmcnt(10)
v_mfma_f32_16x16x32_bf16 v[16:19], a[144:147], a[96:99], v[16:19]
ds_read_b64_tr_b16 a[132:133], v234 offset:27680
v_mfma_f32_16x16x32_bf16 v[20:23], a[144:147], a[100:103], v[20:23]
ds_read_b64_tr_b16 a[134:135], v234 offset:32288
v_mfma_f32_16x16x32_bf16 v[24:27], a[144:147], a[104:107], v[24:27]
v_mfma_f32_16x16x32_bf16 v[28:31], a[144:147], a[108:111], v[28:31]
s_waitcnt lgkmcnt(10)
v_mfma_f32_16x16x32_bf16 v[16:19], a[148:151], a[112:115], v[16:19]
ds_read_b64_tr_b16 a[136:137], v233 offset:9280
v_mfma_f32_16x16x32_bf16 v[20:23], a[148:151], a[116:119], v[20:23]
ds_read_b64_tr_b16 a[138:139], v233 offset:13888
v_mfma_f32_16x16x32_bf16 v[24:27], a[148:151], a[120:123], v[24:27]
v_mfma_f32_16x16x32_bf16 v[28:31], a[148:151], a[124:127], v[28:31]
s_waitcnt lgkmcnt(10)
v_mfma_f32_16x16x32_bf16 v[16:19], a[152:155], a[0:3], v[16:19]
ds_read_b64_tr_b16 a[140:141], v233 offset:18496
v_mfma_f32_16x16x32_bf16 v[20:23], a[152:155], a[4:7], v[20:23]
ds_read_b64_tr_b16 a[142:143], v233 offset:23104
v_mfma_f32_16x16x32_bf16 v[24:27], a[152:155], a[8:11], v[24:27]
v_mfma_f32_16x16x32_bf16 v[28:31], a[152:155], a[12:15], v[28:31]
s_waitcnt lgkmcnt(10)
v_mfma_f32_16x16x32_bf16 v[16:19], a[156:159], a[16:19], v[16:19]
ds_read_b64_tr_b16 a[144:145], v233 offset:27712
v_mfma_f32_16x16x32_bf16 v[20:23], a[156:159], a[20:23], v[20:23]
ds_read_b64_tr_b16 a[146:147], v233 offset:32320
v_mfma_f32_16x16x32_bf16 v[24:27], a[156:159], a[24:27], v[24:27]
v_mfma_f32_16x16x32_bf16 v[28:31], a[156:159], a[28:31], v[28:31]
)"
R"(
s_waitcnt lgkmcnt(10)
v_mfma_f32_16x16x32_bf16 v[16:19], a[160:163], a[32:35], v[16:19]
ds_read_b64_tr_b16 a[148:149], v234 offset:64
v_mfma_f32_16x16x32_bf16 v[20:23], a[160:163], a[36:39], v[20:23]
ds_read_b64_tr_b16 a[150:151], v234 offset:4672
v_mfma_f32_16x16x32_bf16 v[24:27], a[160:163], a[40:43], v[24:27]
v_mfma_f32_16x16x32_bf16 v[28:31], a[160:163], a[44:47], v[28:31]
s_waitcnt vmcnt(5) lgkmcnt(10)
v_mfma_f32_16x16x32_bf16 v[0:3], a[128:131], a[48:51], v[0:3]
ds_read_b64_tr_b16 a[152:153], v234 offset:9280
v_mfma_f32_16x16x32_bf16 v[4:7], a[128:131], a[52:55], v[4:7]
ds_read_b64_tr_b16 a[154:155], v234 offset:13888
v_mfma_f32_16x16x32_bf16 v[8:11], a[128:131], a[56:59], v[8:11]
v_mfma_f32_16x16x32_bf16 v[12:15], a[128:131], a[60:63], v[12:15]
s_waitcnt lgkmcnt(10)
v_mfma_f32_16x16x32_bf16 v[16:19], a[132:135], a[48:51], v[16:19]
ds_read_b64_tr_b16 a[156:157], v234 offset:18496
v_mfma_f32_16x16x32_bf16 v[20:23], a[132:135], a[52:55], v[20:23]
ds_read_b64_tr_b16 a[158:159], v234 offset:23104
v_mfma_f32_16x16x32_bf16 v[24:27], a[132:135], a[56:59], v[24:27]
v_mfma_f32_16x16x32_bf16 v[28:31], a[132:135], a[60:63], v[28:31]
s_waitcnt lgkmcnt(10)
v_mfma_f32_16x16x32_bf16 v[32:35], a[136:139], a[80:83], v[32:35]
ds_read_b64_tr_b16 a[160:161], v234 offset:27712
v_mfma_f32_16x16x32_bf16 v[36:39], a[136:139], a[84:87], v[36:39]
ds_read_b64_tr_b16 a[162:163], v234 offset:32320
v_mfma_f32_16x16x32_bf16 v[40:43], a[136:139], a[88:91], v[40:43]
v_cvt_pk_bf16_f32 v0, v0, v1
v_cvt_pk_bf16_f32 v1, v2, v3
v_mfma_f32_16x16x32_bf16 v[44:47], a[136:139], a[92:95], v[44:47]
v_cvt_pk_bf16_f32 v2, v4, v5
v_cvt_pk_bf16_f32 v3, v6, v7
s_waitcnt lgkmcnt(10)
v_mfma_f32_16x16x32_bf16 v[32:35], a[140:143], a[96:99], v[32:35]
ds_read_b64_tr_b16 a[128:129], v233 offset:9312
v_mfma_f32_16x16x32_bf16 v[36:39], a[140:143], a[100:103], v[36:39]
ds_read_b64_tr_b16 a[130:131], v233 offset:13920
v_mfma_f32_16x16x32_bf16 v[40:43], a[140:143], a[104:107], v[40:43]
v_cvt_pk_bf16_f32 v8, v8, v9
v_cvt_pk_bf16_f32 v9, v10, v11
v_mfma_f32_16x16x32_bf16 v[44:47], a[140:143], a[108:111], v[44:47]
v_cvt_pk_bf16_f32 v10, v12, v13
v_cvt_pk_bf16_f32 v11, v14, v15
s_waitcnt lgkmcnt(10)
v_mfma_f32_16x16x32_bf16 v[32:35], a[144:147], a[112:115], v[32:35]
ds_read_b64_tr_b16 a[132:133], v233 offset:18528
v_mfma_f32_16x16x32_bf16 v[36:39], a[144:147], a[116:119], v[36:39]
ds_read_b64_tr_b16 a[134:135], v233 offset:23136
v_mfma_f32_16x16x32_bf16 v[40:43], a[144:147], a[120:123], v[40:43]
v_mfma_f32_16x16x32_bf16 v[44:47], a[144:147], a[124:127], v[44:47]
s_waitcnt lgkmcnt(10)
v_mfma_f32_16x16x32_bf16 v[32:35], a[148:151], a[0:3], v[32:35]
ds_read_b64_tr_b16 a[136:137], v233 offset:27744
v_mfma_f32_16x16x32_bf16 v[36:39], a[148:151], a[4:7], v[36:39]
ds_read_b64_tr_b16 a[138:139], v233 offset:32352
v_mfma_f32_16x16x32_bf16 v[40:43], a[148:151], a[8:11], v[40:43]
v_cvt_pk_bf16_f32 v16, v16, v17
v_cvt_pk_bf16_f32 v17, v18, v19
v_mfma_f32_16x16x32_bf16 v[44:47], a[148:151], a[12:15], v[44:47]
s_mov_b32 s72, 0
buffer_store_dwordx4 v[0:3], v235, s[48:51], s72 offen offset:0 sc1
v_cvt_pk_bf16_f32 v18, v20, v21
v_cvt_pk_bf16_f32 v19, v22, v23
s_waitcnt lgkmcnt(10)
v_mfma_f32_16x16x32_bf16 v[32:35], a[152:155], a[16:19], v[32:35]
ds_read_b64_tr_b16 a[140:141], v234 offset:96
v_mfma_f32_16x16x32_bf16 v[36:39], a[152:155], a[20:23], v[36:39]
ds_read_b64_tr_b16 a[142:143], v234 offset:4704
v_mfma_f32_16x16x32_bf16 v[40:43], a[152:155], a[24:27], v[40:43]
v_cvt_pk_bf16_f32 v24, v24, v25
v_cvt_pk_bf16_f32 v25, v26, v27
v_mfma_f32_16x16x32_bf16 v[44:47], a[152:155], a[28:31], v[44:47]
v_cvt_pk_bf16_f32 v26, v28, v29
v_cvt_pk_bf16_f32 v27, v30, v31
s_waitcnt lgkmcnt(10)
v_mfma_f32_16x16x32_bf16 v[32:35], a[156:159], a[32:35], v[32:35]
ds_read_b64_tr_b16 a[144:145], v234 offset:9312
v_mfma_f32_16x16x32_bf16 v[36:39], a[156:159], a[36:39], v[36:39]
ds_read_b64_tr_b16 a[146:147], v234 offset:13920
v_mfma_f32_16x16x32_bf16 v[40:43], a[156:159], a[40:43], v[40:43]
buffer_store_dwordx4 v[8:11], v235, s[48:51], s72 offen offset:1024 sc1
s_mov_b32 s72, 2048
buffer_store_dwordx4 v[16:19], v235, s[48:51], s72 offen offset:0 sc1
v_mfma_f32_16x16x32_bf16 v[44:47], a[156:159], a[44:47], v[44:47]
s_waitcnt lgkmcnt(10)
v_mfma_f32_16x16x32_bf16 v[32:35], a[160:163], a[48:51], v[32:35]
ds_read_b64_tr_b16 a[148:149], v234 offset:18528
v_mfma_f32_16x16x32_bf16 v[36:39], a[160:163], a[52:55], v[36:39]
ds_read_b64_tr_b16 a[150:151], v234 offset:23136
v_mfma_f32_16x16x32_bf16 v[40:43], a[160:163], a[56:59], v[40:43]
v_mfma_f32_16x16x32_bf16 v[44:47], a[160:163], a[60:63], v[44:47]
s_waitcnt lgkmcnt(10)
v_mfma_f32_16x16x32_bf16 v[48:51], a[128:131], a[80:83], v[48:51]
ds_read_b64_tr_b16 a[152:153], v234 offset:27744
v_mfma_f32_16x16x32_bf16 v[52:55], a[128:131], a[84:87], v[52:55]
ds_read_b64_tr_b16 a[154:155], v234 offset:32352
v_mfma_f32_16x16x32_bf16 v[56:59], a[128:131], a[88:91], v[56:59]
v_mfma_f32_16x16x32_bf16 v[60:63], a[128:131], a[92:95], v[60:63]
s_waitcnt lgkmcnt(10)
v_mfma_f32_16x16x32_bf16 v[48:51], a[132:135], a[96:99], v[48:51]
ds_read_b64_tr_b16 a[156:157], v233 offset:9344
v_mfma_f32_16x16x32_bf16 v[52:55], a[132:135], a[100:103], v[52:55]
ds_read_b64_tr_b16 a[158:159], v233 offset:13952
v_mfma_f32_16x16x32_bf16 v[56:59], a[132:135], a[104:107], v[56:59]
buffer_store_dwordx4 v[24:27], v235, s[48:51], s72 offen offset:1024 sc1
v_cvt_pk_bf16_f32 v32, v32, v33
v_cvt_pk_bf16_f32 v33, v34, v35
v_mfma_f32_16x16x32_bf16 v[60:63], a[132:135], a[108:111], v[60:63]
v_cvt_pk_bf16_f32 v34, v36, v37
v_cvt_pk_bf16_f32 v35, v38, v39
s_waitcnt lgkmcnt(10)
v_mfma_f32_16x16x32_bf16 v[48:51], a[136:139], a[112:115], v[48:51]
ds_read_b64_tr_b16 a[160:161], v233 offset:18560
v_mfma_f32_16x16x32_bf16 v[52:55], a[136:139], a[116:119], v[52:55]
ds_read_b64_tr_b16 a[162:163], v233 offset:23168
v_mfma_f32_16x16x32_bf16 v[56:59], a[136:139], a[120:123], v[56:59]
v_cvt_pk_bf16_f32 v40, v40, v41
v_cvt_pk_bf16_f32 v41, v42, v43
v_mfma_f32_16x16x32_bf16 v[60:63], a[136:139], a[124:127], v[60:63]
v_cvt_pk_bf16_f32 v42, v44, v45
v_cvt_pk_bf16_f32 v43, v46, v47
s_waitcnt lgkmcnt(10)
v_mfma_f32_16x16x32_bf16 v[48:51], a[140:143], a[0:3], v[48:51]
ds_read_b64_tr_b16 a[128:129], v233 offset:27776
v_mfma_f32_16x16x32_bf16 v[52:55], a[140:143], a[4:7], v[52:55]
ds_read_b64_tr_b16 a[130:131], v233 offset:32384
v_mfma_f32_16x16x32_bf16 v[56:59], a[140:143], a[8:11], v[56:59]
s_mov_b32 s72, 4096
buffer_store_dwordx4 v[32:35], v235, s[48:51], s72 offen offset:0 sc1
v_mfma_f32_16x16x32_bf16 v[60:63], a[140:143], a[12:15], v[60:63]
s_waitcnt lgkmcnt(10)
v_mfma_f32_16x16x32_bf16 v[48:51], a[144:147], a[16:19], v[48:51]
ds_read_b64_tr_b16 a[132:133], v234 offset:128
v_mfma_f32_16x16x32_bf16 v[52:55], a[144:147], a[20:23], v[52:55]
ds_read_b64_tr_b16 a[134:135], v234 offset:4736
v_mfma_f32_16x16x32_bf16 v[56:59], a[144:147], a[24:27], v[56:59]
v_mfma_f32_16x16x32_bf16 v[60:63], a[144:147], a[28:31], v[60:63]
s_waitcnt lgkmcnt(10)
v_mfma_f32_16x16x32_bf16 v[48:51], a[148:151], a[32:35], v[48:51]
ds_read_b64_tr_b16 a[136:137], v234 offset:9344
v_mfma_f32_16x16x32_bf16 v[52:55], a[148:151], a[36:39], v[52:55]
ds_read_b64_tr_b16 a[138:139], v234 offset:13952
v_mfma_f32_16x16x32_bf16 v[56:59], a[148:151], a[40:43], v[56:59]
v_mfma_f32_16x16x32_bf16 v[60:63], a[148:151], a[44:47], v[60:63]
s_waitcnt lgkmcnt(10)
v_mfma_f32_16x16x32_bf16 v[48:51], a[152:155], a[48:51], v[48:51]
ds_read_b64_tr_b16 a[140:141], v234 offset:18560
v_mfma_f32_16x16x32_bf16 v[52:55], a[152:155], a[52:55], v[52:55]
ds_read_b64_tr_b16 a[142:143], v234 offset:23168
v_mfma_f32_16x16x32_bf16 v[56:59], a[152:155], a[56:59], v[56:59]
buffer_store_dwordx4 v[40:43], v235, s[48:51], s72 offen offset:1024 sc1
v_mfma_f32_16x16x32_bf16 v[60:63], a[152:155], a[60:63], v[60:63]
s_waitcnt lgkmcnt(10)
v_mfma_f32_16x16x32_bf16 v[64:67], a[156:159], a[80:83], v[64:67]
ds_read_b64_tr_b16 a[144:145], v234 offset:27776
v_mfma_f32_16x16x32_bf16 v[68:71], a[156:159], a[84:87], v[68:71]
ds_read_b64_tr_b16 a[146:147], v234 offset:32384
v_mfma_f32_16x16x32_bf16 v[72:75], a[156:159], a[88:91], v[72:75]
v_mfma_f32_16x16x32_bf16 v[76:79], a[156:159], a[92:95], v[76:79]
s_waitcnt lgkmcnt(10)
v_mfma_f32_16x16x32_bf16 v[64:67], a[160:163], a[96:99], v[64:67]
ds_read_b64_tr_b16 a[148:149], v233 offset:9376
v_mfma_f32_16x16x32_bf16 v[68:71], a[160:163], a[100:103], v[68:71]
ds_read_b64_tr_b16 a[150:151], v233 offset:13984
v_mfma_f32_16x16x32_bf16 v[72:75], a[160:163], a[104:107], v[72:75]
v_cvt_pk_bf16_f32 v48, v48, v49
v_cvt_pk_bf16_f32 v49, v50, v51
v_mfma_f32_16x16x32_bf16 v[76:79], a[160:163], a[108:111], v[76:79]
v_cvt_pk_bf16_f32 v50, v52, v53
v_cvt_pk_bf16_f32 v51, v54, v55
s_waitcnt lgkmcnt(10)
v_mfma_f32_16x16x32_bf16 v[64:67], a[128:131], a[112:115], v[64:67]
ds_read_b64_tr_b16 a[152:153], v233 offset:18592
v_mfma_f32_16x16x32_bf16 v[68:71], a[128:131], a[116:119], v[68:71]
ds_read_b64_tr_b16 a[154:155], v233 offset:23200
v_mfma_f32_16x16x32_bf16 v[72:75], a[128:131], a[120:123], v[72:75]
v_cvt_pk_bf16_f32 v56, v56, v57
v_cvt_pk_bf16_f32 v57, v58, v59
v_mfma_f32_16x16x32_bf16 v[76:79], a[128:131], a[124:127], v[76:79]
v_cvt_pk_bf16_f32 v58, v60, v61
v_cvt_pk_bf16_f32 v59, v62, v63
s_waitcnt lgkmcnt(10)
v_mfma_f32_16x16x32_bf16 v[64:67], a[132:135], a[0:3], v[64:67]
ds_read_b64_tr_b16 a[156:157], v233 offset:27808
v_mfma_f32_16x16x32_bf16 v[68:71], a[132:135], a[4:7], v[68:71]
ds_read_b64_tr_b16 a[158:159], v233 offset:32416
v_mfma_f32_16x16x32_bf16 v[72:75], a[132:135], a[8:11], v[72:75]
s_mov_b32 s72, 6144
buffer_store_dwordx4 v[48:51], v235, s[48:51], s72 offen offset:0 sc1
v_mfma_f32_16x16x32_bf16 v[76:79], a[132:135], a[12:15], v[76:79]
s_waitcnt lgkmcnt(10)
v_mfma_f32_16x16x32_bf16 v[64:67], a[136:139], a[16:19], v[64:67]
ds_read_b64_tr_b16 a[160:161], v234 offset:160
v_mfma_f32_16x16x32_bf16 v[68:71], a[136:139], a[20:23], v[68:71]
ds_read_b64_tr_b16 a[162:163], v234 offset:4768
v_mfma_f32_16x16x32_bf16 v[72:75], a[136:139], a[24:27], v[72:75]
v_mfma_f32_16x16x32_bf16 v[76:79], a[136:139], a[28:31], v[76:79]
s_waitcnt lgkmcnt(10)
v_mfma_f32_16x16x32_bf16 v[64:67], a[140:143], a[32:35], v[64:67]
ds_read_b64_tr_b16 a[128:129], v234 offset:9376
v_mfma_f32_16x16x32_bf16 v[68:71], a[140:143], a[36:39], v[68:71]
ds_read_b64_tr_b16 a[130:131], v234 offset:13984
v_mfma_f32_16x16x32_bf16 v[72:75], a[140:143], a[40:43], v[72:75]
v_mfma_f32_16x16x32_bf16 v[76:79], a[140:143], a[44:47], v[76:79]
s_waitcnt lgkmcnt(10)
v_mfma_f32_16x16x32_bf16 v[64:67], a[144:147], a[48:51], v[64:67]
ds_read_b64_tr_b16 a[132:133], v234 offset:18592
v_mfma_f32_16x16x32_bf16 v[68:71], a[144:147], a[52:55], v[68:71]
ds_read_b64_tr_b16 a[134:135], v234 offset:23200
v_mfma_f32_16x16x32_bf16 v[72:75], a[144:147], a[56:59], v[72:75]
buffer_store_dwordx4 v[56:59], v235, s[48:51], s72 offen offset:1024 sc1
v_mfma_f32_16x16x32_bf16 v[76:79], a[144:147], a[60:63], v[76:79]
s_waitcnt lgkmcnt(10)
v_mfma_f32_16x16x32_bf16 v[80:83], a[148:151], a[80:83], v[80:83]
ds_read_b64_tr_b16 a[136:137], v234 offset:27808
v_mfma_f32_16x16x32_bf16 v[84:87], a[148:151], a[84:87], v[84:87]
ds_read_b64_tr_b16 a[138:139], v234 offset:32416
v_mfma_f32_16x16x32_bf16 v[88:91], a[148:151], a[88:91], v[88:91]
v_mfma_f32_16x16x32_bf16 v[92:95], a[148:151], a[92:95], v[92:95]
s_waitcnt lgkmcnt(10)
v_mfma_f32_16x16x32_bf16 v[80:83], a[152:155], a[96:99], v[80:83]
ds_read_b64_tr_b16 a[140:141], v233 offset:192
v_mfma_f32_16x16x32_bf16 v[84:87], a[152:155], a[100:103], v[84:87]
ds_read_b64_tr_b16 a[142:143], v233 offset:4800
v_mfma_f32_16x16x32_bf16 v[88:91], a[152:155], a[104:107], v[88:91]
v_cvt_pk_bf16_f32 v64, v64, v65
v_cvt_pk_bf16_f32 v65, v66, v67
v_mfma_f32_16x16x32_bf16 v[92:95], a[152:155], a[108:111], v[92:95]
v_cvt_pk_bf16_f32 v66, v68, v69
v_cvt_pk_bf16_f32 v67, v70, v71
s_waitcnt lgkmcnt(10)
v_mfma_f32_16x16x32_bf16 v[80:83], a[156:159], a[112:115], v[80:83]
ds_read_b64_tr_b16 a[144:145], v233 offset:9408
v_mfma_f32_16x16x32_bf16 v[84:87], a[156:159], a[116:119], v[84:87]
ds_read_b64_tr_b16 a[146:147], v233 offset:14016
v_mfma_f32_16x16x32_bf16 v[88:91], a[156:159], a[120:123], v[88:91]
v_cvt_pk_bf16_f32 v72, v72, v73
v_cvt_pk_bf16_f32 v73, v74, v75
v_mfma_f32_16x16x32_bf16 v[92:95], a[156:159], a[124:127], v[92:95]
v_cvt_pk_bf16_f32 v74, v76, v77
v_cvt_pk_bf16_f32 v75, v78, v79
s_waitcnt lgkmcnt(10)
v_mfma_f32_16x16x32_bf16 v[80:83], a[160:163], a[0:3], v[80:83]
ds_read_b64_tr_b16 a[148:149], v233 offset:18624
v_mfma_f32_16x16x32_bf16 v[84:87], a[160:163], a[4:7], v[84:87]
ds_read_b64_tr_b16 a[150:151], v233 offset:23232
v_mfma_f32_16x16x32_bf16 v[88:91], a[160:163], a[8:11], v[88:91]
s_mov_b32 s72, 8192
buffer_store_dwordx4 v[64:67], v235, s[48:51], s72 offen offset:0 sc1
v_mfma_f32_16x16x32_bf16 v[92:95], a[160:163], a[12:15], v[92:95]
s_waitcnt lgkmcnt(10)
v_mfma_f32_16x16x32_bf16 v[80:83], a[128:131], a[16:19], v[80:83]
ds_read_b64_tr_b16 a[152:153], v233 offset:27840
v_mfma_f32_16x16x32_bf16 v[84:87], a[128:131], a[20:23], v[84:87]
ds_read_b64_tr_b16 a[154:155], v233 offset:32448
v_mfma_f32_16x16x32_bf16 v[88:91], a[128:131], a[24:27], v[88:91]
v_mfma_f32_16x16x32_bf16 v[92:95], a[128:131], a[28:31], v[92:95]
s_waitcnt lgkmcnt(10)
v_mfma_f32_16x16x32_bf16 v[80:83], a[132:135], a[32:35], v[80:83]
ds_read_b64_tr_b16 a[156:157], v234 offset:192
v_mfma_f32_16x16x32_bf16 v[84:87], a[132:135], a[36:39], v[84:87]
ds_read_b64_tr_b16 a[158:159], v234 offset:4800
v_mfma_f32_16x16x32_bf16 v[88:91], a[132:135], a[40:43], v[88:91]
v_mfma_f32_16x16x32_bf16 v[92:95], a[132:135], a[44:47], v[92:95]
s_waitcnt lgkmcnt(10)
v_mfma_f32_16x16x32_bf16 v[80:83], a[136:139], a[48:51], v[80:83]
ds_read_b64_tr_b16 a[160:161], v234 offset:9408
v_mfma_f32_16x16x32_bf16 v[84:87], a[136:139], a[52:55], v[84:87]
ds_read_b64_tr_b16 a[162:163], v234 offset:14016
v_mfma_f32_16x16x32_bf16 v[88:91], a[136:139], a[56:59], v[88:91]
buffer_store_dwordx4 v[72:75], v235, s[48:51], s72 offen offset:1024 sc1
v_mfma_f32_16x16x32_bf16 v[92:95], a[136:139], a[60:63], v[92:95]
s_waitcnt lgkmcnt(10)
v_mfma_f32_16x16x32_bf16 v[96:99], a[140:143], a[64:67], v[96:99]
ds_read_b64_tr_b16 a[128:129], v234 offset:18624
v_mfma_f32_16x16x32_bf16 v[100:103], a[140:143], a[68:71], v[100:103]
ds_read_b64_tr_b16 a[130:131], v234 offset:23232
v_mfma_f32_16x16x32_bf16 v[104:107], a[140:143], a[72:75], v[104:107]
v_mfma_f32_16x16x32_bf16 v[108:111], a[140:143], a[76:79], v[108:111]
s_waitcnt lgkmcnt(10)
v_mfma_f32_16x16x32_bf16 v[96:99], a[144:147], a[80:83], v[96:99]
ds_read_b64_tr_b16 a[132:133], v234 offset:27840
v_mfma_f32_16x16x32_bf16 v[100:103], a[144:147], a[84:87], v[100:103]
ds_read_b64_tr_b16 a[134:135], v234 offset:32448
v_mfma_f32_16x16x32_bf16 v[104:107], a[144:147], a[88:91], v[104:107]
v_cvt_pk_bf16_f32 v80, v80, v81
v_cvt_pk_bf16_f32 v81, v82, v83
v_mfma_f32_16x16x32_bf16 v[108:111], a[144:147], a[92:95], v[108:111]
v_cvt_pk_bf16_f32 v82, v84, v85
v_cvt_pk_bf16_f32 v83, v86, v87
s_waitcnt lgkmcnt(10)
v_mfma_f32_16x16x32_bf16 v[96:99], a[148:151], a[96:99], v[96:99]
ds_read_b64_tr_b16 a[136:137], v233 offset:224
v_mfma_f32_16x16x32_bf16 v[100:103], a[148:151], a[100:103], v[100:103]
ds_read_b64_tr_b16 a[138:139], v233 offset:4832
v_mfma_f32_16x16x32_bf16 v[104:107], a[148:151], a[104:107], v[104:107]
v_cvt_pk_bf16_f32 v88, v88, v89
v_cvt_pk_bf16_f32 v89, v90, v91
v_mfma_f32_16x16x32_bf16 v[108:111], a[148:151], a[108:111], v[108:111]
v_cvt_pk_bf16_f32 v90, v92, v93
v_cvt_pk_bf16_f32 v91, v94, v95
s_waitcnt lgkmcnt(10)
v_mfma_f32_16x16x32_bf16 v[96:99], a[152:155], a[112:115], v[96:99]
ds_read_b64_tr_b16 a[140:141], v233 offset:9440
v_mfma_f32_16x16x32_bf16 v[100:103], a[152:155], a[116:119], v[100:103]
ds_read_b64_tr_b16 a[142:143], v233 offset:14048
v_mfma_f32_16x16x32_bf16 v[104:107], a[152:155], a[120:123], v[104:107]
s_mov_b32 s72, 10240
buffer_store_dwordx4 v[80:83], v235, s[48:51], s72 offen offset:0 sc1
v_mfma_f32_16x16x32_bf16 v[108:111], a[152:155], a[124:127], v[108:111]
s_waitcnt lgkmcnt(10)
v_mfma_f32_16x16x32_bf16 v[96:99], a[156:159], a[0:3], v[96:99]
ds_read_b64_tr_b16 a[144:145], v233 offset:18656
v_mfma_f32_16x16x32_bf16 v[100:103], a[156:159], a[4:7], v[100:103]
ds_read_b64_tr_b16 a[146:147], v233 offset:23264
v_mfma_f32_16x16x32_bf16 v[104:107], a[156:159], a[8:11], v[104:107]
v_mfma_f32_16x16x32_bf16 v[108:111], a[156:159], a[12:15], v[108:111]
s_waitcnt lgkmcnt(10)
v_mfma_f32_16x16x32_bf16 v[96:99], a[160:163], a[16:19], v[96:99]
ds_read_b64_tr_b16 a[148:149], v233 offset:27872
v_mfma_f32_16x16x32_bf16 v[100:103], a[160:163], a[20:23], v[100:103]
ds_read_b64_tr_b16 a[150:151], v233 offset:32480
v_mfma_f32_16x16x32_bf16 v[104:107], a[160:163], a[24:27], v[104:107]
v_mfma_f32_16x16x32_bf16 v[108:111], a[160:163], a[28:31], v[108:111]
s_waitcnt lgkmcnt(10)
v_mfma_f32_16x16x32_bf16 v[96:99], a[128:131], a[32:35], v[96:99]
ds_read_b64_tr_b16 a[152:153], v234 offset:224
v_mfma_f32_16x16x32_bf16 v[100:103], a[128:131], a[36:39], v[100:103]
ds_read_b64_tr_b16 a[154:155], v234 offset:4832
v_mfma_f32_16x16x32_bf16 v[104:107], a[128:131], a[40:43], v[104:107]
buffer_store_dwordx4 v[88:91], v235, s[48:51], s72 offen offset:1024 sc1
v_mfma_f32_16x16x32_bf16 v[108:111], a[128:131], a[44:47], v[108:111]
s_waitcnt lgkmcnt(10)
v_mfma_f32_16x16x32_bf16 v[96:99], a[132:135], a[48:51], v[96:99]
ds_read_b64_tr_b16 a[156:157], v234 offset:9440
v_mfma_f32_16x16x32_bf16 v[100:103], a[132:135], a[52:55], v[100:103]
ds_read_b64_tr_b16 a[158:159], v234 offset:14048
v_mfma_f32_16x16x32_bf16 v[104:107], a[132:135], a[56:59], v[104:107]
v_mfma_f32_16x16x32_bf16 v[108:111], a[132:135], a[60:63], v[108:111]
s_waitcnt lgkmcnt(10)
v_mfma_f32_16x16x32_bf16 v[112:115], a[136:139], a[64:67], v[112:115]
ds_read_b64_tr_b16 a[160:161], v234 offset:18656
v_mfma_f32_16x16x32_bf16 v[116:119], a[136:139], a[68:71], v[116:119]
ds_read_b64_tr_b16 a[162:163], v234 offset:23264
v_mfma_f32_16x16x32_bf16 v[120:123], a[136:139], a[72:75], v[120:123]
v_mfma_f32_16x16x32_bf16 v[124:127], a[136:139], a[76:79], v[124:127]
s_waitcnt lgkmcnt(10)
v_mfma_f32_16x16x32_bf16 v[112:115], a[140:143], a[80:83], v[112:115]
ds_read_b64_tr_b16 a[128:129], v234 offset:27872
v_mfma_f32_16x16x32_bf16 v[116:119], a[140:143], a[84:87], v[116:119]
ds_read_b64_tr_b16 a[130:131], v234 offset:32480
v_mfma_f32_16x16x32_bf16 v[120:123], a[140:143], a[88:91], v[120:123]
v_cvt_pk_bf16_f32 v96, v96, v97
v_cvt_pk_bf16_f32 v97, v98, v99
v_mfma_f32_16x16x32_bf16 v[124:127], a[140:143], a[92:95], v[124:127]
v_cvt_pk_bf16_f32 v98, v100, v101
v_cvt_pk_bf16_f32 v99, v102, v103
s_waitcnt lgkmcnt(10)
v_mfma_f32_16x16x32_bf16 v[112:115], a[144:147], a[96:99], v[112:115]
ds_read_b64_tr_b16 a[132:133], v233 offset:256
v_mfma_f32_16x16x32_bf16 v[116:119], a[144:147], a[100:103], v[116:119]
ds_read_b64_tr_b16 a[134:135], v233 offset:4864
v_mfma_f32_16x16x32_bf16 v[120:123], a[144:147], a[104:107], v[120:123]
v_cvt_pk_bf16_f32 v104, v104, v105
v_cvt_pk_bf16_f32 v105, v106, v107
v_mfma_f32_16x16x32_bf16 v[124:127], a[144:147], a[108:111], v[124:127]
v_cvt_pk_bf16_f32 v106, v108, v109
v_cvt_pk_bf16_f32 v107, v110, v111
s_waitcnt lgkmcnt(10)
v_mfma_f32_16x16x32_bf16 v[112:115], a[148:151], a[112:115], v[112:115]
ds_read_b64_tr_b16 a[136:137], v233 offset:9472
v_mfma_f32_16x16x32_bf16 v[116:119], a[148:151], a[116:119], v[116:119]
ds_read_b64_tr_b16 a[138:139], v233 offset:14080
v_mfma_f32_16x16x32_bf16 v[120:123], a[148:151], a[120:123], v[120:123]
s_mov_b32 s72, 12288
buffer_store_dwordx4 v[96:99], v235, s[48:51], s72 offen offset:0 sc1
v_mfma_f32_16x16x32_bf16 v[124:127], a[148:151], a[124:127], v[124:127]
s_waitcnt lgkmcnt(10)
v_mfma_f32_16x16x32_bf16 v[112:115], a[152:155], a[0:3], v[112:115]
ds_read_b64_tr_b16 a[140:141], v233 offset:18688
v_mfma_f32_16x16x32_bf16 v[116:119], a[152:155], a[4:7], v[116:119]
ds_read_b64_tr_b16 a[142:143], v233 offset:23296
v_mfma_f32_16x16x32_bf16 v[120:123], a[152:155], a[8:11], v[120:123]
v_mfma_f32_16x16x32_bf16 v[124:127], a[152:155], a[12:15], v[124:127]
s_waitcnt lgkmcnt(10)
v_mfma_f32_16x16x32_bf16 v[112:115], a[156:159], a[16:19], v[112:115]
ds_read_b64_tr_b16 a[144:145], v233 offset:27904
v_mfma_f32_16x16x32_bf16 v[116:119], a[156:159], a[20:23], v[116:119]
ds_read_b64_tr_b16 a[146:147], v233 offset:32512
v_mfma_f32_16x16x32_bf16 v[120:123], a[156:159], a[24:27], v[120:123]
v_mfma_f32_16x16x32_bf16 v[124:127], a[156:159], a[28:31], v[124:127]
s_waitcnt lgkmcnt(10)
v_mfma_f32_16x16x32_bf16 v[112:115], a[160:163], a[32:35], v[112:115]
ds_read_b64_tr_b16 a[148:149], v234 offset:256
v_mfma_f32_16x16x32_bf16 v[116:119], a[160:163], a[36:39], v[116:119]
ds_read_b64_tr_b16 a[150:151], v234 offset:4864
v_mfma_f32_16x16x32_bf16 v[120:123], a[160:163], a[40:43], v[120:123]
buffer_store_dwordx4 v[104:107], v235, s[48:51], s72 offen offset:1024 sc1
v_mfma_f32_16x16x32_bf16 v[124:127], a[160:163], a[44:47], v[124:127]
s_waitcnt lgkmcnt(10)
)"
R"(
v_mfma_f32_16x16x32_bf16 v[112:115], a[128:131], a[48:51], v[112:115]
ds_read_b64_tr_b16 a[152:153], v234 offset:9472
v_mfma_f32_16x16x32_bf16 v[116:119], a[128:131], a[52:55], v[116:119]
ds_read_b64_tr_b16 a[154:155], v234 offset:14080
v_mfma_f32_16x16x32_bf16 v[120:123], a[128:131], a[56:59], v[120:123]
v_mfma_f32_16x16x32_bf16 v[124:127], a[128:131], a[60:63], v[124:127]
s_waitcnt lgkmcnt(10)
v_mfma_f32_16x16x32_bf16 v[128:131], a[132:135], a[64:67], v[128:131]
ds_read_b64_tr_b16 a[156:157], v234 offset:18688
v_mfma_f32_16x16x32_bf16 v[132:135], a[132:135], a[68:71], v[132:135]
ds_read_b64_tr_b16 a[158:159], v234 offset:23296
v_mfma_f32_16x16x32_bf16 v[136:139], a[132:135], a[72:75], v[136:139]
v_mfma_f32_16x16x32_bf16 v[140:143], a[132:135], a[76:79], v[140:143]
s_waitcnt lgkmcnt(10)
v_mfma_f32_16x16x32_bf16 v[128:131], a[136:139], a[80:83], v[128:131]
ds_read_b64_tr_b16 a[160:161], v234 offset:27904
v_mfma_f32_16x16x32_bf16 v[132:135], a[136:139], a[84:87], v[132:135]
ds_read_b64_tr_b16 a[162:163], v234 offset:32512
v_mfma_f32_16x16x32_bf16 v[136:139], a[136:139], a[88:91], v[136:139]
v_cvt_pk_bf16_f32 v112, v112, v113
v_cvt_pk_bf16_f32 v113, v114, v115
v_mfma_f32_16x16x32_bf16 v[140:143], a[136:139], a[92:95], v[140:143]
v_cvt_pk_bf16_f32 v114, v116, v117
v_cvt_pk_bf16_f32 v115, v118, v119
s_waitcnt lgkmcnt(10)
v_mfma_f32_16x16x32_bf16 v[128:131], a[140:143], a[96:99], v[128:131]
v_mfma_f32_16x16x32_bf16 v[132:135], a[140:143], a[100:103], v[132:135]
v_mfma_f32_16x16x32_bf16 v[136:139], a[140:143], a[104:107], v[136:139]
v_cvt_pk_bf16_f32 v120, v120, v121
v_cvt_pk_bf16_f32 v121, v122, v123
v_mfma_f32_16x16x32_bf16 v[140:143], a[140:143], a[108:111], v[140:143]
v_cvt_pk_bf16_f32 v122, v124, v125
v_cvt_pk_bf16_f32 v123, v126, v127
s_waitcnt lgkmcnt(8)
v_mfma_f32_16x16x32_bf16 v[128:131], a[144:147], a[112:115], v[128:131]
v_mfma_f32_16x16x32_bf16 v[132:135], a[144:147], a[116:119], v[132:135]
v_mfma_f32_16x16x32_bf16 v[136:139], a[144:147], a[120:123], v[136:139]
s_mov_b32 s72, 14336
buffer_store_dwordx4 v[112:115], v235, s[48:51], s72 offen offset:0 sc1
v_mfma_f32_16x16x32_bf16 v[140:143], a[144:147], a[124:127], v[140:143]
s_waitcnt lgkmcnt(6)
v_mfma_f32_16x16x32_bf16 v[128:131], a[148:151], a[0:3], v[128:131]
s_waitcnt vmcnt(3)
s_add_u32 s77, s39, 4096
v_mov_b32 v238, s82
v_mov_b32 v239, s83
s_mov_b64 exec, 1
buffer_store_dwordx2 v[238:239], off, s[52:55], s77 offset:0 sc1
s_mov_b64 exec, -1
s_nop 1
v_mfma_f32_16x16x32_bf16 v[132:135], a[148:151], a[4:7], v[132:135]
v_mfma_f32_16x16x32_bf16 v[136:139], a[148:151], a[8:11], v[136:139]
v_mfma_f32_16x16x32_bf16 v[140:143], a[148:151], a[12:15], v[140:143]
s_waitcnt lgkmcnt(4)
v_mfma_f32_16x16x32_bf16 v[128:131], a[152:155], a[16:19], v[128:131]
v_mfma_f32_16x16x32_bf16 v[132:135], a[152:155], a[20:23], v[132:135]
v_mfma_f32_16x16x32_bf16 v[136:139], a[152:155], a[24:27], v[136:139]
v_mfma_f32_16x16x32_bf16 v[140:143], a[152:155], a[28:31], v[140:143]
s_waitcnt lgkmcnt(2)
v_mfma_f32_16x16x32_bf16 v[128:131], a[156:159], a[32:35], v[128:131]
v_mfma_f32_16x16x32_bf16 v[132:135], a[156:159], a[36:39], v[132:135]
v_mfma_f32_16x16x32_bf16 v[136:139], a[156:159], a[40:43], v[136:139]
buffer_store_dwordx4 v[120:123], v235, s[48:51], s72 offen offset:1024 sc1
v_mfma_f32_16x16x32_bf16 v[140:143], a[156:159], a[44:47], v[140:143]
s_waitcnt lgkmcnt(0)
v_mfma_f32_16x16x32_bf16 v[128:131], a[160:163], a[48:51], v[128:131]
v_mfma_f32_16x16x32_bf16 v[132:135], a[160:163], a[52:55], v[132:135]
v_mfma_f32_16x16x32_bf16 v[136:139], a[160:163], a[56:59], v[136:139]
v_mfma_f32_16x16x32_bf16 v[140:143], a[160:163], a[60:63], v[140:143]
s_nop 4
v_cvt_pk_bf16_f32 v128, v128, v129
v_cvt_pk_bf16_f32 v129, v130, v131
v_cvt_pk_bf16_f32 v130, v132, v133
v_cvt_pk_bf16_f32 v131, v134, v135
v_cvt_pk_bf16_f32 v136, v136, v137
v_cvt_pk_bf16_f32 v137, v138, v139
v_cvt_pk_bf16_f32 v138, v140, v141
v_cvt_pk_bf16_f32 v139, v142, v143
s_mov_b32 s72, 16384
buffer_store_dwordx4 v[128:131], v235, s[48:51], s72 offen offset:0 sc1
buffer_store_dwordx4 v[136:139], v235, s[48:51], s72 offen offset:1024 sc1
buffer_load_dwordx2 v[250:251], v252, s[52:55], s92 offen offset:0 sc1
s_waitcnt vmcnt(0)
s_add_u32 s77, s39, 5120
v_mov_b32 v238, s82
v_mov_b32 v239, s83
s_mov_b64 exec, 1
buffer_store_dwordx2 v[238:239], off, s[52:55], s77 offset:0 sc1
s_mov_b64 exec, -1
s_nop 1
v_cmp_ne_u64 vcc, s[82:83], v[250:251]
s_cmp_lg_u64 vcc, 0
s_cbranch_scc1 CK_PA
CK_PA_R:
s_mov_b32 s75, 9437184
buffer_load_dwordx2 v[152:153], v241, s[44:47], s75 offen offset:0 sc1
s_add_u32 s75, s75, 4096
buffer_load_dwordx2 v[168:169], v241, s[44:47], s75 offen offset:0 sc1
s_add_u32 s75, s75, 4096
buffer_load_dwordx2 v[184:185], v241, s[44:47], s75 offen offset:0 sc1
s_mov_b32 s75, 11796480
buffer_load_dwordx2 v[154:155], v241, s[44:47], s75 offen offset:0 sc1
s_add_u32 s75, s75, 4096
buffer_load_dwordx2 v[170:171], v241, s[44:47], s75 offen offset:0 sc1
s_add_u32 s75, s75, 4096
buffer_load_dwordx2 v[186:187], v241, s[44:47], s75 offen offset:0 sc1
s_mov_b32 s75, 14155776
buffer_load_dwordx2 v[156:157], v241, s[44:47], s75 offen offset:0 sc1
s_add_u32 s75, s75, 4096
buffer_load_dwordx2 v[172:173], v241, s[44:47], s75 offen offset:0 sc1
s_add_u32 s75, s75, 4096
buffer_load_dwordx2 v[188:189], v241, s[44:47], s75 offen offset:0 sc1
s_mov_b32 s75, 16515072
buffer_load_dwordx2 v[158:159], v241, s[44:47], s75 offen offset:0 sc1
s_add_u32 s75, s75, 4096
buffer_load_dwordx2 v[174:175], v241, s[44:47], s75 offen offset:0 sc1
s_add_u32 s75, s75, 4096
buffer_load_dwordx2 v[190:191], v241, s[44:47], s75 offen offset:0 sc1
s_mov_b32 s75, 0
buffer_load_dwordx2 v[144:145], v241, s[44:47], s75 offen offset:0 sc1
s_add_u32 s75, s75, 4096
buffer_load_dwordx2 v[160:161], v241, s[44:47], s75 offen offset:0 sc1
s_add_u32 s75, s75, 4096
buffer_load_dwordx2 v[176:177], v241, s[44:47], s75 offen offset:0 sc1
s_mov_b32 s75, 2359296
buffer_load_dwordx2 v[146:147], v241, s[44:47], s75 offen offset:0 sc1
s_add_u32 s75, s75, 4096
buffer_load_dwordx2 v[162:163], v241, s[44:47], s75 offen offset:0 sc1
s_add_u32 s75, s75, 4096
buffer_load_dwordx2 v[178:179], v241, s[44:47], s75 offen offset:0 sc1
s_mov_b32 s75, 4718592
buffer_load_dwordx2 v[148:149], v241, s[44:47], s75 offen offset:0 sc1
s_add_u32 s75, s75, 4096
buffer_load_dwordx2 v[164:165], v241, s[44:47], s75 offen offset:0 sc1
s_add_u32 s75, s75, 4096
buffer_load_dwordx2 v[180:181], v241, s[44:47], s75 offen offset:0 sc1
s_mov_b32 s75, 7077888
buffer_load_dwordx2 v[150:151], v241, s[44:47], s75 offen offset:0 sc1
s_add_u32 s75, s75, 4096
buffer_load_dwordx2 v[166:167], v241, s[44:47], s75 offen offset:0 sc1
s_add_u32 s75, s75, 4096
buffer_load_dwordx2 v[182:183], v241, s[44:47], s75 offen offset:0 sc1
s_add_u32 s92, s92, 1024
buffer_load_dwordx2 v[250:251], v252, s[52:55], s92 offen offset:0 sc1
s_waitcnt vmcnt(0)
v_cmp_ne_u64 vcc, s[82:83], v[250:251]
s_cbranch_vccnz LATEC
s_mov_b32 s75, 9449472
buffer_load_dwordx2 v[200:201], v241, s[44:47], s75 offen offset:0 sc1
s_add_u32 s75, s75, 4096
buffer_load_dwordx2 v[216:217], v243, s[44:47], s75 offen offset:0 sc1
s_mov_b32 s75, 11808768
buffer_load_dwordx2 v[202:203], v241, s[44:47], s75 offen offset:0 sc1
s_add_u32 s75, s75, 4096
buffer_load_dwordx2 v[218:219], v243, s[44:47], s75 offen offset:0 sc1
s_mov_b32 s75, 14168064
buffer_load_dwordx2 v[204:205], v241, s[44:47], s75 offen offset:0 sc1
s_add_u32 s75, s75, 4096
buffer_load_dwordx2 v[220:221], v243, s[44:47], s75 offen offset:0 sc1
s_mov_b32 s75, 16527360
buffer_load_dwordx2 v[206:207], v241, s[44:47], s75 offen offset:0 sc1
s_add_u32 s75, s75, 4096
buffer_load_dwordx2 v[222:223], v243, s[44:47], s75 offen offset:0 sc1
s_mov_b32 s75, 12288
buffer_load_dwordx2 v[192:193], v241, s[44:47], s75 offen offset:0 sc1
s_add_u32 s75, s75, 4096
buffer_load_dwordx2 v[208:209], v243, s[44:47], s75 offen offset:0 sc1
s_mov_b32 s75, 2371584
buffer_load_dwordx2 v[194:195], v241, s[44:47], s75 offen offset:0 sc1
s_add_u32 s75, s75, 4096
buffer_load_dwordx2 v[210:211], v243, s[44:47], s75 offen offset:0 sc1
s_mov_b32 s75, 4730880
buffer_load_dwordx2 v[196:197], v241, s[44:47], s75 offen offset:0 sc1
s_add_u32 s75, s75, 4096
buffer_load_dwordx2 v[212:213], v243, s[44:47], s75 offen offset:0 sc1
s_mov_b32 s75, 7090176
buffer_load_dwordx2 v[198:199], v241, s[44:47], s75 offen offset:0 sc1
s_add_u32 s75, s75, 4096
buffer_load_dwordx2 v[214:215], v243, s[44:47], s75 offen offset:0 sc1
v_dot2_f32_bf16 v80, v152, s90, 0
v_dot2_f32_bf16 v81, v152, s91, 0
v_dot2_f32_bf16 v82, v153, s90, 0
v_dot2_f32_bf16 v83, v153, s91, 0
v_dot2_f32_bf16 v84, v168, s90, 0
v_dot2_f32_bf16 v85, v168, s91, 0
v_dot2_f32_bf16 v86, v169, s90, 0
v_dot2_f32_bf16 v87, v169, s91, 0
v_dot2_f32_bf16 v88, v184, s90, 0
v_dot2_f32_bf16 v89, v184, s91, 0
v_dot2_f32_bf16 v90, v185, s90, 0
v_dot2_f32_bf16 v91, v185, s91, 0
v_dot2_f32_bf16 v80, v154, s90, v80
v_dot2_f32_bf16 v81, v154, s91, v81
v_dot2_f32_bf16 v82, v155, s90, v82
v_dot2_f32_bf16 v83, v155, s91, v83
v_dot2_f32_bf16 v84, v170, s90, v84
v_dot2_f32_bf16 v85, v170, s91, v85
v_dot2_f32_bf16 v86, v171, s90, v86
v_dot2_f32_bf16 v87, v171, s91, v87
v_dot2_f32_bf16 v88, v186, s90, v88
v_dot2_f32_bf16 v89, v186, s91, v89
v_dot2_f32_bf16 v90, v187, s90, v90
v_dot2_f32_bf16 v91, v187, s91, v91
v_dot2_f32_bf16 v80, v156, s90, v80
v_dot2_f32_bf16 v81, v156, s91, v81
v_dot2_f32_bf16 v82, v157, s90, v82
v_dot2_f32_bf16 v83, v157, s91, v83
v_dot2_f32_bf16 v84, v172, s90, v84
v_dot2_f32_bf16 v85, v172, s91, v85
v_dot2_f32_bf16 v86, v173, s90, v86
v_dot2_f32_bf16 v87, v173, s91, v87
v_dot2_f32_bf16 v88, v188, s90, v88
v_dot2_f32_bf16 v89, v188, s91, v89
v_dot2_f32_bf16 v90, v189, s90, v90
v_dot2_f32_bf16 v91, v189, s91, v91
v_dot2_f32_bf16 v80, v158, s90, v80
v_dot2_f32_bf16 v81, v158, s91, v81
v_dot2_f32_bf16 v82, v159, s90, v82
v_dot2_f32_bf16 v83, v159, s91, v83
v_dot2_f32_bf16 v84, v174, s90, v84
v_dot2_f32_bf16 v85, v174, s91, v85
v_dot2_f32_bf16 v86, v175, s90, v86
v_dot2_f32_bf16 v87, v175, s91, v87
v_dot2_f32_bf16 v88, v190, s90, v88
v_dot2_f32_bf16 v89, v190, s91, v89
v_dot2_f32_bf16 v90, v191, s90, v90
v_dot2_f32_bf16 v91, v191, s91, v91
v_lshlrev_b32 v40, 16, v224
v_and_b32 v41, 4294901760, v224
v_lshlrev_b32 v42, 16, v225
v_and_b32 v43, 4294901760, v225
v_lshlrev_b32 v44, 16, v226
v_and_b32 v45, 4294901760, v226
v_lshlrev_b32 v46, 16, v227
v_and_b32 v47, 4294901760, v227
v_lshlrev_b32 v48, 16, v228
v_and_b32 v49, 4294901760, v228
v_lshlrev_b32 v50, 16, v229
v_and_b32 v51, 4294901760, v229
v_mul_f32 v100, 0xc13504f3, v80
v_mul_f32 v101, 0xc13504f3, v81
v_mul_f32 v102, 0xc13504f3, v82
v_mul_f32 v103, 0xc13504f3, v83
v_mul_f32 v104, 0xc13504f3, v84
v_mul_f32 v105, 0xc13504f3, v85
v_mul_f32 v106, 0xc13504f3, v86
v_mul_f32 v107, 0xc13504f3, v87
v_mul_f32 v108, 0xc13504f3, v88
v_mul_f32 v109, 0xc13504f3, v89
v_mul_f32 v110, 0xc13504f3, v90
v_mul_f32 v111, 0xc13504f3, v91
v_exp_f32 v100, v100
v_exp_f32 v101, v101
v_exp_f32 v102, v102
v_exp_f32 v103, v103
v_exp_f32 v104, v104
v_exp_f32 v105, v105
v_exp_f32 v106, v106
v_exp_f32 v107, v107
v_exp_f32 v108, v108
v_exp_f32 v109, v109
v_exp_f32 v110, v110
v_exp_f32 v111, v111
v_dot2_f32_bf16 v20, v144, s90, 0
v_dot2_f32_bf16 v21, v144, s91, 0
v_dot2_f32_bf16 v22, v145, s90, 0
v_dot2_f32_bf16 v23, v145, s91, 0
v_dot2_f32_bf16 v24, v160, s90, 0
v_dot2_f32_bf16 v25, v160, s91, 0
v_dot2_f32_bf16 v26, v161, s90, 0
v_dot2_f32_bf16 v27, v161, s91, 0
v_dot2_f32_bf16 v28, v176, s90, 0
v_dot2_f32_bf16 v29, v176, s91, 0
v_dot2_f32_bf16 v30, v177, s90, 0
v_dot2_f32_bf16 v31, v177, s91, 0
v_dot2_f32_bf16 v20, v146, s90, v20
v_dot2_f32_bf16 v21, v146, s91, v21
v_dot2_f32_bf16 v22, v147, s90, v22
v_dot2_f32_bf16 v23, v147, s91, v23
v_dot2_f32_bf16 v24, v162, s90, v24
v_dot2_f32_bf16 v25, v162, s91, v25
v_dot2_f32_bf16 v26, v163, s90, v26
v_dot2_f32_bf16 v27, v163, s91, v27
v_dot2_f32_bf16 v28, v178, s90, v28
v_dot2_f32_bf16 v29, v178, s91, v29
v_dot2_f32_bf16 v30, v179, s90, v30
v_dot2_f32_bf16 v31, v179, s91, v31
v_dot2_f32_bf16 v20, v148, s90, v20
v_dot2_f32_bf16 v21, v148, s91, v21
v_dot2_f32_bf16 v22, v149, s90, v22
v_dot2_f32_bf16 v23, v149, s91, v23
v_dot2_f32_bf16 v24, v164, s90, v24
v_dot2_f32_bf16 v25, v164, s91, v25
v_dot2_f32_bf16 v26, v165, s90, v26
v_dot2_f32_bf16 v27, v165, s91, v27
v_dot2_f32_bf16 v28, v180, s90, v28
v_dot2_f32_bf16 v29, v180, s91, v29
v_dot2_f32_bf16 v30, v181, s90, v30
v_dot2_f32_bf16 v31, v181, s91, v31
v_dot2_f32_bf16 v20, v150, s90, v20
v_dot2_f32_bf16 v21, v150, s91, v21
v_dot2_f32_bf16 v22, v151, s90, v22
v_dot2_f32_bf16 v23, v151, s91, v23
v_dot2_f32_bf16 v24, v166, s90, v24
v_dot2_f32_bf16 v25, v166, s91, v25
v_dot2_f32_bf16 v26, v167, s90, v26
v_dot2_f32_bf16 v27, v167, s91, v27
v_dot2_f32_bf16 v28, v182, s90, v28
v_dot2_f32_bf16 v29, v182, s91, v29
v_dot2_f32_bf16 v30, v183, s90, v30
v_dot2_f32_bf16 v31, v183, s91, v31
v_add_f32 v100, 1.0, v100
v_add_f32 v101, 1.0, v101
v_add_f32 v102, 1.0, v102
v_add_f32 v103, 1.0, v103
v_add_f32 v104, 1.0, v104
v_add_f32 v105, 1.0, v105
v_add_f32 v106, 1.0, v106
v_add_f32 v107, 1.0, v107
v_add_f32 v108, 1.0, v108
v_add_f32 v109, 1.0, v109
v_add_f32 v110, 1.0, v110
v_add_f32 v111, 1.0, v111
v_rcp_f32 v100, v100
v_rcp_f32 v101, v101
v_rcp_f32 v102, v102
v_rcp_f32 v103, v103
v_rcp_f32 v104, v104
v_rcp_f32 v105, v105
v_rcp_f32 v106, v106
v_rcp_f32 v107, v107
v_rcp_f32 v108, v108
v_rcp_f32 v109, v109
v_rcp_f32 v110, v110
v_rcp_f32 v111, v111
v_mul_f32 v80, v80, v20
v_mul_f32 v81, v81, v21
v_mul_f32 v82, v82, v22
v_mul_f32 v83, v83, v23
v_mul_f32 v84, v84, v24
v_mul_f32 v85, v85, v25
v_mul_f32 v86, v86, v26
v_mul_f32 v87, v87, v27
v_mul_f32 v88, v88, v28
v_mul_f32 v89, v89, v29
v_mul_f32 v90, v90, v30
v_mul_f32 v91, v91, v31
v_fma_f32 v40, v80, v100, v40
v_fma_f32 v41, v81, v101, v41
v_fma_f32 v42, v82, v102, v42
v_fma_f32 v43, v83, v103, v43
v_fma_f32 v44, v84, v104, v44
v_fma_f32 v45, v85, v105, v45
v_fma_f32 v46, v86, v106, v46
v_fma_f32 v47, v87, v107, v47
v_fma_f32 v48, v88, v108, v48
v_fma_f32 v49, v89, v109, v49
v_fma_f32 v50, v90, v110, v50
v_fma_f32 v51, v91, v111, v51
v_cvt_pk_bf16_f32 v0, v40, v41
v_cvt_pk_bf16_f32 v1, v42, v43
buffer_store_dwordx2 v[0:1], v240, s[60:63], 0 offen offset:0
v_cvt_pk_bf16_f32 v2, v44, v45
v_cvt_pk_bf16_f32 v3, v46, v47
buffer_store_dwordx2 v[2:3], v240, s[60:63], 0 offen offset:64
v_cvt_pk_bf16_f32 v4, v48, v49
v_cvt_pk_bf16_f32 v5, v50, v51
buffer_store_dwordx2 v[4:5], v240, s[60:63], 0 offen offset:128
LATECR:
s_waitcnt vmcnt(3)
v_dot2_f32_bf16 v92, v200, s90, 0
v_dot2_f32_bf16 v93, v200, s91, 0
v_dot2_f32_bf16 v94, v201, s90, 0
v_dot2_f32_bf16 v95, v201, s91, 0
v_dot2_f32_bf16 v96, v216, s90, 0
v_dot2_f32_bf16 v97, v216, s91, 0
v_dot2_f32_bf16 v98, v217, s90, 0
v_dot2_f32_bf16 v99, v217, s91, 0
v_dot2_f32_bf16 v92, v202, s90, v92
v_dot2_f32_bf16 v93, v202, s91, v93
v_dot2_f32_bf16 v94, v203, s90, v94
v_dot2_f32_bf16 v95, v203, s91, v95
v_dot2_f32_bf16 v96, v218, s90, v96
v_dot2_f32_bf16 v97, v218, s91, v97
v_dot2_f32_bf16 v98, v219, s90, v98
v_dot2_f32_bf16 v99, v219, s91, v99
v_dot2_f32_bf16 v92, v204, s90, v92
v_dot2_f32_bf16 v93, v204, s91, v93
v_dot2_f32_bf16 v94, v205, s90, v94
v_dot2_f32_bf16 v95, v205, s91, v95
v_dot2_f32_bf16 v96, v220, s90, v96
v_dot2_f32_bf16 v97, v220, s91, v97
v_dot2_f32_bf16 v98, v221, s90, v98
v_dot2_f32_bf16 v99, v221, s91, v99
v_dot2_f32_bf16 v92, v206, s90, v92
v_dot2_f32_bf16 v93, v206, s91, v93
v_dot2_f32_bf16 v94, v207, s90, v94
v_dot2_f32_bf16 v95, v207, s91, v95
v_dot2_f32_bf16 v96, v222, s90, v96
v_dot2_f32_bf16 v97, v222, s91, v97
)"
R"(
v_dot2_f32_bf16 v98, v223, s90, v98
v_dot2_f32_bf16 v99, v223, s91, v99
v_lshlrev_b32 v52, 16, v230
v_and_b32 v53, 4294901760, v230
v_lshlrev_b32 v54, 16, v231
v_and_b32 v55, 4294901760, v231
v_lshlrev_b32 v56, 16, v244
v_and_b32 v57, 4294901760, v244
v_lshlrev_b32 v58, 16, v245
v_and_b32 v59, 4294901760, v245
v_mul_f32 v112, 0xc13504f3, v92
v_mul_f32 v113, 0xc13504f3, v93
v_mul_f32 v114, 0xc13504f3, v94
v_mul_f32 v115, 0xc13504f3, v95
v_mul_f32 v116, 0xc13504f3, v96
v_mul_f32 v117, 0xc13504f3, v97
v_mul_f32 v118, 0xc13504f3, v98
v_mul_f32 v119, 0xc13504f3, v99
v_exp_f32 v112, v112
v_exp_f32 v113, v113
v_exp_f32 v114, v114
v_exp_f32 v115, v115
v_exp_f32 v116, v116
v_exp_f32 v117, v117
v_exp_f32 v118, v118
v_exp_f32 v119, v119
v_dot2_f32_bf16 v32, v192, s90, 0
v_dot2_f32_bf16 v33, v192, s91, 0
v_dot2_f32_bf16 v34, v193, s90, 0
v_dot2_f32_bf16 v35, v193, s91, 0
v_dot2_f32_bf16 v36, v208, s90, 0
v_dot2_f32_bf16 v37, v208, s91, 0
v_dot2_f32_bf16 v38, v209, s90, 0
v_dot2_f32_bf16 v39, v209, s91, 0
v_dot2_f32_bf16 v32, v194, s90, v32
v_dot2_f32_bf16 v33, v194, s91, v33
v_dot2_f32_bf16 v34, v195, s90, v34
v_dot2_f32_bf16 v35, v195, s91, v35
v_dot2_f32_bf16 v36, v210, s90, v36
v_dot2_f32_bf16 v37, v210, s91, v37
v_dot2_f32_bf16 v38, v211, s90, v38
v_dot2_f32_bf16 v39, v211, s91, v39
v_dot2_f32_bf16 v32, v196, s90, v32
v_dot2_f32_bf16 v33, v196, s91, v33
v_dot2_f32_bf16 v34, v197, s90, v34
v_dot2_f32_bf16 v35, v197, s91, v35
v_dot2_f32_bf16 v36, v212, s90, v36
v_dot2_f32_bf16 v37, v212, s91, v37
v_dot2_f32_bf16 v38, v213, s90, v38
v_dot2_f32_bf16 v39, v213, s91, v39
v_dot2_f32_bf16 v32, v198, s90, v32
v_dot2_f32_bf16 v33, v198, s91, v33
v_dot2_f32_bf16 v34, v199, s90, v34
v_dot2_f32_bf16 v35, v199, s91, v35
v_dot2_f32_bf16 v36, v214, s90, v36
v_dot2_f32_bf16 v37, v214, s91, v37
v_dot2_f32_bf16 v38, v215, s90, v38
v_dot2_f32_bf16 v39, v215, s91, v39
v_add_f32 v112, 1.0, v112
v_add_f32 v113, 1.0, v113
v_add_f32 v114, 1.0, v114
v_add_f32 v115, 1.0, v115
v_add_f32 v116, 1.0, v116
v_add_f32 v117, 1.0, v117
v_add_f32 v118, 1.0, v118
v_add_f32 v119, 1.0, v119
v_rcp_f32 v112, v112
v_rcp_f32 v113, v113
v_rcp_f32 v114, v114
v_rcp_f32 v115, v115
v_rcp_f32 v116, v116
v_rcp_f32 v117, v117
v_rcp_f32 v118, v118
v_rcp_f32 v119, v119
v_mul_f32 v92, v92, v32
v_mul_f32 v93, v93, v33
v_mul_f32 v94, v94, v34
v_mul_f32 v95, v95, v35
v_mul_f32 v96, v96, v36
v_mul_f32 v97, v97, v37
v_mul_f32 v98, v98, v38
v_mul_f32 v99, v99, v39
v_fma_f32 v52, v92, v112, v52
v_fma_f32 v53, v93, v113, v53
v_fma_f32 v54, v94, v114, v54
v_fma_f32 v55, v95, v115, v55
v_fma_f32 v56, v96, v116, v56
v_fma_f32 v57, v97, v117, v57
v_fma_f32 v58, v98, v118, v58
v_fma_f32 v59, v99, v119, v59
v_cvt_pk_bf16_f32 v6, v52, v53
v_cvt_pk_bf16_f32 v7, v54, v55
buffer_store_dwordx2 v[6:7], v240, s[60:63], 0 offen offset:192
v_cvt_pk_bf16_f32 v8, v56, v57
v_cvt_pk_bf16_f32 v9, v58, v59
buffer_store_dwordx2 v[8:9], v242, s[60:63], 0 offen offset:256
s_or_b32 s77, s6, %[wid]
s_cbranch_scc1 FEND
v_mov_b32 v238, s82
v_mov_b32 v239, s83
s_mov_b64 exec, 1
buffer_store_dwordx2 v[238:239], off, s[52:55], 0 offset:0 sc1
s_mov_b64 exec, -1
s_nop 1
FEND:
s_waitcnt vmcnt(0) lgkmcnt(0)
s_branch KEND
SLOW4:
s_getpc_b64 s[36:37]
s_branch SLOWG0
s_branch SLOWR4
SLOW5:
s_getpc_b64 s[36:37]
s_branch SLOWG1
s_branch SLOWR5
SLOW6:
s_getpc_b64 s[36:37]
s_branch SLOWG2
s_branch SLOWR6
SLOW7:
s_getpc_b64 s[36:37]
s_branch SLOWG3
s_branch SLOWR7
SLOW8:
s_getpc_b64 s[36:37]
s_branch SLOWG0
s_branch SLOWR8
SLOW9:
s_getpc_b64 s[36:37]
s_branch SLOWG1
s_branch SLOWR9
SLOW10:
s_getpc_b64 s[36:37]
s_branch SLOWG2
s_branch SLOWR10
SLOW11:
s_getpc_b64 s[36:37]
s_branch SLOWG3
s_branch SLOWR11
SLOW12:
s_getpc_b64 s[36:37]
s_branch SLOWG0
s_branch SLOWR12
SLOW13:
s_getpc_b64 s[36:37]
s_branch SLOWG1
s_branch SLOWR13
SLOW14:
s_getpc_b64 s[36:37]
s_branch SLOWG2
s_branch SLOWR14
SLOW15:
s_getpc_b64 s[36:37]
s_branch SLOWG3
s_branch SLOWR15
SLOW16:
s_getpc_b64 s[36:37]
s_branch SLOWG0
s_branch SLOWR16
SLOW17:
s_getpc_b64 s[36:37]
s_branch SLOWG1
s_branch SLOWR17
SLOW18:
s_getpc_b64 s[36:37]
s_branch SLOWG2
s_branch SLOWR18
SLOW19:
s_getpc_b64 s[36:37]
s_branch SLOWG3
s_branch SLOWR19
SLOW20:
s_getpc_b64 s[36:37]
s_branch SLOWG0
s_branch SLOWR20
SLOW21:
s_getpc_b64 s[36:37]
s_branch SLOWG1
s_branch SLOWR21
SLOW22:
s_getpc_b64 s[36:37]
s_branch SLOWG2
s_branch SLOWR22
SLOW23:
s_getpc_b64 s[36:37]
s_branch SLOWG3
s_branch SLOWR23
SLOW24:
s_getpc_b64 s[36:37]
s_branch SLOWG0
s_branch SLOWR24
SLOW25:
s_getpc_b64 s[36:37]
s_branch SLOWG1
s_branch SLOWR25
SLOW26:
s_getpc_b64 s[36:37]
s_branch SLOWG2
s_branch SLOWR26
SLOW27:
s_getpc_b64 s[36:37]
s_branch SLOWG3
s_branch SLOWR27
SLOW28:
s_getpc_b64 s[36:37]
s_branch SLOWG0
s_branch SLOWR28
SLOW29:
s_getpc_b64 s[36:37]
s_branch SLOWG1
s_branch SLOWR29
SLOW30:
s_getpc_b64 s[36:37]
s_branch SLOWG2
s_branch SLOWR30
SLOW31:
s_getpc_b64 s[36:37]
s_branch SLOWG3
s_branch SLOWR31
SLOW32:
s_getpc_b64 s[36:37]
s_branch SLOWG0
s_branch SLOWR32
SLOW33:
s_getpc_b64 s[36:37]
s_branch SLOWG1
s_branch SLOWR33
SLOW34:
s_getpc_b64 s[36:37]
s_branch SLOWG2
s_branch SLOWR34
SLOW35:
s_getpc_b64 s[36:37]
s_branch SLOWG3
s_branch SLOWR35
SLOW36:
s_getpc_b64 s[36:37]
s_branch SLOWG0
s_branch SLOWR36
SLOW37:
s_getpc_b64 s[36:37]
s_branch SLOWG1
s_branch SLOWR37
SLOW38:
s_getpc_b64 s[36:37]
s_branch SLOWG2
s_branch SLOWR38
SLOW39:
s_getpc_b64 s[36:37]
s_branch SLOWG3
s_branch SLOWR39
SLOW40:
s_getpc_b64 s[36:37]
s_branch SLOWG0
s_branch SLOWR40
SLOW41:
s_getpc_b64 s[36:37]
s_branch SLOWG1
s_branch SLOWR41
SLOW42:
s_getpc_b64 s[36:37]
s_branch SLOWG2
s_branch SLOWR42
SLOW43:
s_getpc_b64 s[36:37]
s_branch SLOWG3
s_branch SLOWR43
SLOW44:
s_getpc_b64 s[36:37]
s_branch SLOWG0
s_branch SLOWR44
SLOW45:
s_getpc_b64 s[36:37]
s_branch SLOWG1
s_branch SLOWR45
SLOW46:
s_getpc_b64 s[36:37]
s_branch SLOWG2
s_branch SLOWR46
SLOW47:
s_getpc_b64 s[36:37]
s_branch SLOWG3
s_branch SLOWR47
SLOW48:
s_getpc_b64 s[36:37]
s_branch SLOWG0
s_branch SLOWR48
SLOW49:
s_getpc_b64 s[36:37]
s_branch SLOWG1
s_branch SLOWR49
SLOW50:
s_getpc_b64 s[36:37]
s_branch SLOWG2
s_branch SLOWR50
SLOW51:
s_getpc_b64 s[36:37]
s_branch SLOWG3
s_branch SLOWR51
SLOW52:
s_getpc_b64 s[36:37]
s_branch SLOWG0
s_branch SLOWR52
SLOW53:
s_getpc_b64 s[36:37]
s_branch SLOWG1
s_branch SLOWR53
SLOW54:
s_getpc_b64 s[36:37]
s_branch SLOWG2
s_branch SLOWR54
SLOW55:
s_getpc_b64 s[36:37]
s_branch SLOWG3
s_branch SLOWR55
SLOW56:
s_getpc_b64 s[36:37]
s_branch SLOWG0
s_branch SLOWR56
SLOW57:
s_getpc_b64 s[36:37]
s_branch SLOWG1
s_branch SLOWR57
SLOW58:
s_getpc_b64 s[36:37]
s_branch SLOWG2
s_branch SLOWR58
SLOW59:
s_getpc_b64 s[36:37]
s_branch SLOWG3
s_branch SLOWR59
SLOW60:
s_getpc_b64 s[36:37]
s_branch SLOWG0
s_branch SLOWR60
SLOW61:
s_getpc_b64 s[36:37]
s_branch SLOWG1
s_branch SLOWR61
SLOW62:
s_getpc_b64 s[36:37]
s_branch SLOWG2
s_branch SLOWR62
SLOW63:
s_getpc_b64 s[36:37]
s_branch SLOWG3
s_branch SLOWR63
SLOWG0:
s_nop 7
v_max3_f32 v177, v128, v129, v130
v_max3_f32 v178, v131, v132, v133
v_max3_f32 v177, v177, v134, v135
v_max_f32 v177, v177, v178
v_mov_b32 v178, v177
s_nop 1
v_permlane16_swap_b32 v177, v178
v_max_f32 v177, v177, v178
v_mov_b32 v178, v177
s_nop 1
v_permlane32_swap_b32 v177, v178
v_max_f32 v177, v177, v178
v_add_f32 v177, 0x41500000, v177
v_max_f32 v177, 0, v177
v_sub_f32 v178, 0, v177
v_exp_f32 v178, v178
v_sub_f32 v128, v128, v177
v_sub_f32 v129, v129, v177
v_sub_f32 v130, v130, v177
v_sub_f32 v131, v131, v177
v_sub_f32 v132, v132, v177
v_sub_f32 v133, v133, v177
v_sub_f32 v134, v134, v177
v_sub_f32 v135, v135, v177
v_sub_f32 v160, v160, v177
v_sub_f32 v161, v161, v177
v_sub_f32 v162, v162, v177
v_sub_f32 v163, v163, v177
v_mul_f32 v208, v208, v178
v_mul_f32 v209, v209, v178
v_mul_f32 v210, v210, v178
v_mul_f32 v211, v211, v178
v_mul_f32 v0, v0, v178
v_mul_f32 v1, v1, v178
v_mul_f32 v2, v2, v178
v_mul_f32 v3, v3, v178
v_mul_f32 v16, v16, v178
v_mul_f32 v17, v17, v178
v_mul_f32 v18, v18, v178
v_mul_f32 v19, v19, v178
v_mul_f32 v32, v32, v178
v_mul_f32 v33, v33, v178
v_mul_f32 v34, v34, v178
v_mul_f32 v35, v35, v178
v_mul_f32 v48, v48, v178
v_mul_f32 v49, v49, v178
v_mul_f32 v50, v50, v178
v_mul_f32 v51, v51, v178
v_mul_f32 v64, v64, v178
v_mul_f32 v65, v65, v178
v_mul_f32 v66, v66, v178
)"
R"(
v_mul_f32 v67, v67, v178
v_mul_f32 v80, v80, v178
v_mul_f32 v81, v81, v178
v_mul_f32 v82, v82, v178
v_mul_f32 v83, v83, v178
v_mul_f32 v96, v96, v178
v_mul_f32 v97, v97, v178
v_mul_f32 v98, v98, v178
v_mul_f32 v99, v99, v178
v_mul_f32 v112, v112, v178
v_mul_f32 v113, v113, v178
v_mul_f32 v114, v114, v178
v_mul_f32 v115, v115, v178
v_exp_f32 v144, v128
v_exp_f32 v145, v129
v_exp_f32 v146, v130
v_exp_f32 v147, v131
v_exp_f32 v148, v132
v_exp_f32 v149, v133
v_exp_f32 v150, v134
v_exp_f32 v151, v135
v_cvt_pk_bf16_f32 v152, v144, v145
v_cvt_pk_bf16_f32 v153, v146, v147
v_cvt_pk_bf16_f32 v154, v148, v149
v_cvt_pk_bf16_f32 v155, v150, v151
s_add_u32 s36, s36, 4
s_addc_u32 s37, s37, 0
s_setpc_b64 s[36:37]
SLOWG1:
s_nop 7
v_max3_f32 v177, v136, v137, v138
v_max3_f32 v178, v139, v140, v141
v_max3_f32 v177, v177, v142, v143
v_max_f32 v177, v177, v178
v_mov_b32 v178, v177
s_nop 1
v_permlane16_swap_b32 v177, v178
v_max_f32 v177, v177, v178
v_mov_b32 v178, v177
s_nop 1
v_permlane32_swap_b32 v177, v178
v_max_f32 v177, v177, v178
v_add_f32 v177, 0x41500000, v177
v_max_f32 v177, 0, v177
v_sub_f32 v178, 0, v177
v_exp_f32 v178, v178
v_sub_f32 v136, v136, v177
v_sub_f32 v137, v137, v177
v_sub_f32 v138, v138, v177
v_sub_f32 v139, v139, v177
v_sub_f32 v140, v140, v177
v_sub_f32 v141, v141, v177
v_sub_f32 v142, v142, v177
v_sub_f32 v143, v143, v177
v_sub_f32 v164, v164, v177
v_sub_f32 v165, v165, v177
v_sub_f32 v166, v166, v177
v_sub_f32 v167, v167, v177
v_mul_f32 v212, v212, v178
v_mul_f32 v213, v213, v178
v_mul_f32 v214, v214, v178
v_mul_f32 v215, v215, v178
v_mul_f32 v4, v4, v178
v_mul_f32 v5, v5, v178
v_mul_f32 v6, v6, v178
v_mul_f32 v7, v7, v178
v_mul_f32 v20, v20, v178
v_mul_f32 v21, v21, v178
v_mul_f32 v22, v22, v178
v_mul_f32 v23, v23, v178
v_mul_f32 v36, v36, v178
v_mul_f32 v37, v37, v178
v_mul_f32 v38, v38, v178
v_mul_f32 v39, v39, v178
v_mul_f32 v52, v52, v178
v_mul_f32 v53, v53, v178
v_mul_f32 v54, v54, v178
v_mul_f32 v55, v55, v178
v_mul_f32 v68, v68, v178
v_mul_f32 v69, v69, v178
v_mul_f32 v70, v70, v178
v_mul_f32 v71, v71, v178
v_mul_f32 v84, v84, v178
v_mul_f32 v85, v85, v178
v_mul_f32 v86, v86, v178
v_mul_f32 v87, v87, v178
v_mul_f32 v100, v100, v178
v_mul_f32 v101, v101, v178
v_mul_f32 v102, v102, v178
v_mul_f32 v103, v103, v178
v_mul_f32 v116, v116, v178
v_mul_f32 v117, v117, v178
v_mul_f32 v118, v118, v178
v_mul_f32 v119, v119, v178
v_exp_f32 v144, v136
v_exp_f32 v145, v137
v_exp_f32 v146, v138
v_exp_f32 v147, v139
v_exp_f32 v148, v140
v_exp_f32 v149, v141
v_exp_f32 v150, v142
v_exp_f32 v151, v143
v_cvt_pk_bf16_f32 v156, v144, v145
v_cvt_pk_bf16_f32 v157, v146, v147
v_cvt_pk_bf16_f32 v158, v148, v149
v_cvt_pk_bf16_f32 v159, v150, v151
s_add_u32 s36, s36, 4
s_addc_u32 s37, s37, 0
s_setpc_b64 s[36:37]
SLOWG2:
s_nop 7
v_max3_f32 v177, v128, v129, v130
v_max3_f32 v178, v131, v132, v133
v_max3_f32 v177, v177, v134, v135
v_max_f32 v177, v177, v178
v_mov_b32 v178, v177
s_nop 1
v_permlane16_swap_b32 v177, v178
v_max_f32 v177, v177, v178
v_mov_b32 v178, v177
s_nop 1
v_permlane32_swap_b32 v177, v178
v_max_f32 v177, v177, v178
v_add_f32 v177, 0x41500000, v177
v_max_f32 v177, 0, v177
v_sub_f32 v178, 0, v177
v_exp_f32 v178, v178
v_sub_f32 v128, v128, v177
v_sub_f32 v129, v129, v177
v_sub_f32 v130, v130, v177
v_sub_f32 v131, v131, v177
v_sub_f32 v132, v132, v177
v_sub_f32 v133, v133, v177
v_sub_f32 v134, v134, v177
v_sub_f32 v135, v135, v177
v_sub_f32 v168, v168, v177
v_sub_f32 v169, v169, v177
v_sub_f32 v170, v170, v177
v_sub_f32 v171, v171, v177
v_mul_f32 v216, v216, v178
v_mul_f32 v217, v217, v178
v_mul_f32 v218, v218, v178
v_mul_f32 v219, v219, v178
v_mul_f32 v8, v8, v178
v_mul_f32 v9, v9, v178
v_mul_f32 v10, v10, v178
v_mul_f32 v11, v11, v178
v_mul_f32 v24, v24, v178
v_mul_f32 v25, v25, v178
v_mul_f32 v26, v26, v178
v_mul_f32 v27, v27, v178
v_mul_f32 v40, v40, v178
v_mul_f32 v41, v41, v178
v_mul_f32 v42, v42, v178
v_mul_f32 v43, v43, v178
v_mul_f32 v56, v56, v178
v_mul_f32 v57, v57, v178
v_mul_f32 v58, v58, v178
v_mul_f32 v59, v59, v178
v_mul_f32 v72, v72, v178
v_mul_f32 v73, v73, v178
v_mul_f32 v74, v74, v178
v_mul_f32 v75, v75, v178
v_mul_f32 v88, v88, v178
v_mul_f32 v89, v89, v178
v_mul_f32 v90, v90, v178
v_mul_f32 v91, v91, v178
v_mul_f32 v104, v104, v178
v_mul_f32 v105, v105, v178
v_mul_f32 v106, v106, v178
v_mul_f32 v107, v107, v178
v_mul_f32 v120, v120, v178
v_mul_f32 v121, v121, v178
v_mul_f32 v122, v122, v178
v_mul_f32 v123, v123, v178
v_exp_f32 v144, v128
v_exp_f32 v145, v129
v_exp_f32 v146, v130
v_exp_f32 v147, v131
v_exp_f32 v148, v132
v_exp_f32 v149, v133
v_exp_f32 v150, v134
v_exp_f32 v151, v135
v_cvt_pk_bf16_f32 v152, v144, v145
v_cvt_pk_bf16_f32 v153, v146, v147
v_cvt_pk_bf16_f32 v154, v148, v149
v_cvt_pk_bf16_f32 v155, v150, v151
s_add_u32 s36, s36, 4
s_addc_u32 s37, s37, 0
s_setpc_b64 s[36:37]
SLOWG3:
s_nop 7
v_max3_f32 v177, v136, v137, v138
v_max3_f32 v178, v139, v140, v141
v_max3_f32 v177, v177, v142, v143
v_max_f32 v177, v177, v178
v_mov_b32 v178, v177
s_nop 1
v_permlane16_swap_b32 v177, v178
v_max_f32 v177, v177, v178
v_mov_b32 v178, v177
s_nop 1
v_permlane32_swap_b32 v177, v178
v_max_f32 v177, v177, v178
v_add_f32 v177, 0x41500000, v177
v_max_f32 v177, 0, v177
v_sub_f32 v178, 0, v177
v_exp_f32 v178, v178
v_sub_f32 v136, v136, v177
v_sub_f32 v137, v137, v177
v_sub_f32 v138, v138, v177
v_sub_f32 v139, v139, v177
v_sub_f32 v140, v140, v177
v_sub_f32 v141, v141, v177
v_sub_f32 v142, v142, v177
v_sub_f32 v143, v143, v177
v_sub_f32 v172, v172, v177
v_sub_f32 v173, v173, v177
v_sub_f32 v174, v174, v177
v_sub_f32 v175, v175, v177
v_mul_f32 v220, v220, v178
v_mul_f32 v221, v221, v178
v_mul_f32 v222, v222, v178
v_mul_f32 v223, v223, v178
v_mul_f32 v12, v12, v178
v_mul_f32 v13, v13, v178
v_mul_f32 v14, v14, v178
v_mul_f32 v15, v15, v178
v_mul_f32 v28, v28, v178
v_mul_f32 v29, v29, v178
v_mul_f32 v30, v30, v178
v_mul_f32 v31, v31, v178
v_mul_f32 v44, v44, v178
v_mul_f32 v45, v45, v178
v_mul_f32 v46, v46, v178
v_mul_f32 v47, v47, v178
v_mul_f32 v60, v60, v178
v_mul_f32 v61, v61, v178
v_mul_f32 v62, v62, v178
v_mul_f32 v63, v63, v178
v_mul_f32 v76, v76, v178
v_mul_f32 v77, v77, v178
v_mul_f32 v78, v78, v178
v_mul_f32 v79, v79, v178
v_mul_f32 v92, v92, v178
v_mul_f32 v93, v93, v178
v_mul_f32 v94, v94, v178
v_mul_f32 v95, v95, v178
v_mul_f32 v108, v108, v178
v_mul_f32 v109, v109, v178
v_mul_f32 v110, v110, v178
v_mul_f32 v111, v111, v178
v_mul_f32 v124, v124, v178
v_mul_f32 v125, v125, v178
v_mul_f32 v126, v126, v178
v_mul_f32 v127, v127, v178
v_exp_f32 v144, v136
v_exp_f32 v145, v137
v_exp_f32 v146, v138
v_exp_f32 v147, v139
v_exp_f32 v148, v140
v_exp_f32 v149, v141
v_exp_f32 v150, v142
v_exp_f32 v151, v143
v_cvt_pk_bf16_f32 v156, v144, v145
v_cvt_pk_bf16_f32 v157, v146, v147
v_cvt_pk_bf16_f32 v158, v148, v149
v_cvt_pk_bf16_f32 v159, v150, v151
s_add_u32 s36, s36, 4
s_addc_u32 s37, s37, 0
s_setpc_b64 s[36:37]
CK_H:
s_mov_b32 s86, 0
CK_H_P:
buffer_load_dwordx2 v[250:251], v252, s[52:55], s92 offen offset:0 sc1
s_waitcnt vmcnt(0)
v_cmp_ne_u64 vcc, s[82:83], v[250:251]
s_cbranch_vccz CK_H_P_OK
s_sleep 1
s_add_u32 s86, s86, 1
s_cmp_lt_u32 s86, 400000
s_cbranch_scc1 CK_H_P
CK_H_P_OK:
s_branch CK_H_R
CK_O1:
s_mov_b32 s86, 0
CK_O1_P:
buffer_load_dwordx2 v[250:251], v252, s[52:55], s92 offen offset:0 sc1
s_waitcnt vmcnt(0)
v_cmp_ne_u64 vcc, s[82:83], v[250:251]
s_cbranch_vccz CK_O1_P_OK
s_sleep 1
s_add_u32 s86, s86, 1
s_cmp_lt_u32 s86, 400000
s_cbranch_scc1 CK_O1_P
CK_O1_P_OK:
s_branch CK_O1_R
CK_PA:
s_mov_b32 s86, 0
CK_PA_P:
buffer_load_dwordx2 v[250:251], v252, s[52:55], s92 offen offset:0 sc1
s_waitcnt vmcnt(0)
v_cmp_ne_u64 vcc, s[82:83], v[250:251]
s_cbranch_vccz CK_PA_P_OK
s_sleep 1
s_add_u32 s86, s86, 1
s_cmp_lt_u32 s86, 400000
s_cbranch_scc1 CK_PA_P
CK_PA_P_OK:
s_branch CK_PA_R
LATEC:
v_dot2_f32_bf16 v80, v152, s90, 0
v_dot2_f32_bf16 v81, v152, s91, 0
v_dot2_f32_bf16 v82, v153, s90, 0
v_dot2_f32_bf16 v83, v153, s91, 0
v_dot2_f32_bf16 v84, v168, s90, 0
v_dot2_f32_bf16 v85, v168, s91, 0
v_dot2_f32_bf16 v86, v169, s90, 0
v_dot2_f32_bf16 v87, v169, s91, 0
v_dot2_f32_bf16 v88, v184, s90, 0
v_dot2_f32_bf16 v89, v184, s91, 0
v_dot2_f32_bf16 v90, v185, s90, 0
v_dot2_f32_bf16 v91, v185, s91, 0
v_dot2_f32_bf16 v80, v154, s90, v80
v_dot2_f32_bf16 v81, v154, s91, v81
v_dot2_f32_bf16 v82, v155, s90, v82
v_dot2_f32_bf16 v83, v155, s91, v83
v_dot2_f32_bf16 v84, v170, s90, v84
v_dot2_f32_bf16 v85, v170, s91, v85
v_dot2_f32_bf16 v86, v171, s90, v86
v_dot2_f32_bf16 v87, v171, s91, v87
v_dot2_f32_bf16 v88, v186, s90, v88
v_dot2_f32_bf16 v89, v186, s91, v89
v_dot2_f32_bf16 v90, v187, s90, v90
v_dot2_f32_bf16 v91, v187, s91, v91
v_dot2_f32_bf16 v80, v156, s90, v80
v_dot2_f32_bf16 v81, v156, s91, v81
v_dot2_f32_bf16 v82, v157, s90, v82
v_dot2_f32_bf16 v83, v157, s91, v83
v_dot2_f32_bf16 v84, v172, s90, v84
v_dot2_f32_bf16 v85, v172, s91, v85
v_dot2_f32_bf16 v86, v173, s90, v86
v_dot2_f32_bf16 v87, v173, s91, v87
v_dot2_f32_bf16 v88, v188, s90, v88
v_dot2_f32_bf16 v89, v188, s91, v89
v_dot2_f32_bf16 v90, v189, s90, v90
v_dot2_f32_bf16 v91, v189, s91, v91
v_dot2_f32_bf16 v80, v158, s90, v80
v_dot2_f32_bf16 v81, v158, s91, v81
v_dot2_f32_bf16 v82, v159, s90, v82
v_dot2_f32_bf16 v83, v159, s91, v83
v_dot2_f32_bf16 v84, v174, s90, v84
v_dot2_f32_bf16 v85, v174, s91, v85
v_dot2_f32_bf16 v86, v175, s90, v86
v_dot2_f32_bf16 v87, v175, s91, v87
v_dot2_f32_bf16 v88, v190, s90, v88
v_dot2_f32_bf16 v89, v190, s91, v89
v_dot2_f32_bf16 v90, v191, s90, v90
v_dot2_f32_bf16 v91, v191, s91, v91
v_lshlrev_b32 v40, 16, v224
v_and_b32 v41, 4294901760, v224
v_lshlrev_b32 v42, 16, v225
v_and_b32 v43, 4294901760, v225
v_lshlrev_b32 v44, 16, v226
v_and_b32 v45, 4294901760, v226
v_lshlrev_b32 v46, 16, v227
v_and_b32 v47, 4294901760, v227
v_lshlrev_b32 v48, 16, v228
v_and_b32 v49, 4294901760, v228
v_lshlrev_b32 v50, 16, v229
v_and_b32 v51, 4294901760, v229
v_mul_f32 v100, 0xc13504f3, v80
v_mul_f32 v101, 0xc13504f3, v81
v_mul_f32 v102, 0xc13504f3, v82
v_mul_f32 v103, 0xc13504f3, v83
v_mul_f32 v104, 0xc13504f3, v84
v_mul_f32 v105, 0xc13504f3, v85
v_mul_f32 v106, 0xc13504f3, v86
v_mul_f32 v107, 0xc13504f3, v87
v_mul_f32 v108, 0xc13504f3, v88
v_mul_f32 v109, 0xc13504f3, v89
v_mul_f32 v110, 0xc13504f3, v90
v_mul_f32 v111, 0xc13504f3, v91
v_exp_f32 v100, v100
v_exp_f32 v101, v101
v_exp_f32 v102, v102
v_exp_f32 v103, v103
v_exp_f32 v104, v104
v_exp_f32 v105, v105
v_exp_f32 v106, v106
v_exp_f32 v107, v107
v_exp_f32 v108, v108
v_exp_f32 v109, v109
v_exp_f32 v110, v110
v_exp_f32 v111, v111
v_dot2_f32_bf16 v20, v144, s90, 0
v_dot2_f32_bf16 v21, v144, s91, 0
v_dot2_f32_bf16 v22, v145, s90, 0
v_dot2_f32_bf16 v23, v145, s91, 0
v_dot2_f32_bf16 v24, v160, s90, 0
)"
R"(
v_dot2_f32_bf16 v25, v160, s91, 0
v_dot2_f32_bf16 v26, v161, s90, 0
v_dot2_f32_bf16 v27, v161, s91, 0
v_dot2_f32_bf16 v28, v176, s90, 0
v_dot2_f32_bf16 v29, v176, s91, 0
v_dot2_f32_bf16 v30, v177, s90, 0
v_dot2_f32_bf16 v31, v177, s91, 0
v_dot2_f32_bf16 v20, v146, s90, v20
v_dot2_f32_bf16 v21, v146, s91, v21
v_dot2_f32_bf16 v22, v147, s90, v22
v_dot2_f32_bf16 v23, v147, s91, v23
v_dot2_f32_bf16 v24, v162, s90, v24
v_dot2_f32_bf16 v25, v162, s91, v25
v_dot2_f32_bf16 v26, v163, s90, v26
v_dot2_f32_bf16 v27, v163, s91, v27
v_dot2_f32_bf16 v28, v178, s90, v28
v_dot2_f32_bf16 v29, v178, s91, v29
v_dot2_f32_bf16 v30, v179, s90, v30
v_dot2_f32_bf16 v31, v179, s91, v31
v_dot2_f32_bf16 v20, v148, s90, v20
v_dot2_f32_bf16 v21, v148, s91, v21
v_dot2_f32_bf16 v22, v149, s90, v22
v_dot2_f32_bf16 v23, v149, s91, v23
v_dot2_f32_bf16 v24, v164, s90, v24
v_dot2_f32_bf16 v25, v164, s91, v25
v_dot2_f32_bf16 v26, v165, s90, v26
v_dot2_f32_bf16 v27, v165, s91, v27
v_dot2_f32_bf16 v28, v180, s90, v28
v_dot2_f32_bf16 v29, v180, s91, v29
v_dot2_f32_bf16 v30, v181, s90, v30
v_dot2_f32_bf16 v31, v181, s91, v31
v_dot2_f32_bf16 v20, v150, s90, v20
v_dot2_f32_bf16 v21, v150, s91, v21
v_dot2_f32_bf16 v22, v151, s90, v22
v_dot2_f32_bf16 v23, v151, s91, v23
v_dot2_f32_bf16 v24, v166, s90, v24
v_dot2_f32_bf16 v25, v166, s91, v25
v_dot2_f32_bf16 v26, v167, s90, v26
v_dot2_f32_bf16 v27, v167, s91, v27
v_dot2_f32_bf16 v28, v182, s90, v28
v_dot2_f32_bf16 v29, v182, s91, v29
v_dot2_f32_bf16 v30, v183, s90, v30
v_dot2_f32_bf16 v31, v183, s91, v31
v_add_f32 v100, 1.0, v100
v_add_f32 v101, 1.0, v101
v_add_f32 v102, 1.0, v102
v_add_f32 v103, 1.0, v103
v_add_f32 v104, 1.0, v104
v_add_f32 v105, 1.0, v105
v_add_f32 v106, 1.0, v106
v_add_f32 v107, 1.0, v107
v_add_f32 v108, 1.0, v108
v_add_f32 v109, 1.0, v109
v_add_f32 v110, 1.0, v110
v_add_f32 v111, 1.0, v111
v_rcp_f32 v100, v100
v_rcp_f32 v101, v101
v_rcp_f32 v102, v102
v_rcp_f32 v103, v103
v_rcp_f32 v104, v104
v_rcp_f32 v105, v105
v_rcp_f32 v106, v106
v_rcp_f32 v107, v107
v_rcp_f32 v108, v108
v_rcp_f32 v109, v109
v_rcp_f32 v110, v110
v_rcp_f32 v111, v111
v_mul_f32 v80, v80, v20
v_mul_f32 v81, v81, v21
v_mul_f32 v82, v82, v22
v_mul_f32 v83, v83, v23
v_mul_f32 v84, v84, v24
v_mul_f32 v85, v85, v25
v_mul_f32 v86, v86, v26
v_mul_f32 v87, v87, v27
v_mul_f32 v88, v88, v28
v_mul_f32 v89, v89, v29
v_mul_f32 v90, v90, v30
v_mul_f32 v91, v91, v31
v_fma_f32 v40, v80, v100, v40
v_fma_f32 v41, v81, v101, v41
v_fma_f32 v42, v82, v102, v42
v_fma_f32 v43, v83, v103, v43
v_fma_f32 v44, v84, v104, v44
v_fma_f32 v45, v85, v105, v45
v_fma_f32 v46, v86, v106, v46
v_fma_f32 v47, v87, v107, v47
v_fma_f32 v48, v88, v108, v48
v_fma_f32 v49, v89, v109, v49
v_fma_f32 v50, v90, v110, v50
v_fma_f32 v51, v91, v111, v51
v_cvt_pk_bf16_f32 v0, v40, v41
v_cvt_pk_bf16_f32 v1, v42, v43
buffer_store_dwordx2 v[0:1], v240, s[60:63], 0 offen offset:0
v_cvt_pk_bf16_f32 v2, v44, v45
v_cvt_pk_bf16_f32 v3, v46, v47
buffer_store_dwordx2 v[2:3], v240, s[60:63], 0 offen offset:64
v_cvt_pk_bf16_f32 v4, v48, v49
v_cvt_pk_bf16_f32 v5, v50, v51
buffer_store_dwordx2 v[4:5], v240, s[60:63], 0 offen offset:128
s_mov_b32 s86, 0
P_PC:
buffer_load_dwordx2 v[250:251], v252, s[52:55], s92 offen offset:0 sc1
s_waitcnt vmcnt(0)
v_cmp_ne_u64 vcc, s[82:83], v[250:251]
s_cbranch_vccz P_PC_OK
s_sleep 1
s_add_u32 s86, s86, 1
s_cmp_lt_u32 s86, 400000
s_cbranch_scc1 P_PC
P_PC_OK:
s_mov_b32 s75, 9449472
buffer_load_dwordx2 v[200:201], v241, s[44:47], s75 offen offset:0 sc1
s_add_u32 s75, s75, 4096
buffer_load_dwordx2 v[216:217], v243, s[44:47], s75 offen offset:0 sc1
s_mov_b32 s75, 11808768
buffer_load_dwordx2 v[202:203], v241, s[44:47], s75 offen offset:0 sc1
s_add_u32 s75, s75, 4096
buffer_load_dwordx2 v[218:219], v243, s[44:47], s75 offen offset:0 sc1
s_mov_b32 s75, 14168064
buffer_load_dwordx2 v[204:205], v241, s[44:47], s75 offen offset:0 sc1
s_add_u32 s75, s75, 4096
buffer_load_dwordx2 v[220:221], v243, s[44:47], s75 offen offset:0 sc1
s_mov_b32 s75, 16527360
buffer_load_dwordx2 v[206:207], v241, s[44:47], s75 offen offset:0 sc1
s_add_u32 s75, s75, 4096
buffer_load_dwordx2 v[222:223], v243, s[44:47], s75 offen offset:0 sc1
s_mov_b32 s75, 12288
buffer_load_dwordx2 v[192:193], v241, s[44:47], s75 offen offset:0 sc1
s_add_u32 s75, s75, 4096
buffer_load_dwordx2 v[208:209], v243, s[44:47], s75 offen offset:0 sc1
s_mov_b32 s75, 2371584
buffer_load_dwordx2 v[194:195], v241, s[44:47], s75 offen offset:0 sc1
s_add_u32 s75, s75, 4096
buffer_load_dwordx2 v[210:211], v243, s[44:47], s75 offen offset:0 sc1
s_mov_b32 s75, 4730880
buffer_load_dwordx2 v[196:197], v241, s[44:47], s75 offen offset:0 sc1
s_add_u32 s75, s75, 4096
buffer_load_dwordx2 v[212:213], v243, s[44:47], s75 offen offset:0 sc1
s_mov_b32 s75, 7090176
buffer_load_dwordx2 v[198:199], v241, s[44:47], s75 offen offset:0 sc1
s_add_u32 s75, s75, 4096
buffer_load_dwordx2 v[214:215], v243, s[44:47], s75 offen offset:0 sc1
s_waitcnt vmcnt(0)
s_branch LATECR
KEND:
)"
    :
    : [ka] "s"(__builtin_amdgcn_kernarg_segment_ptr()), [bid] "s"(blockIdx.x), [wid] "s"(wid), [lds] "s"((unsigned)(size_t)lds)
    : CLOBBERS);
}

extern "C" void kernel_launch(void* const* d_in, const int* in_sizes, int n_in, void* d_out, int out_size, void* d_ws, size_t ws_size,
                              hipStream_t stream) {
  fused_block<<<256, 256, 0, stream>>>((const char*)d_in[0], (const char*)d_in[1], (const char*)d_in[2], (const char*)d_in[3], (const char*)d_in[4],
                                       (const char*)d_in[6], (const char*)d_in[7], (char*)d_out, (char*)d_ws);
}
```
